```python
import torch

def _softmax(x, axis=-1):
    return torch.softmax(x, dim=axis)
def _gelu(x):
    return torch.nn.functional.gelu(x, approximate="tanh")
def _silu(x):
    return torch.nn.functional.silu(x)
def _sigmoid(x):
    return torch.sigmoid(x)
def _layernorm(x, axis=-1, eps=1e-5):
    m = x.mean(dim=axis, keepdim=True)
    v = x.var(dim=axis, keepdim=True, unbiased=False)
    return (x - m) / torch.sqrt(v + eps)
def _scatter_add(tgt, idx, src, axis=0):
    return tgt.clone().index_add_(axis, idx, src)
def _segment_sum(src, seg, n):
    out = torch.zeros((n,) + src.shape[1:], dtype=src.dtype)
    return out.index_add_(0, seg, src)
def _pad(x, pw):
    flat = tuple(v for pair in reversed(pw) for v in pair)
    return torch.nn.functional.pad(x, flat)

kernel_name = 'interleave_layers__sliding_local_dilated__moe_arch_mod__prompt'

def setup_inputs():

    _w = (lambda a: (lambda t: t.to(torch.bfloat16) if t.is_floating_point() else t)(torch.as_tensor(a)))
    def _A_setup():
        B, S, D = 1, 2048, 512
        return {
            "x": _w((lambda *s: torch.randn(*s, dtype=torch.bfloat16) * 0.05)(B, S, D)),
            "wqkv": _w((lambda *s: torch.randn(*s, dtype=torch.bfloat16) * 0.05)(D, 3 * D)),
            "wo": _w((lambda *s: torch.randn(*s, dtype=torch.bfloat16) * 0.05)(D, D)),
        }
    _A_in = _A_setup()

    def _B_setup():
        B, S, D, F, C = 1, 2048, 512, 1376, 2
        return {
            "x": _w((lambda *s: torch.randn(*s, dtype=torch.bfloat16) * 0.05)(B, S, D)),
            "w_r": _w((lambda *s: torch.randn(*s, dtype=torch.bfloat16) * 0.05)(D)),
            "w_gate": _w((lambda *s: torch.randn(*s, dtype=torch.bfloat16) * 0.05)(D, F)),
            "w_up": _w((lambda *s: torch.randn(*s, dtype=torch.bfloat16) * 0.05)(D, F)),
            "w_down": _w((lambda *s: torch.randn(*s, dtype=torch.bfloat16) * 0.05)(F, D)),
        }
    _B_in = _B_setup()

    return {
        "A_x": _A_in["x"],
        "A_wqkv": _A_in["wqkv"],
        "A_wo": _A_in["wo"],
        "B_x": _B_in["x"],
        "B_w_r": _B_in["w_r"],
        "B_w_gate": _B_in["w_gate"],
        "B_w_up": _B_in["w_up"],
        "B_w_down": _B_in["w_down"],
    }

def reference(A_x, A_wqkv, A_wo, B_x, B_w_r, B_w_gate, B_w_up, B_w_down):
    def _A_ref(x, wqkv, wo):
        B, S, D = x.shape
        H = 32; Dh = D // H
        W, dil = 8, 2
        qkv = (lambda x, *d: x.permute(*d))((x @ wqkv).reshape(B, S, 3, H, Dh), 2, 0, 3, 1, 4)
        q, k, v = qkv[0], qkv[1], qkv[2]
        scale = Dh ** -0.5
        pos = torch.arange(S)
        off = torch.arange(W) * dil
        idx = pos[:, None] - off[None, :]
        mask = idx >= 0
        idx_c = torch.where(mask, idx, 0).reshape(-1)
        kg = k[:, :, idx_c, :].reshape(B, H, S, W, Dh)
        vg = v[:, :, idx_c, :].reshape(B, H, S, W, Dh)
        logits = torch.einsum("bhsd,bhswd->bhsw", q, kg) * scale
        logits = torch.where(mask[None, None, :, :], logits, -1e9)
        attn = _softmax(logits, axis=-1)
        o = torch.einsum("bhsw,bhswd->bhsd", attn, vg)
        return (lambda x, *d: x.permute(*d))(o, 0, 2, 1, 3).reshape(B, S, D) @ wo

    def _B_ref(x, w_r, w_gate, w_up, w_down):
        B, S, D = x.shape
        C = 2
        r = x @ w_r
        idx = torch.argsort(-r, axis=-1)[:, :C]
        idx_e = (lambda x, n, axis: torch.repeat_interleave(x, n, dim=axis))(idx.reshape(B, C, 1), D, axis=2)
        x_sel = (lambda x, i, axis: torch.take_along_dim(x, i, dim=axis))(x, idx_e, axis=1)
        g = _silu(x_sel @ w_gate)
        u = x_sel @ w_up
        y = (g * u) @ w_down
        r_sel = _sigmoid((lambda x, i, axis: torch.take_along_dim(x, i, dim=axis))(r, idx, axis=1))
        y = y * r_sel.reshape(B, C, 1)
        b_off = (torch.arange(B) * S).reshape(B, 1)
        flat_idx = (idx + b_off).reshape(B * C)
        zero = (x * 0.0).reshape(B * S, D)
        upd = _scatter_add(zero, flat_idx, y.reshape(B * C, D), axis=0)
        return x + upd.reshape(B, S, D)

    x = A_x
    x = x + _A_ref(_layernorm(x, axis=-1), A_wqkv, A_wo)
    x = x + _B_ref(_layernorm(x, axis=-1), B_w_r, B_w_gate, B_w_up, B_w_down)
    x = x + _A_ref(_layernorm(x, axis=-1), A_wqkv, A_wo)
    x = x + _B_ref(_layernorm(x, axis=-1), B_w_r, B_w_gate, B_w_up, B_w_down)
    return x
```

```cpp
#include <hip/hip_runtime.h>
#include <hip/hip_bf16.h>
#include <stdint.h>
typedef unsigned short u16;
constexpr int S=2048, D=512, H=32, DH=16, F=1376;
__device__ inline float b2f(u16 v){ return __uint_as_float(((unsigned)v)<<16); }
__device__ inline u16 f2b(float f){ unsigned u=__float_as_uint(f); u += 0x7FFF + ((u>>16)&1); return (u16)(u>>16); }
__device__ inline float rb(float f){ return b2f(f2b(f)); }

__global__ void k_ln(const float* x, float* h){
  int r=blockIdx.x, t=threadIdx.x; float v[8]; float s=0;
  for(int i=0;i<8;i++){ v[i]=x[r*D+t+64*i]; s+=v[i]; }
  for(int o=32;o;o>>=1) s+=__shfl_xor(s,o);
  float m=s/D; float q=0;
  for(int i=0;i<8;i++){ float d=v[i]-m; q+=d*d; }
  for(int o=32;o;o>>=1) q+=__shfl_xor(q,o);
  float rs=1.0f/sqrtf(q/D+1e-5f);
  for(int i=0;i<8;i++) h[r*D+t+64*i]=rb((v[i]-m)*rs);
}
__global__ void k_cvt(const u16* a, float* x, int n){ int i=blockIdx.x*256+threadIdx.x; if(i<n) x[i]=b2f(a[i]); }
__global__ void k_gemm(const float* A, const u16* W, float* C, int N, int K){
  int n=blockIdx.x*256+threadIdx.x, m0=blockIdx.y*8; float acc[8]={0};
  for(int k=0;k<K;k++){ float w=b2f(W[k*N+n]); for(int i=0;i<8;i++) acc[i]+=A[(m0+i)*K+k]*w; }
  for(int i=0;i<8;i++) C[(m0+i)*N+n]=rb(acc[i]);
}
__global__ void k_attn(const float* qkv, float* o){
  int id=blockIdx.x*256+threadIdx.x; int s=id/H, h=id%H;
  float l[8]; float mx=-3e38f;
  for(int w=0;w<8;w++){ int j=s-2*w; float a=0; int jc=j<0?0:j;
    for(int d=0;d<DH;d++) a+=qkv[s*1536+h*16+d]*qkv[jc*1536+512+h*16+d];
    a*=0.25f; if(j<0) a=-1e9f; l[w]=a; mx=fmaxf(mx,a);}
  float sum=0; for(int w=0;w<8;w++){ l[w]=__expf(l[w]-mx); sum+=l[w]; }
  for(int d=0;d<DH;d++){ float a=0; for(int w=0;w<8;w++){ int j=s-2*w; int jc=j<0?0:j; a+=l[w]*qkv[jc*1536+1024+h*16+d]; } o[s*D+h*16+d]=rb(a/sum);}
}
__global__ void k_add(float* x, const float* a, int n){ int i=blockIdx.x*256+threadIdx.x; if(i<n) x[i]=rb(x[i]+a[i]); }
__global__ void k_score(const float* h, const u16* wr, float* r){
  int row=blockIdx.x,t=threadIdx.x; float s=0; for(int i=0;i<8;i++) s+=h[row*D+t+64*i]*b2f(wr[t+64*i]);
  for(int o=32;o;o>>=1) s+=__shfl_xor(s,o); if(t==0) r[row]=rb(s);
}
__global__ void k_top2(const float* r, int* idx){
  if(threadIdx.x==0){ int b0=-1,b1=-1; float v0=-3e38f,v1=-3e38f;
    for(int i=0;i<S;i++){ float v=r[i]; if(v>v0){v1=v0;b1=b0;v0=v;b0=i;} else if(v>v1){v1=v;b1=i;} }
    idx[0]=b0; idx[1]=b1; }
}
__global__ void k_gu(const float* h, const int* idx, const u16* wg, const u16* wu, float* act){
  int f=blockIdx.x*64+threadIdx.x; int c=blockIdx.y; if(f>=F) return; const float* xr=h+idx[c]*D; float g=0,u=0;
  for(int k=0;k<D;k++){ g+=xr[k]*b2f(wg[k*F+f]); u+=xr[k]*b2f(wu[k*F+f]); }
  g=rb(g); u=rb(u); g=rb(g/(1.f+__expf(-g))); act[c*F+f]=rb(g*u);
}
__global__ void k_down(const float* act, const int* idx, const float* r, const u16* wd, float* h){
  int n=blockIdx.x*64+threadIdx.x; int c=blockIdx.y; float a=0;
  for(int k=0;k<F;k++) a+=act[c*F+k]*b2f(wd[k*D+n]);
  a=rb(a); float sg=rb(1.f/(1.f+__expf(-r[idx[c]]))); a=rb(a*sg);
  h[idx[c]*D+n]=rb(h[idx[c]*D+n]+a);
}
__global__ void k_out(const float* x, u16* o, int n){ int i=blockIdx.x*256+threadIdx.x; if(i<n) o[i]=f2b(x[i]); }

extern "C" void kernel_launch(void* const* d_in, const int* in_sizes, int n_in, void* d_out, int out_size, void* d_ws, size_t ws_size, hipStream_t st){
  const u16* Ax=(const u16*)d_in[0]; const u16* wqkv=(const u16*)d_in[1]; const u16* wo=(const u16*)d_in[2];
  const u16* wr=(const u16*)d_in[4]; const u16* wg=(const u16*)d_in[5]; const u16* wu=(const u16*)d_in[6]; const u16* wd=(const u16*)d_in[7];
  float* x=(float*)d_ws; float* h=x+S*D; float* qkv=h+S*D; float* o=qkv+S*1536; float* a=o+S*D; float* r=a+S*D; float* act=r+S; int* idx=(int*)(act+2*F);
  int n=S*D;
  k_cvt<<<n/256,256,0,st>>>(Ax,x,n);
  for(int L=0;L<2;L++){
    k_ln<<<S,64,0,st>>>(x,h);
    k_gemm<<<dim3(1536/256,S/8),256,0,st>>>(h,wqkv,qkv,1536,512);
    k_attn<<<S*H/256,256,0,st>>>(qkv,o);
    k_gemm<<<dim3(512/256,S/8),256,0,st>>>(o,wo,a,512,512);
    k_add<<<n/256,256,0,st>>>(x,a,n);
    k_ln<<<S,64,0,st>>>(x,h);
    k_score<<<S,64,0,st>>>(h,wr,r);
    k_top2<<<1,64,0,st>>>(r,idx);
    k_gu<<<dim3((F+63)/64,2),64,0,st>>>(h,idx,wg,wu,act);
    k_down<<<dim3(D/64,2),64,0,st>>>(act,idx,r,wd,h);
    k_add<<<n/256,256,0,st>>>(x,h,n);
  }
  k_out<<<n/256,256,0,st>>>(x,(u16*)d_out,n);
}
```

```cpp
#include <hip/hip_runtime.h>
#include <hip/hip_bf16.h>
#include <stdint.h>

typedef unsigned short u16;
typedef unsigned long long u64;
typedef __bf16 bf2 __attribute__((ext_vector_type(2)));
typedef short s4 __attribute__((ext_vector_type(4)));
typedef float f4 __attribute__((ext_vector_type(4)));
typedef float f2 __attribute__((ext_vector_type(2)));
typedef unsigned u4 __attribute__((ext_vector_type(4)));
typedef unsigned u2 __attribute__((ext_vector_type(2)));
typedef __amdgpu_buffer_rsrc_t rsrc_t;
#define LAS __attribute__((address_space(3)))
#define DEV __device__ __forceinline__

constexpr unsigned TILEB = 81920;
constexpr unsigned BUFB = 30 * TILEB;
constexpr unsigned WS_EPOCH = 0;
constexpr unsigned WS_FF = 4096;
constexpr unsigned WS_SEL = 8192;
constexpr unsigned WS_CAND = 16384;
constexpr unsigned WS_FA = 32768;
constexpr unsigned WS_FX = 65536;
constexpr unsigned WS_YP = 131072;
constexpr unsigned WS_ST = 262144;
constexpr unsigned STB = 30 * 8 * 80 * 16;
constexpr unsigned WS_OB = 1048576;
constexpr unsigned WS_XB = WS_OB + 2 * BUFB;
constexpr unsigned WS_USED = WS_XB + 2 * BUFB;
constexpr unsigned L_RING = 81920;
constexpr unsigned L_RED2 = L_RING + 16384;
constexpr unsigned L_MISC = 155648;
constexpr unsigned O_RED = 0;
constexpr unsigned O_RC = 4096, O_RM = O_RC + 320, O_RA = O_RM + 320, O_RB = O_RA + 320;
constexpr unsigned LDS_BYTES = 163840;
constexpr unsigned SENT = 0xFFFFFFFFu, OOB = 0x80000000u, ONE2 = 0x3F803F80u;
#define FDELAY 200
constexpr int SPIN_CAP = 1 << 15;
constexpr float EPS = 1e-5f;

#define CFENCE() asm volatile("" ::: "memory")
#define WAITC(vm, lg) do { CFENCE(); __builtin_amdgcn_s_waitcnt(((vm) & 15) | 0x70 | ((lg) << 8) | (((vm) >> 4) << 14)); CFENCE(); } while (0)
#define WAITV(n) asm volatile("s_waitcnt vmcnt(%0)" ::"i"(n) : "memory")
#define WAITL() WAITC(63, 0)
#define BAR() do { CFENCE(); __builtin_amdgcn_s_barrier(); CFENCE(); } while (0)
#define SB() __builtin_amdgcn_sched_barrier(0)
#define OPQ(x) asm volatile("" : "+v"(x))
#define KEEP(x) asm volatile("" ::"v"(x))

struct Params {
  const u16* x; const u16* wqkv; const u16* wo; const u16* wr; const u16* wg; const u16* wu; const u16* wd;
  u16* out; char* ws;
};

DEV rsrc_t mk(const void* p, unsigned bytes) { return __builtin_amdgcn_make_buffer_rsrc((void*)p, 0, bytes, 0x00020000); }
DEV void setm0(unsigned base, int imm) { asm volatile("s_add_u32 m0, %0, %1" ::"s"(base), "i"(imm) : "scc", "memory"); }
template <int SC> DEV void dma_go(rsrc_t rs, unsigned voff, unsigned soff) {
  if (SC) asm volatile("buffer_load_dwordx4 %0, %1, %2 offen sc1 lds" ::"v"(voff), "s"(rs), "s"(soff) : "memory");
  else asm volatile("buffer_load_dwordx4 %0, %1, %2 offen lds" ::"v"(voff), "s"(rs), "s"(soff) : "memory");
}
DEV void dma_go_m(rsrc_t rs, unsigned voff, unsigned soff, u64 mask) {
  asm volatile("s_mov_b64 exec, %3\n\tbuffer_load_dwordx4 %0, %1, %2 offen lds\n\ts_mov_b64 exec, -1" ::"v"(voff), "s"(rs), "s"(soff), "s"(mask) : "memory");
}
template <int SC> DEV void dma(rsrc_t rs, unsigned voff, unsigned soff, unsigned base, int imm) {
  if (SC) asm volatile("s_add_u32 m0, %3, %4\n\ts_nop 0\n\tbuffer_load_dwordx4 %0, %1, %2 offen sc1 lds" ::"v"(voff), "s"(rs), "s"(soff), "s"(base), "i"(imm) : "scc", "memory");
  else asm volatile("s_add_u32 m0, %3, %4\n\ts_nop 0\n\tbuffer_load_dwordx4 %0, %1, %2 offen lds" ::"v"(voff), "s"(rs), "s"(soff), "s"(base), "i"(imm) : "scc", "memory");
}
DEV void st16(rsrc_t rs, u4 v, unsigned voff, unsigned soff) {
  asm volatile("buffer_store_dwordx4 %0, %1, %2, %3 offen sc1\n\ts_nop 1" ::"v"(v), "v"(voff), "s"(rs), "s"(soff) : "memory");
}
DEV void st4(rsrc_t rs, unsigned v, unsigned voff, unsigned soff) {
  asm volatile("buffer_store_dword %0, %1, %2, %3 offen sc1" ::"v"(v), "v"(voff), "s"(rs), "s"(soff) : "memory");
}
DEV u4 ld16(rsrc_t rs, unsigned voff, unsigned soff) { return __builtin_amdgcn_raw_buffer_load_b128(rs, voff, soff, 16); }
DEV unsigned ld4(rsrc_t rs, unsigned voff, unsigned soff) { return __builtin_amdgcn_raw_buffer_load_b32(rs, voff, soff, 16); }

template <bool Z = false> DEV void mWA(f4& c, u4 w, u4 a) {
  if (Z) asm volatile("v_mfma_f32_16x16x32_bf16 %0, %1, %2, 0" : "=v"(c) : "a"(w), "v"(a));
  else asm volatile("v_mfma_f32_16x16x32_bf16 %0, %1, %2, %0" : "+v"(c) : "a"(w), "v"(a));
}
template <bool Z = false> DEV void mAW(f4& c, u4 a, u4 w) {
  if (Z) asm volatile("v_mfma_f32_16x16x32_bf16 %0, %1, %2, 0" : "=v"(c) : "v"(a), "a"(w));
  else asm volatile("v_mfma_f32_16x16x32_bf16 %0, %1, %2, %0" : "+v"(c) : "v"(a), "a"(w));
}
template <bool Z = false> DEV void mVV(f4& c, u4 a, u4 b) {
  if (Z) asm volatile("v_mfma_f32_16x16x32_bf16 %0, %1, %2, 0" : "=v"(c) : "v"(a), "v"(b));
  else asm volatile("v_mfma_f32_16x16x32_bf16 %0, %1, %2, %0" : "+v"(c) : "v"(a), "v"(b));
}
template <bool Z = false> DEV void m16(f4& c, u2 a, u2 b) {
  if (Z) asm volatile("v_mfma_f32_16x16x16_bf16 %0, %1, %2, 0" : "=v"(c) : "v"(a), "v"(b));
  else asm volatile("v_mfma_f32_16x16x16_bf16 %0, %1, %2, %0" : "+v"(c) : "v"(a), "v"(b));
}
DEV f4 m16c(u2 a, u2 b, f4 c) { f4 d; asm volatile("v_mfma_f32_16x16x16_bf16 %0, %1, %2, %3" : "=&v"(d) : "v"(a), "v"(b), "v"(c)); return d; }
template <bool Z = false> DEV void mf32(f4& c, float a, float b) {
  if (Z) asm volatile("v_mfma_f32_16x16x4_f32 %0, %1, %2, 0" : "=&v"(c) : "v"(a), "v"(b));
  else asm volatile("v_mfma_f32_16x16x4_f32 %0, %1, %2, %0" : "+v"(c) : "v"(a), "v"(b));
}
#define NOPS(n) asm volatile("s_nop %0" ::"i"((n) - 1))
DEV unsigned pk(float a, float b) { f2 v = {a, b}; return __builtin_bit_cast(unsigned, __builtin_convertvector(v, bf2)); }
DEV u2 pk4(f4 v) { return (u2){pk(v[0], v[1]), pk(v[2], v[3])}; }
DEV float lo(unsigned u) { return __uint_as_float(u << 16); }
DEV float hi(unsigned u) { return __uint_as_float(u & 0xFFFF0000u); }
DEV f4 up4(u2 u) { return (f4){lo(u[0]), hi(u[0]), lo(u[1]), hi(u[1])}; }
DEV f4 asf(u4 v) { return __builtin_bit_cast(f4, v); }
DEV u4 asu(f4 v) { return __builtin_bit_cast(u4, v); }
DEV unsigned bsel(unsigned m, unsigned a, unsigned b) { return __builtin_amdgcn_bitop3_b32(m, a, b, 0xCA); }
DEV float bself(unsigned m, float a, float b) { return __uint_as_float(bsel(m, __float_as_uint(a), __float_as_uint(b))); }
DEV f4 bsel4(unsigned m, f4 a, f4 b) { return (f4){bself(m, a[0], b[0]), bself(m, a[1], b[1]), bself(m, a[2], b[2]), bself(m, a[3], b[3])}; }
DEV unsigned lmask(bool c) { unsigned m = c ? ~0u : 0u; OPQ(m); return m; }
DEV float fmx(float a, float b) { return __builtin_elementwise_maximum(a, b); }
template <class T> DEV T lds_r(unsigned a) { return *(const LAS T*)(size_t)a; }
template <class T> DEV void lds_w(unsigned a, T v) { *(LAS T*)(size_t)a = v; }
DEV u4 trfrag2(unsigned a, unsigned step) {
  u2 x = __builtin_bit_cast(u2, __builtin_amdgcn_ds_read_tr16_b64_v4i16((LAS s4*)(size_t)a));
  u2 y = __builtin_bit_cast(u2, __builtin_amdgcn_ds_read_tr16_b64_v4i16((LAS s4*)(size_t)(a + step)));
  return (u4){x[0], x[1], y[0], y[1]};
}
DEV f2 swap16(float a, float b) { auto r = __builtin_amdgcn_permlane16_swap(__float_as_uint(a), __float_as_uint(b), false, false); return (f2){__uint_as_float(r[0]), __uint_as_float(r[1])}; }
DEV f2 swap32(float a, float b) { auto r = __builtin_amdgcn_permlane32_swap(__float_as_uint(a), __float_as_uint(b), false, false); return (f2){__uint_as_float(r[0]), __uint_as_float(r[1])}; }
DEV float xadd16(float v) { f2 r = swap16(v, v); return r[0] + r[1]; }
DEV float xadd32(float v) { f2 r = swap32(v, v); return r[0] + r[1]; }
template <int C> DEV unsigned dpp(unsigned v) { return __builtin_amdgcn_update_dpp(0u, v, C, 0xF, 0xF, true); }
template <int C> DEV float dppf(float v) { return __uint_as_float(dpp<C>(__float_as_uint(v))); }
DEV float wave_sum(float v) {
  v += dppf<0xB1>(v); v += dppf<0x4E>(v); v += dppf<0x141>(v); v += dppf<0x140>(v);
  return xadd32(xadd16(v));
}
DEV unsigned max_row16(unsigned v) { v = max(v, dpp<0xB1>(v)); v = max(v, dpp<0x4E>(v)); v = max(v, dpp<0x141>(v)); return max(v, dpp<0x140>(v)); }
DEV unsigned max_x16(unsigned v) { auto r = __builtin_amdgcn_permlane16_swap(v, v, false, false); return max(r[0], r[1]); }
DEV unsigned max_x32(unsigned v) { auto r = __builtin_amdgcn_permlane32_swap(v, v, false, false); return max(r[0], r[1]); }
DEV unsigned umin(unsigned a, unsigned b) { return a < b ? a : b; }
DEV unsigned rfl(unsigned v) { return __builtin_amdgcn_readfirstlane(v); }
DEV unsigned rdl(unsigned v, int l) { return __builtin_amdgcn_readlane(v, l); }
DEV float rsq(float v) { return __builtin_amdgcn_rsqf(v); }
DEV float sigm(float v) { return __builtin_amdgcn_rcpf(1.f + __builtin_amdgcn_exp2f(-1.44269504f * v)); }
DEV float key_score(unsigned k) { unsigned u = k & 0xFFFFF800u; return __uint_as_float((u & 0x80000000u) ? (u ^ 0x80000000u) : ~u); }

#define STAMP(i)
#define STAMP_INIT()
#define STAMP_FLUSH()
DEV unsigned tag_issue(const char* ws) { unsigned e; asm volatile("s_load_dword %0, %1, 0x0" : "=s"(e) : "s"(ws) : "memory"); return e; }
DEV unsigned tag_finish(unsigned e) { unsigned t; asm volatile("s_waitcnt lgkmcnt(0)\n\ts_add_u32 %0, %1, 1" : "=s"(t) : "s"(e) : "memory", "scc"); return t; }

DEV void wait_flags(rsrc_t ws, unsigned voff, unsigned base, unsigned tag) {
  for (int sp = 0; sp < SPIN_CAP; ++sp) {
    CFENCE();
    if (__all(ld4(ws, voff, base) == tag)) break;
    __builtin_amdgcn_s_sleep(1);
  }
}
DEV u4 wait_rec(rsrc_t ws, unsigned voff, unsigned base, unsigned tag) {
  u4 c;
  for (int sp = 0; sp < SPIN_CAP; ++sp) {
    CFENCE();
    c = ld16(ws, voff, base);
    if (__all(c[3] == tag)) break;
    __builtin_amdgcn_s_sleep(1);
  }
  return c;
}

struct RowC { float c[5], cm[5]; f4 c4[5], cm4[5]; };
DEV void rowc_read(RowC& R, unsigned r1, unsigned r4, int i) {
  const int t = i / 3;
  if (i % 3 == 0) { R.c[t] = lds_r<float>(r1 + O_RC + t * 64); R.cm[t] = lds_r<float>(r1 + O_RM + t * 64); }
  else if (i % 3 == 1) R.c4[t] = lds_r<f4>(r4 + O_RC + t * 64);
  else R.cm4[t] = lds_r<f4>(r4 + O_RM + t * 64);
}

DEV void role_a(const Params& P, const int b, const int wv, const int l) {
  const int xcd = b & 7, r = b >> 3;
  const int q = (xcd >> 1) * 30 + r, tile = q >> 2, hg = (xcd & 1) * 4 + (q & 3);
  const int lr = l & 15, g = l >> 4;
  const rsrc_t ws = mk(P.ws, WS_USED);
  const unsigned epoch = tag_issue(P.ws);
  STAMP_INIT();
  STAMP(0);
  u4 W[16][3];
  f4 aq[5], ak[5], av[5];
  u2 xp[5];
  RowC R;
  unsigned tag, pm;
  f4 mask;
  u2 wr2;
  const unsigned trb = L_RING + (8 * g + (lr >> 2)) * 128 + (((2 * wv + ((l & 3) >> 1)) ^ ((((lr >> 3) & 1) | ((g & 1) << 1)) << 1)) << 4) + 8 * (l & 1);
  {
    const int par = tile >= 15, p0 = 68 * (tile - 15 * par);
    const rsrc_t rx = mk(P.x, 2048 * 1024), rw = mk(P.wqkv, 512 * 3072), rwo = mk(P.wo, 512 * 1024);
    unsigned xoff[5];
    {
      const int rr = 4 * wv + g;
#pragma unroll
      for (int j = 0; j < 5; ++j) { int p = min(p0 + 15 * j + rr, 1023); xoff[j] = (2 * p + par) * 1024 + ((lr ^ rr) << 4); }
    }
    const u64 xmask = wv == 3 ? 0x0000FFFFFFFFFFFFull : ~0ull;
    const int k8 = l >> 3;
    const unsigned swA = (((l & 7) ^ (((k8 >> 1) & 1) << 1)) << 4) ^ ((wv & 1) << 6), swB = swA ^ 64;
    const unsigned woffA = k8 * 3072 + hg * 128 + swA, woffB = k8 * 3072 + hg * 128 + swB;
    const unsigned lx = wv * 1024, lw = L_RING + wv * 3072;
    unsigned wso[3];
#pragma unroll
    for (int j = 0; j < 3; ++j) { const int i = wv * 3 + j; wso[j] = (i & 3) * 8 * 3072 + (i >> 2) * 1024; }
    auto ldx = [&](int pr) {
#pragma unroll
      for (int j = 0; j < 5; ++j) { setm0(lx, pr * 20480 + j * 4096); dma_go_m(rx, xoff[j], pr * 256, xmask); }
    };
    auto ldw = [&](int s) {
#pragma unroll
      for (int j = 0; j < 3; ++j) dma<0>(rw, (j & 1) ? woffB : woffA, wso[j] + s * 98304, lw, (s % 6) * 12288 + j * 1024);
    };
    asm volatile("global_load_dwordx2 %0, %1, %2" : "=v"(wr2) : "v"((16 * wv + 4 * g) * 2), "s"(P.wr + 64 * hg) : "memory");
    ldx(0); ldw(0); ldw(1);
    unsigned touch;
    {
      int rh = (3 - (int)(((size_t)P.wqkv) >> 7)) & 3, idx = min(r * 52 + wv * 13 + min(l, 12), 1535);
      unsigned off = (idx / 3) * 3072 + (idx % 3) * 1024 + ((xcd & 1) * 4 + rh) * 128;
      asm volatile("buffer_load_dword %0, %1, %2, 0 offen" : "=v"(touch) : "v"(off), "s"(rw) : "memory");
    }
    ldw(2); ldx(1); ldw(3); ldw(4);
    tag = tag_finish(epoch);
    {
      const u4 one = {ONE2, ONE2, ONE2, ONE2};
      const int row = wv * 4 + g;
      lds_w<u4>((row / 5) * 20480 + ((row % 5) * 16 + 15) * 256 + lr * 16, one);
      if (wv == 0) lds_w<u4>(3 * 20480 + ((g + 1) * 16 + 15) * 256 + lr * 16, one);
    }
    const unsigned ooff = tile * TILEB + (((g & 1) * 32 + 4 * hg + wv) * 2 + (g >> 1)) * 256 + lr * 16;
    const unsigned ooff4 = (g & 1) ? OOB : ooff;
    u4 sent = {SENT, SENT, SENT, SENT};
    OPQ(sent);
    const unsigned wooffA = k8 * 1024 + hg * 128 + swA, wooffB = k8 * 1024 + hg * 128 + swB;
    unsigned xa[4];
#pragma unroll
    for (int i = 0; i < 4; ++i) xa[i] = lr * 256 + (((4 * i + g) ^ lr) << 4);
    const unsigned xg = lr * 256 + (((4 * wv + g) ^ lr) << 4);
    const unsigned xr = (hg >> 1) * 20480 + lr * 256 + ((((hg & 1) * 8 + 2 * wv + (g >> 1)) ^ lr) << 4) + 8 * (g & 1);
    unsigned mq = L_MISC + O_RED + (g == (lr >> 2) ? (wv * 80 + lr) * 8 + 4 : 2560 + l * 4), ms = L_MISC + O_RED + (g == 3 ? (wv * 80 + lr) * 8 : 2560 + l * 4);
    unsigned rb_ = L_MISC + O_RED + (wv * 20 + min(l, 19)) * 8, rc_ = L_MISC + (wv * 20 + min(l, 19)) * 4;
    unsigned r1 = L_MISC + lr * 4, r4 = L_MISC + g * 16;
    OPQ(mq); OPQ(ms); OPQ(rb_); OPQ(rc_); OPQ(r1); OPQ(r4);
    const unsigned m1 = lmask(l & 1), m2 = lmask(l & 2);
    pm = lmask(lr < 8 && g >= 2);
#pragma unroll
    for (int i = 0; i < 4; ++i) { const int rk = 4 * g + i, d = (lr < 8 && g >= 2) ? lr + 15 - rk : lr - rk; mask[i] = (d >= 0 && d <= 7 && rk != 15) ? 0.f : -1e30f; }
    OPQ(mask);
    f4 gr[5];
#pragma unroll
    for (int t = 0; t < 5; ++t) { aq[t] = ak[t] = av[t] = gr[t] = (f4){0, 0, 0, 0}; OPQ(aq[t]); OPQ(ak[t]); OPQ(av[t]); OPQ(gr[t]); }
    u4 a[2][5], z[2][5];
    f2 rv[4];
#pragma unroll
    for (int s = 0; s <= 16; ++s) {
      constexpr int NV[17] = {18, 20, 20, 16, 17, 17, 17, 17, 17, 15, 14, 16, 15, 15, 15, 15, 15};
      switch (s) {
#define C_(n) case n: WAITC(NV[n], 0); break;
        C_(0) C_(1) C_(2) C_(3) C_(4) C_(5) C_(6) C_(7) C_(8) C_(9) C_(10) C_(11) C_(12) C_(13) C_(14) C_(15) C_(16)
#undef C_
      }
      BAR();
      if (s == 0) STAMP(1);
      if (s == 4) STAMP(19);
      if (s == 8) STAMP(20);
      if (s == 12) STAMP(21);
      if (s == 16) STAMP(22);
      SB();
      auto gtile = [](int s_, int i) {
        if (s_ < 0) return -1;
        if (s_ < 5) return i == 0 ? s_ : -1;
        if (s_ < 7) return i < 2 ? 8 + 2 * (s_ - 5) + i : -1;
        if (s_ == 7) return i == 0 ? 12 : -1;
        if (s_ == 8) return i < 2 ? 16 + i : -1;
        if (s_ < 12) return i == 0 ? 16 + s_ - 7 : -1;
        if (s_ == 12) return 24 + i;
        return -1;
      };
      const int nx = s < 8 ? ((s & 3) == 0 ? 2 : 1) : 0, nd = s < 16 ? nx + 3 : 0;
#pragma unroll
      for (int i = 0; i < 20; ++i) {
        if (s > 0) {
          const int pb = (s - 1) & 1;
          if (i < 15) {
            const int t = i / 3;
            if (i % 3 == 0) mWA(aq[t], W[s - 1][0], a[pb][t]); else if (i % 3 == 1) mWA(ak[t], W[s - 1][1], a[pb][t]); else mAW(av[t], a[pb][t], W[s - 1][2]);
          } else if (gtile(s - 1, i - 15) >= 0) mVV(gr[gtile(s - 1, i - 15) & 7], z[pb][i - 15], z[pb][i - 15]);
        }
        if (s < 16) {
          if (i < 3) W[s][i] = trfrag2(trb + (s % 6) * 12288 + i * 4096, 512);
          else if (i < 8) a[s & 1][i - 3] = lds_r<u4>(xa[s & 3] + (s >> 2) * 20480 + (i - 3) * 4096);
          else if (i < 13 && gtile(s, i - 8) >= 0) z[s & 1][i - 8] = lds_r<u4>(xg + (gtile(s, i - 8) >> 3) * 20480 + (gtile(s, i - 8) & 7) * 4096);
          if (i >= 3 && (i - 3) / 2 < nd) {
            const int k = (i - 3) / 2, ph = (i - 3) & 1;
            if (k < nx) {
              const int pr = 2 + (s >> 2), j = (s & 3) == 0 ? k : (s & 3) + 1;
              if (ph == 0) setm0(lx, pr * 20480 + j * 4096); else dma_go_m(rx, xoff[j], pr * 256, xmask);
            } else {
              const int j = k - nx;
              if (ph == 0) setm0(lw, ((s + 5) % 6) * 12288 + j * 1024);
              else if (s < 11) dma_go<0>(rw, (j & 1) ? woffB : woffA, wso[j] + (s + 5) * 98304);
              else dma_go<0>(rwo, (j & 1) ? wooffB : wooffA, (((3 * (s - 11) + ((wv * 3 + j) >> 2) + 2 * hg) & 15) * 32 + ((wv * 3 + j) & 3) * 8) * 1024);
            }
          }
          if (s == 10 && i >= 10 && i < 13) st16(ws, sent, i < 12 ? ooff : ooff4, WS_OB + (i - 10) * 32768);
          if (s == 13 && i >= 10 && i < 15) xp[i - 10] = lds_r<u2>(xr + (i - 10) * 4096);
          if (s == 14 && i >= 8 && i < 13) {
            const f4 v = gr[i - 8];
            lds_w<float>(mq + (i - 8) * 128, bself(m2, bself(m1, v[3], v[2]), bself(m1, v[1], v[0])));
            lds_w<float>(ms + (i - 8) * 128, v[3]);
          }
          if (s == 15 && i == 8) { rv[0] = lds_r<f2>(rb_); rv[1] = lds_r<f2>(rb_ + 640); rv[2] = lds_r<f2>(rb_ + 1280); rv[3] = lds_r<f2>(rb_ + 1920); }
          if (s == 15 && i == 12) {
            const f2 v = (rv[0] + rv[1]) + (rv[2] + rv[3]);
            const float m = v[0] * (1.f / 512), rs = rsq(fmx(v[1] * (1.f / 512) - m * m, 0.f) + EPS);
            lds_w<float>(rc_ + O_RC, rs); lds_w<float>(rc_ + O_RM, rs * m); lds_w<float>(rc_ + O_RA, 1.f); lds_w<float>(rc_ + O_RB, 0.f);
          }
        } else {
          if (i < 15) rowc_read(R, r1, r4, i);
          if (i == 0 && wv == 0) st4(ws, tag, l == 0 ? 0 : OOB, WS_FA + (tile * 8 + hg) * 128);
          if (i == 2) dma<0>(rwo, wooffA, (((15 + 2 * hg) & 15) * 32 + wv * 8) * 1024, lx, L_RING + 3 * 12288);
          if (i >= 4 && i < 7) st16(ws, sent, i < 6 ? ooff : ooff4, WS_OB + BUFB + (i - 4) * 32768);
        }
        SB();
      }
    }
    asm volatile("" ::"v"(touch));
    OPQ(wr2);
  }
  NOPS(2);
  SB();
  f4 csq, csk;
#pragma unroll
  for (int i = 0; i < 4; ++i) { csq[i] = dppf<0x15F>(aq[0][i]); csk[i] = dppf<0x15F>(ak[0][i]); }
  float csv;
  { f2 x = swap32(av[0][3], av[0][3]); f2 y = swap16(x[1], x[1]); csv = y[1]; }
  STAMP(2);
  u4 WO[16];

#pragma unroll 1
  for (int L = 0; L < 2; ++L) {
#define FRESH(n) int l##n = l, wv##n = wv, hg##n = hg, tile##n = tile; asm volatile("" : "+v"(l##n)); asm volatile("" : "+s"(wv##n), "+s"(hg##n), "+s"(tile##n)); \
    const int lr##n = l##n & 15, g##n = l##n >> 4; (void)lr##n; (void)g##n;
    FRESH(1)
    const unsigned ob = WS_OB + L * BUFB + tile1 * TILEB;
    unsigned fa;
    {
      u2 kb[5], vb[5], qb[5], o[5];
      f4 ss[5], sp[5], sc[5];
      float mx[5];
      auto scale = [&](int t) {
        qb[t] = pk4(aq[t] * R.c[t] - csq * R.cm[t]);
        kb[t] = pk4(ak[t] * R.c[t] - csk * R.cm[t]);
        vb[t] = pk4(av[t] * R.c4[t] - R.cm4[t] * csv);
      };
      auto smm = [&](int t) { ss[t] = m16c(kb[t], qb[t], mask); if (t) sp[t] = m16c(kb[t - 1], qb[t], mask); };
      auto pick = [&](int t) {
        sc[t] = bsel4(pm, t ? sp[t] : (f4){-1e30f, -1e30f, -1e30f, -1e30f}, ss[t]);
        mx[t] = fmx(fmx(fmx(sc[t][0], sc[t][1]), sc[t][2]), sc[t][3]);
      };
      scale(0); SB();
      scale(1); SB(); smm(0); SB();
      scale(2); SB(); smm(1); SB();
      scale(3); SB(); smm(2); SB();
      scale(4); SB(); smm(3); smm(4); SB();
      pick(0); pick(1); pick(2); SB();
      pick(3); pick(4);
      {
        f2 x = swap16(mx[0], mx[1]), y = swap16(mx[2], mx[3]), w = swap16(mx[4], mx[4]);
        f2 u = swap32(fmx(x[0], x[1]), fmx(y[0], y[1]));
        const float w1 = fmx(w[0], w[1]), c = fmx(u[0], u[1]) * 0.36067376f;
        f2 w2 = swap32(w1, w1), v = swap32(c, c);
        f2 v0 = swap16(v[0], v[0]), v1 = swap16(v[1], v[1]);
        mx[0] = v0[0]; mx[1] = v0[1]; mx[2] = v1[0]; mx[3] = v1[1]; mx[4] = fmx(w2[0], w2[1]) * 0.36067376f;
      }
      asm volatile("buffer_load_dword %0, %1, %2, %3 offen sc1" : "=v"(fa) : "v"((l1 & 7) * 128), "s"(ws), "s"(WS_FA + tile1 * 1024) : "memory");
      u2 one2 = {ONE2, ONE2};
      float kap = 0.36067376f;
      OPQ(one2); OPQ(kap);
      u2 pb[5], ps[5], pp[5];
      f4 sum[5], oa[5];
      auto expo = [&](int t) {
        const f4 d = sc[t] * kap - mx[t];
        f4 e;
#pragma unroll
        for (int i = 0; i < 4; ++i) e[i] = __builtin_amdgcn_exp2f(d[i]);
        pb[t] = pk4(e);
        if (t) { pp[t] = pb[t] & pm; ps[t] = pb[t] ^ pp[t]; } else ps[t] = pb[t];
      };
      auto pv = [&](int t) { m16<true>(oa[t], vb[t], ps[t]); if (t) m16(oa[t], vb[t - 1], pp[t]); m16<true>(sum[t], one2, pb[t]); };
      auto norm = [&](int t) { KEEP(sum[t]); o[t] = pk4(oa[t] * __builtin_amdgcn_rcpf(sum[t][0])); };
      const unsigned go = (((g1 & 1) * 32 + 4 * hg1 + wv1) * 2 + (g1 >> 1)) * 256 + lr1 * 16;
      const unsigned lo_ = (((g1 & 1) * 32 + wv1) * 2 + (g1 >> 1)) * 256 + lr1 * 16;
      auto put = [&](int i) {
        u2 A = o[2 * i], B = o[i < 2 ? 2 * i + 1 : 4];
        auto s0 = __builtin_amdgcn_permlane16_swap(A[0], B[0], false, false); auto s1 = __builtin_amdgcn_permlane16_swap(A[1], B[1], false, false);
        const u4 e = {umin(s0[0], SENT - 1), s1[0], s0[1], s1[1]};
        st16(ws, e, (i == 2 && (g1 & 1)) ? OOB : go, ob + i * 32768);
        lds_w<u4>(i < 2 ? lo_ + i * 32768 : ((g1 & 1) ? lo_ + 2048 : lo_ + 65536), e);
      };
      SB();
      expo(0); SB();
      expo(1); SB(); pv(0); SB();
      expo(2); SB(); pv(1); SB();
      expo(3); SB(); pv(2); SB(); norm(0); SB();
      expo(4); SB(); pv(3); SB(); norm(1); SB(); pv(4); SB();
      put(0); norm(2); SB(); norm(3); SB();
      put(1); norm(4); SB();
      put(2);
    }
    STAMP(3 + 8 * L);
    FRESH(2)
    const unsigned l16 = l2 * 16;
    unsigned l16h = l16 + 65536, r1 = L_MISC + lr2 * 4;
    OPQ(l16h); OPQ(r1);
    f4 acc[5];
#pragma unroll
    for (int t = 0; t < 5; ++t) acc[t] = up4(xp[t]) * lds_r<float>(r1 + O_RA + t * 64) + lds_r<float>(r1 + O_RB + t * 64);
    WAITC(3, 0);
    OPQ(fa);
    BAR();
    if (L == 0) {
      unsigned tb = trb;
      OPQ(tb);
#pragma unroll
      for (int j = 0; j < 16; ++j) { WO[j] = trfrag2(tb + ((4 + j / 3) % 6) * 12288 + (j % 3) * 4096, 512); asm volatile("" : "+a"(WO[j])); }
      if (!__all(fa == tag)) wait_flags(ws, (l2 & 7) * 128, WS_FA + tile2 * 1024, tag);
    }
    u4 a[3][5];
    auto rda = [&](int j, int t) { a[j % 3][t] = lds_r<u4>((t < 4 ? l16 + t * 16384 : l16h) + j * 1024); };
#pragma unroll
    for (int t = 0; t < 5; ++t) { rda(0, t); rda(1, t); }
    const unsigned lw2 = wv2 * 1024;
    SB();
#pragma unroll
    for (int i = 0; i < 3; ++i) {
      const unsigned so = ob + ((2 * hg2 + 2 + 4 * i + wv2) & 15) * 1024;
#pragma unroll
      for (int t = 0; t < 5; ++t) {
        if (i == 0) { setm0(lw2, 2048 + t * 16384); SB(); mWA(acc[t], WO[0], a[0][t]); SB(); dma_go<1>(ws, l16, so + t * 16384); }
        else dma<1>(ws, l16, so + t * 16384, lw2, (2 + 4 * i) * 1024 + t * 16384);
      }
    }
    {
      const unsigned so = ob + ((2 * hg2 + 14 + (wv2 & 1)) & 15) * 1024;
#pragma unroll
      for (int i = 0; i < 3; ++i) { const unsigned t = min((wv2 >> 1) * 3 + i, 4); dma<1>(ws, l16, so + t * 16384, (wv2 & 1) * 1024 + t * 16384, 14 * 1024); }
    }
    STAMP(4 + 8 * L);
    auto check = [&](int j) {
      u4 c0 = a[j % 3][0], c1 = a[j % 3][1], c2 = a[j % 3][2], c3 = a[j % 3][3], c4 = a[j % 3][4];
      unsigned m = max(max(max(c0[0], c1[0]), max(c2[0], c3[0])), c4[0]);
      if (__builtin_expect(__any(m == SENT), 0)) {
        const unsigned so = ob + ((2 * hg2 + j) & 15) * 1024;
        for (int sp = 0; sp < SPIN_CAP; ++sp) {
          CFENCE();
          c0 = ld16(ws, l16, so); c1 = ld16(ws, l16, so + 16384); c2 = ld16(ws, l16, so + 32768); c3 = ld16(ws, l16, so + 49152); c4 = ld16(ws, l16, so + 65536);
          m = max(max(max(c0[0], c1[0]), max(c2[0], c3[0])), c4[0]);
          if (!__any(m == SENT)) break;
          __builtin_amdgcn_s_sleep(1);
        }
        a[j % 3][0] = c0; a[j % 3][1] = c1; a[j % 3][2] = c2; a[j % 3][3] = c3; a[j % 3][4] = c4;
        NOPS(2);
      }
    };
    WAITV(13);
    BAR();
#pragma unroll
    for (int t = 0; t < 5; ++t) rda(2, t);
    SB();
#pragma unroll
    for (int j = 1; j < 16; ++j) {
      if (j == 4) WAITV(8); else if (j == 8) WAITV(3); else if (j == 12) WAITV(0);
      if (j == 4 || j == 8 || j == 12) BAR();
      SB();
#pragma unroll
      for (int t = 0; t < 5; ++t) { mWA(acc[t], WO[j], a[j % 3][t]); if (j + 2 < 16) rda(j + 2, t); SB(); }
      if (j + 1 < 16) check(j + 1);
      SB();
    }
    STAMP(5 + 8 * L);
    FRESH(3)
    const unsigned xb = WS_XB + L * BUFB + tile3 * TILEB;
    const int row3 = wv3 < 2 ? wv3 * 24 + min(l3, 23) : 16 + wv3 * 16 + min(l3, 15);
    const bool rowok = l3 < (wv3 < 2 ? 24 : 16);
    unsigned rw_ = L_RED2 + wv3 * 2048 + l3 * 16, rr_ = L_RED2 + row3 * 16, rc_ = L_MISC + row3 * 4;
    OPQ(rw_); OPQ(rr_); OPQ(rc_);
    {
      u2 spa[4]; float spf[4];
#pragma unroll
      for (int t = 0; t < 4; ++t) { spa[t] = lr3 == 4 * t ? (u2){ONE2, ONE2} : lr3 == 4 * t + 1 ? wr2 : (u2){0, 0}; spf[t] = lr3 == 4 * t + 2 ? 1.f : 0.f; }
      SB();
      float q2[5];
      f4 da, db, ea, eb;
#pragma unroll
      for (int t = 0; t < 5; ++t) {
        xp[t] = pk4(acc[t]);
        const f2 h = (f2){acc[t][0], acc[t][1]} * (f2){acc[t][0], acc[t][1]} + (f2){acc[t][2], acc[t][3]} * (f2){acc[t][2], acc[t][3]};
        q2[t] = h[0] + h[1];
        SB();
        if (t == 1) { m16<true>(da, spa[0], xp[0]); mf32<true>(db, spf[0], q2[0]); }
        else if (t > 1) { m16(da, spa[t - 1], xp[t - 1]); mf32(db, spf[t - 1], q2[t - 1]); }
        SB();
      }
      m16<true>(ea, spa[0], xp[4]); mf32<true>(eb, spf[0], q2[4]);
      SB();
      NOPS(8);
      SB();
      KEEP(da); KEEP(db);
      lds_w<f4>(rw_, (f4){da[0], da[1], db[2], 0.f});
      SB();
      KEEP(ea); KEEP(eb);
      lds_w<f4>(rw_ + 1024, (f4){ea[0], ea[1], eb[2], 0.f});
    }
    WAITL();
    BAR();
    {
      u4 w = asu((lds_r<f4>(rr_) + lds_r<f4>(rr_ + 2048)) + (lds_r<f4>(rr_ + 4096) + lds_r<f4>(rr_ + 6144)));
      w[3] = tag;
      st16(ws, w, rowok ? row3 * 16 : OOB, WS_ST + L * STB + (tile3 * 8 + hg3) * 1280);
      STAMP(6 + 8 * L);
      const unsigned go = (((g3 & 1) * 32 + 4 * hg3 + wv3) * 2 + (g3 >> 1)) * 256 + lr3 * 16;
#pragma unroll
      for (int i = 0; i < 3; ++i) {
        u2 A = xp[2 * i], B = xp[i < 2 ? 2 * i + 1 : 4];
        auto s0 = __builtin_amdgcn_permlane16_swap(A[0], B[0], false, false); auto s1 = __builtin_amdgcn_permlane16_swap(A[1], B[1], false, false);
        st16(ws, (u4){s0[0], s1[0], s0[1], s1[1]}, (i == 2 && (g3 & 1)) ? OOB : go, xb + i * 32768);
      }
    }
    WAITV(0);
    BAR();
    if (wv3 == 0) st4(ws, tag, l3 == 0 ? 0 : OOB, WS_FX + L * 32768 + (tile3 * 8 + hg3) * 128);
    const int par3 = tile3 >= 15, ti3 = tile3 - 15 * par3, p03 = 68 * ti3;
    const bool halo = L == 0 && ti3 > 0;
    wait_flags(ws, g3 == 1 ? WS_FF + lr3 * 128 : WS_FX + L * 32768 + ((tile3 - (halo ? (lr3 >> 3) : 0)) * 8 + (l3 & 7)) * 128, 0, tag);
    STAMP(7 + 8 * L);
    {
      const unsigned so = (halo && row3 < 7) ? ((tile3 - 1) * 640 + 72 + row3) * 16 : (tile3 * 640 + row3) * 16;
      u4 v[8];
#pragma unroll
      for (int i = 0; i < 8; ++i) v[i] = ld16(ws, so, WS_ST + L * STB + i * 1280);
      if (L == 0) {
        const unsigned g1_ = l3 * 16 + 16384, g0_ = g1_ - ((halo && lr3 < 7) ? 16256 : 0);
#pragma unroll
        for (int i = 0; i < 4; ++i)
#pragma unroll
          for (int t = 0; t < 5; ++t) dma<1>(ws, t ? g1_ : g0_, xb - 16384 + wv3 * 1024 + t * 16384 + i * 4096, wv3 * 1024, t * 16384 + i * 4096);
      }
      const f4 st = ((asf(v[0]) + asf(v[1])) + (asf(v[2]) + asf(v[3]))) + ((asf(v[4]) + asf(v[5])) + (asf(v[6]) + asf(v[7])));
      const float m = st[0] * (1.f / 512), var = fmx(st[2] * (1.f / 512) - m * m, 0.f), rs = rsq(var + EPS), a1 = 1.f + rs;
      const float c = a1 * rsq(var * a1 * a1 + EPS);
      lds_w<float>(rc_ + O_RC, c); lds_w<float>(rc_ + O_RM, c * m); lds_w<float>(rc_ + O_RA, a1); lds_w<float>(rc_ + O_RB, -m * rs);
    }
    FRESH(4)
    unsigned q1 = L_MISC + lr4 * 4, q4 = L_MISC + g4 * 16;
    OPQ(q1); OPQ(q4);
    if (L == 0) {
      unsigned zl = l4 * 16, zh = zl + 65536;
      OPQ(zh);
      WAITC(0, 0);
      BAR();
      STAMP(26);
      {
        u4 z[2][5];
#pragma unroll
        for (int t = 0; t < 5; ++t) z[0][t] = lds_r<u4>(t < 4 ? zl + t * 16384 : zh);
        SB();
#pragma unroll
        for (int s = 0; s < 16; ++s) {
#pragma unroll
          for (int t = 0; t < 5; ++t) {
            if (s == 0) mWA<true>(aq[t], W[s][0], z[s & 1][t]); else mWA(aq[t], W[s][0], z[s & 1][t]);
            if (s < 15) z[(s + 1) & 1][t] = lds_r<u4>((t < 4 ? zl + t * 16384 : zh) + (s + 1) * 1024); else rowc_read(R, q1, q4, 3 * t);
            SB();
            if (s == 0) mWA<true>(ak[t], W[s][1], z[s & 1][t]); else mWA(ak[t], W[s][1], z[s & 1][t]);
            if (s == 15) rowc_read(R, q1, q4, 3 * t + 1);
            SB();
            if (s == 0) mAW<true>(av[t], z[s & 1][t], W[s][2]); else mAW(av[t], z[s & 1][t], W[s][2]);
            if (s == 15) rowc_read(R, q1, q4, 3 * t + 2);
            SB();
          }
        }
      }
      STAMP(8);
      const u4 sel = wait_rec(ws, (l4 & 1) * 16, WS_SEL + (b & 15) * 128, tag);
      STAMP(9);
#pragma unroll 1
      for (int i = 0; i < 2; ++i) {
        const unsigned key = rdl(sel[0], i);
        const int tok = 2047 - (key & 2047), u = (tok >> 1) - p03;
        if ((tok & 1) != par3 || u < 0 || u >= 75) continue;
        FRESH(5)
        const float s_m = __uint_as_float(rdl(sel[1], i)), s_rs = __uint_as_float(rdl(sel[2], i));
        const float sg = sigm(key_score(key)), a1 = 1.f + s_rs, b1 = -s_m * s_rs;
        const int ts = u / 15, rs_ = u - 15 * ts;
        unsigned yb = L_RING + l5 * 32, rb = L_RING + 8192 + wv5 * 1024, zb = rb + g5 * 16;
        OPQ(yb); OPQ(zb);
        const u4 xr = lds_r<u4>((ts * 16 + (l5 >> 2)) * 1024 + (l5 & 3) * 256 + rs_ * 16);
        const f4 x0 = up4((u2){xr[0], xr[1]}) * a1 + b1, x1 = up4((u2){xr[2], xr[3]}) * a1 + b1;
        const unsigned me = lmask(lr5 == rs_);
        BAR();
        u4 y[8];
        for (int sp = 0; sp < SPIN_CAP; ++sp) {
          CFENCE();
          unsigned m = 0;
#pragma unroll
          for (int j = 0; j < 8; ++j) { y[j] = ld16(ws, l5 * 32 + (j & 1) * 16, WS_YP + (4 * wv5 + (j >> 1)) * 4096 + i * 2048); m = max(m, y[j][0]); }
          if (!__any(m == SENT)) break;
          __builtin_amdgcn_s_sleep(1);
        }
        STAMP(27);
        lds_w<f4>(yb + wv5 * 2048, (asf(y[0]) + asf(y[2])) + (asf(y[4]) + asf(y[6])));
        lds_w<f4>(yb + wv5 * 2048 + 16, (asf(y[1]) + asf(y[3])) + (asf(y[5]) + asf(y[7])));
        WAITL();
        BAR();
        const f4 y0 = (lds_r<f4>(yb) + lds_r<f4>(yb + 2048)) + (lds_r<f4>(yb + 4096) + lds_r<f4>(yb + 6144));
        const f4 y1 = (lds_r<f4>(yb + 16) + lds_r<f4>(yb + 2048 + 16)) + (lds_r<f4>(yb + 4096 + 16) + lds_r<f4>(yb + 6144 + 16));
        const u2 z0 = pk4(y0 * sg + x0), z1 = pk4(y1 * sg + x1);
        lds_w<u4>(rb + l5 * 16, (u4){z0[0], z0[1], z1[0], z1[1]});
        f4 tq, tk, tv;
        u4 z[16];
#pragma unroll
        for (int s = 0; s < 4; ++s) z[s] = lds_r<u4>(zb + s * 64);
        const u2 zx = lds_r<u2>(rb + (64 * hg5 + 16 * wv5 + 4 * g5) * 2);
        SB();
#pragma unroll
        for (int s = 0; s < 16; ++s) {
          if (s == 0) mWA<true>(tq, W[s][0], z[s]); else mWA(tq, W[s][0], z[s]);
          if (s + 4 < 16) z[s + 4] = lds_r<u4>(zb + (s + 4) * 64);
          SB();
          if (s == 0) { mWA<true>(tk, W[s][1], z[s]); mAW<true>(tv, z[s], W[s][2]); } else { mWA(tk, W[s][1], z[s]); mAW(tv, z[s], W[s][2]); }
          SB();
        }
        const f4 f0 = up4(z0), f1 = up4(z1), fs = f0 + f1, fq = f0 * f0 + f1 * f1;
        const float m2 = wave_sum((fs[0] + fs[1]) + (fs[2] + fs[3])) * (1.f / 512), q2 = wave_sum((fq[0] + fq[1]) + (fq[2] + fq[3])) * (1.f / 512);
        const float r2 = rsq(fmx(q2 - m2 * m2, 0.f) + EPS), rm2 = r2 * m2;
        SB();
        if (l5 == 0) { const unsigned ra = L_MISC + (ts * 16 + rs_) * 4; lds_w<float>(ra + O_RA, 1.f); lds_w<float>(ra + O_RB, 0.f); }
#pragma unroll
        for (int t = 0; t < 5; ++t)
          if (t == ts) {
            aq[t] = bsel4(me, tq, aq[t]); ak[t] = bsel4(me, tk, ak[t]);
            xp[t] = (u2){bsel(me, zx[0], xp[t][0]), bsel(me, zx[1], xp[t][1])};
            R.c[t] = bself(me, r2, R.c[t]); R.cm[t] = bself(me, rm2, R.cm[t]);
#pragma unroll
            for (int e = 0; e < 4; ++e) {
              const bool mine = 4 * g5 + e == rs_;
              av[t][e] = mine ? tv[e] : av[t][e]; R.c4[t][e] = mine ? r2 : R.c4[t][e]; R.cm4[t][e] = mine ? rm2 : R.cm4[t][e];
            }
          }
      }
      WAITL();
      BAR();
      STAMP(10);
    } else {
      const rsrc_t ro = mk(P.out, 2048 * 1024);
      WAITL();
      BAR();
#pragma unroll
      for (int t = 0; t < 5; ++t) {
        const float ra = lds_r<float>(q1 + O_RA + t * 64), rb = lds_r<float>(q1 + O_RB + t * 64);
        lds_w<u2>((16 * t + lr4) * 144 + wv4 * 32 + g4 * 8, pk4(up4(xp[t]) * ra + rb));
      }
      WAITL();
      BAR();
#pragma unroll
      for (int i = 0; i < 3; ++i) {
        const int row = min((i * 4 + wv4) * 8 + (l4 >> 3), 79), u = row - (row >> 4), p = p03 + u;
        const bool ok = (i * 4 + wv4) < 10 && (row & 15) != 15 && p < 1024 && (ti3 == 0 || u >= 7);
        st16(ro, lds_r<u4>(row * 144 + (l4 & 7) * 16), ok ? (2 * p + par3) * 1024 + hg4 * 128 + (l4 & 7) * 16 : OOB, 0);
      }
      STAMP(16);
      WAITV(0);
      BAR();
      if (wv4 < 2) {
        const u4 sel = wait_rec(ws, wv4 * 16, WS_SEL + 2048 + (b & 15) * 128, tag);
        STAMP(17);
        const unsigned key = rfl(sel[0]);
        const int tok = 2047 - (key & 2047), u = (tok >> 1) - p03;
        if ((tok & 1) == par3 && u >= (ti3 ? 7 : 0) && u < 75) {
          const f4 base = up4(lds_r<u2>((u + u / 15) * 144 + lr4 * 8));
          const float sg = sigm(key_score(key));
          u4 y[4];
          for (int sp = 0; sp < SPIN_CAP; ++sp) {
            CFENCE();
            unsigned m = 0;
#pragma unroll
            for (int j = 0; j < 4; ++j) { y[j] = ld16(ws, g4 * 4096 + lr4 * 16, WS_YP + 65536 + j * 16384 + wv4 * 2048 + hg4 * 256); m = max(m, y[j][0]); }
            if (!__any(m == SENT)) break;
            __builtin_amdgcn_s_sleep(1);
          }
          const f4 s = (asf(y[0]) + asf(y[1])) + (asf(y[2]) + asf(y[3]));
          const f2 x = swap16(s[0], s[1]), w = swap16(s[2], s[3]), v = swap32(x[0] + x[1], w[0] + w[1]);
          const float e = v[0] + v[1];
          const f2 v2 = swap32(e, e), e01 = swap16(v2[0], v2[0]), e23 = swap16(v2[1], v2[1]);
          __builtin_amdgcn_raw_buffer_store_b64(pk4((f4){e01[0], e01[1], e23[0], e23[1]} * sg + base), ro, l4 < 16 ? tok * 1024 + hg4 * 128 + lr4 * 8 : OOB, 0, 16);
        }
        STAMP(18);
      }
    }
  }
  STAMP_FLUSH();
}

DEV void role_f(const Params& P, const int j, const int wv, const int l) {
  const int lr = l & 15, g = l >> 4, tid = wv * 64 + l;
  const rsrc_t ws = mk(P.ws, WS_USED);
  const u64 t0 = __builtin_amdgcn_s_memrealtime();
  STAMP_INIT();
  STAMP(0);
  const unsigned tag = tag_finish(tag_issue(P.ws));
  const u4 sent = {SENT, SENT, SENT, SENT};
  st16(ws, sent, j * 4096 + tid * 16, WS_YP); st16(ws, sent, j * 4096 + tid * 16, WS_YP + 65536);
  float wsum;
  { u4 w = *(const u4*)(P.wr + l * 8); f4 a = up4((u2){w[0], w[1]}) + up4((u2){w[2], w[3]}); wsum = wave_sum((a[0] + a[1]) + (a[2] + a[3])); }
  WAITV(0);
  BAR();
  if (wv == 0) st4(ws, tag, l == 0 ? 0 : OOB, WS_FF + j * 128);
  for (int sp = 0; sp < 4096 && __builtin_amdgcn_s_memrealtime() - t0 < FDELAY; ++sp) __builtin_amdgcn_s_sleep(4);
  const int noct = j < 12 ? 11 : 10, f0 = 8 * (j < 12 ? 11 * j : 132 + 10 * (j - 12));
  u4 G[16][3], Dn[3][8];
  {
    const rsrc_t rgu = mk(wv < 2 ? P.wg : P.wu, 512 * 2752), rd = mk(P.wd, 1376 * 1024);
    unsigned go[6];
#pragma unroll
    for (int i = 0; i < 6; ++i) { const int n = 64 * ((wv & 1) * 6 + i) + l, row = n / 12; go[i] = row * 2752 + f0 * 2 + (n - 12 * row) * 16; }
    const unsigned dof = (f0 + g) * 1024 + wv * 256 + lr * 16;
    auto issue = [&](int rd_) {
      if (rd_ < 8) {
#pragma unroll
        for (int i = 0; i < 6; ++i) dma<0>(rgu, go[i], rd_ * 64 * 2752, wv * 6144, (rd_ & 3) * 32768 + i * 1024);
      } else {
#pragma unroll
        for (int i = 0; i < 8; ++i) dma<0>(rd, dof, (32 * (rd_ - 8) + 4 * i) * 1024, wv * 8192, (rd_ & 3) * 32768 + i * 1024);
      }
    };
    unsigned tg = ((l >> 1) & 1) * 12288 + (8 * g + (lr >> 2)) * 192 + wv * 48 + (l & 1) * 8, td = wv * 8192 + (8 * g + (lr >> 2)) * 256 + (l & 3) * 8;
    issue(0); issue(1); issue(2);
#pragma unroll
    for (int rd_ = 0; rd_ < 11; ++rd_) {
      if (rd_ < 6) WAITC(12, 0); else if (rd_ == 6) WAITC(14, 0); else if (rd_ < 9) WAITC(16, 0); else if (rd_ == 9) WAITC(8, 0); else WAITC(0, 0);
      BAR();
      if (rd_ + 3 < 11) issue(rd_ + 3);
      unsigned bg = tg + (rd_ & 3) * 32768, bd = td + (rd_ & 3) * 32768;
      OPQ(bg); OPQ(bd);
      if (rd_ < 8) {
#pragma unroll
        for (int ks = 0; ks < 2; ++ks)
#pragma unroll
          for (int c = 0; c < 3; ++c) G[2 * rd_ + ks][c] = trfrag2(bg + ks * 32 * 192 + c * 16, 4 * 192);
      } else {
#pragma unroll
        for (int c = 0; c < 8; ++c) Dn[rd_ - 8][c] = trfrag2(bd + c * 32, 4 * 256);
      }
    }
    WAITL();
    BAR();
  }
  constexpr unsigned F_ACT = 0;
  const unsigned mg1 = lmask(g == 1), mg2 = lmask(g >= 2), mlo = lmask(lr < 8);
  auto gather = [&](const f4& a0, const f4& a1, const f4& a2, float& gv, float& uv) {
    KEEP(a0); KEEP(a1); KEEP(a2);
    const float d0 = bself(mg2, a2[0], bself(mg1, a1[0], a0[0])), d1 = bself(mg2, a2[1], bself(mg1, a1[1], a0[1]));
    gv = __uint_as_float(__builtin_amdgcn_update_dpp(__float_as_uint(d0), __float_as_uint(d1), 0x128, 0xF, 0xC, false));
    uv = __uint_as_float(__builtin_amdgcn_update_dpp(__float_as_uint(d1), __float_as_uint(d0), 0x128, 0xF, 0x3, false));
  };
  float csg, csu;
  {
    u4 one = {ONE2, ONE2, ONE2, ONE2};
    OPQ(one);
    NOPS(2);
    SB();
    f4 a[3];
#pragma unroll
    for (int ks = 0; ks < 16; ++ks)
#pragma unroll
      for (int c = 0; c < 3; ++c) { if (ks == 0) mAW<true>(a[c], one, G[ks][c]); else mAW(a[c], one, G[ks][c]); }
    SB();
    NOPS(8);
    SB();
    gather(a[0], a[1], a[2], csg, csu);
  }
  const bool live = g < 3 && 3 * wv + g < noct;
  const unsigned actw = F_ACT + (g < 3 ? (lr >> 3) * 192 + (24 * wv + 8 * g + (l & 7)) * 2 : 512 + l * 2);
  STAMP(1);
  const int tok = 32 * (4 * j + wv) + (l & 31);
  unsigned sto;
  { const int p = tok >> 1, ti = max(p - 7, 0) / 68, u = p - 68 * ti; sto = ((((tok & 1) * 15 + ti) * 8 + 4 * (l >> 5)) * 80 + u + u / 15) * 16; }

#pragma unroll 1
  for (int L = 0; L < 2; ++L) {
    u4 v[4];
    for (int sp = 0; sp < SPIN_CAP; ++sp) {
      CFENCE();
#pragma unroll
      for (int i = 0; i < 4; ++i) v[i] = ld16(ws, sto, WS_ST + L * STB + i * 1280);
      if (__all(v[0][3] == tag && v[1][3] == tag && v[2][3] == tag && v[3][3] == tag)) break;
      __builtin_amdgcn_s_sleep(1);
    }
    STAMP(2 + 8 * L);
    {
      const f4 s = (asf(v[0]) + asf(v[1])) + (asf(v[2]) + asf(v[3]));
      const float m = xadd32(s[0]) * (1.f / 512), dt = xadd32(s[1]), qq = xadd32(s[2]) * (1.f / 512);
      const float rs = rsq(fmx(qq - m * m, 0.f) + EPS), sc = (dt - m * wsum) * rs;
      unsigned u = __float_as_uint(sc); u ^= (unsigned)((int)u >> 31) | 0x80000000u;
      const unsigned key = (u & 0xFFFFF800u) | (2047u - tok);
      const unsigned k1 = max_x16(max_row16(key)), k2 = max_x16(max_row16(key == k1 ? 0u : key));
      st16(ws, (u4){key, __float_as_uint(m), __float_as_uint(rs), tag}, l >= 32 ? OOB : key == k1 ? 0 : key == k2 ? 16 : OOB, WS_CAND + L * 8192 + (4 * j + wv) * 128);
    }
    STAMP(3 + 8 * L);
    u4 c0, c1;
    for (int sp = 0; sp < SPIN_CAP; ++sp) {
      CFENCE();
      c0 = ld16(ws, l * 128, WS_CAND + L * 8192); c1 = ld16(ws, l * 128 + 16, WS_CAND + L * 8192);
      const unsigned f = ld4(ws, (wv * 60 + min(l, 59)) * 128, WS_FX + L * 32768);
      if (__all(c0[3] == tag && c1[3] == tag && f == tag)) break;
      __builtin_amdgcn_s_sleep(1);
    }
    BAR();
    STAMP(4 + 8 * L);
    const unsigned k1 = rfl(max_x32(max_x16(max_row16(c0[0]))));
    const bool w1 = c0[0] == k1;
    const u4 cb = w1 ? c1 : c0;
    const unsigned k2 = rfl(max_x32(max_x16(max_row16(cb[0]))));
    u4 h[16];
    {
      const int t = 2047 - (((l & 1) ? k2 : k1) & 2047), p = t >> 1, ti = max(p - 7, 0) / 68, u = p - 68 * ti, ts = u / 15;
      const unsigned vo = ((t & 1) * 15 + ti) * TILEB + ts * 16384 + g * 256 + (u - 15 * ts) * 16;
#pragma unroll
      for (int ks = 0; ks < 16; ++ks) h[ks] = ld16(ws, vo + (ks & 3) * 1024, WS_XB + L * BUFB + (ks >> 2) * 4096);
    }
    SB();
    const int s1 = __builtin_ctzll(__ballot(w1) | (1ull << 63)), s2 = __builtin_ctzll(__ballot(cb[0] == k2) | (1ull << 63));
    const unsigned ma = rdl(c0[1], s1), ra = rdl(c0[2], s1), mb = rdl(cb[1], s2), rb = rdl(cb[2], s2);
    if (wv == 0) st16(ws, (l & 1) ? (u4){k2, mb, rb, tag} : (u4){k1, ma, ra, tag}, l < 2 ? l * 16 : OOB, WS_SEL + L * 2048 + j * 128);
    const float m = __uint_as_float(bsel(mlo, ma, mb)), rs = __uint_as_float(bsel(mlo, ra, rb));
    SB();
    {
      f4 a[3];
#pragma unroll
      for (int ks = 0; ks < 16; ++ks)
#pragma unroll
        for (int c = 0; c < 3; ++c) { if (ks == 0) mAW<true>(a[c], h[ks], G[ks][c]); else mAW(a[c], h[ks], G[ks][c]); }
      SB();
      NOPS(8);
      SB();
      float gg, uu;
      gather(a[0], a[1], a[2], gg, uu);
      gg = (gg - m * csg) * rs; uu = (uu - m * csu) * rs;
      const float act = live ? gg * sigm(gg) * uu : 0.f;
      lds_w<u16>(actw, (u16)pk(act, act));
    }
    STAMP(5 + 8 * L);
    WAITL();
    BAR();
    {
      u4 b[3];
#pragma unroll
      for (int ks = 0; ks < 3; ++ks) b[ks] = lds_r<u4>(F_ACT + (l & 1) * 192 + ks * 64 + g * 16);
      const unsigned m2 = lmask(l & 2);
      const unsigned vo = lr < 4 ? j * 4096 + (l & 1) * 2048 + wv * 512 + (l & 2) * 32 + g * 16 : OOB;
      f4 y[8];
      SB();
#pragma unroll
      for (int c = 0; c < 8; ++c) { mWA<true>(y[c], Dn[0][c], b[0]); mWA(y[c], Dn[1][c], b[1]); mVV(y[c], Dn[2][c], b[2]); }
      SB();
#pragma unroll
      for (int c = 0; c < 4; ++c) {
        if (c == 0) { NOPS(1); SB(); }
        u4 w = asu(bsel4(m2, y[2 * c + 1], y[2 * c]));
        w[0] = umin(w[0], SENT - 1);
        st16(ws, w, vo, WS_YP + L * 65536 + c * 128);
        SB();
      }
    }
    STAMP(6 + 8 * L);
  }
  if (j == 0 && wv == 0) st4(ws, tag, l == 0 ? 0 : OOB, WS_EPOCH);
  STAMP_FLUSH();
}

__global__ void __launch_bounds__(256, 1) fused_kernel(Params P) {
  const int b = blockIdx.x, wv = __builtin_amdgcn_readfirstlane(threadIdx.x >> 6), l = threadIdx.x & 63;
  if (b < 240) role_a(P, b, wv, l);
  else role_f(P, b - 240, wv, l);
}

extern "C" void kernel_launch(void* const* d_in, const int* in_sizes, int n_in, void* d_out, int out_size, void* d_ws, size_t ws_size, hipStream_t stream) {
  Params P;
  P.x = (const u16*)d_in[0]; P.wqkv = (const u16*)d_in[1]; P.wo = (const u16*)d_in[2];
  P.wr = (const u16*)d_in[4]; P.wg = (const u16*)d_in[5]; P.wu = (const u16*)d_in[6]; P.wd = (const u16*)d_in[7];
  P.out = (u16*)d_out; P.ws = (char*)d_ws;
  (void)hipFuncSetAttribute((const void*)fused_kernel, hipFuncAttributeMaxDynamicSharedMemorySize, LDS_BYTES);
  fused_kernel<<<256, 256, LDS_BYTES, stream>>>(P);
}
```

```cpp
#define PF_CODE
#define PF_TLB
#include <hip/hip_runtime.h>
#include <hip/hip_bf16.h>
#include <stdint.h>

typedef unsigned short u16;
typedef unsigned long long u64;
typedef __bf16 bf2 __attribute__((ext_vector_type(2)));
typedef short s4 __attribute__((ext_vector_type(4)));
typedef float f4 __attribute__((ext_vector_type(4)));
typedef float f2 __attribute__((ext_vector_type(2)));
typedef unsigned u4 __attribute__((ext_vector_type(4)));
typedef unsigned u2 __attribute__((ext_vector_type(2)));
typedef __amdgpu_buffer_rsrc_t rsrc_t;
#define LAS __attribute__((address_space(3)))
#define DEV __device__ __forceinline__

constexpr unsigned TILEB = 81920;
constexpr unsigned BUFB = 30 * TILEB;
constexpr unsigned WS_EPOCH = 0;
constexpr unsigned WS_FF = 4096;
constexpr unsigned WS_SEL = 8192;
constexpr unsigned WS_CAND = 16384;
constexpr unsigned WS_FA = 32768;
constexpr unsigned WS_FX = 65536;
constexpr unsigned WS_YP = 131072;
constexpr unsigned WS_ST = 262144;
constexpr unsigned STB = 30 * 8 * 80 * 16;
constexpr unsigned WS_OB = 1048576;
constexpr unsigned WS_XB = WS_OB + 2 * BUFB;
constexpr unsigned WS_USED = WS_XB + 2 * BUFB;
constexpr unsigned L_RING = 81920;
constexpr unsigned L_RED2 = L_RING + 16384;
constexpr unsigned L_MISC = 155648;
constexpr unsigned O_RED = 0;
constexpr unsigned O_RC = 4096, O_RM = O_RC + 320, O_RA = O_RM + 320, O_RB = O_RA + 320;
constexpr unsigned LDS_BYTES = 163840;
constexpr unsigned SENT = 0xFFFFFFFFu, OOB = 0x80000000u, ONE2 = 0x3F803F80u;
#define FDELAY 200
constexpr int SPIN_CAP = 1 << 15;
constexpr float EPS = 1e-5f;

#define CFENCE() asm volatile("" ::: "memory")
#define WAITC(vm, lg) do { CFENCE(); __builtin_amdgcn_s_waitcnt(((vm) & 15) | 0x70 | ((lg) << 8) | (((vm) >> 4) << 14)); CFENCE(); } while (0)
#define WAITV(n) asm volatile("s_waitcnt vmcnt(%0)" ::"i"(n) : "memory")
#define WAITL() WAITC(63, 0)
#define BAR() do { CFENCE(); __builtin_amdgcn_s_barrier(); CFENCE(); } while (0)
#define SB() __builtin_amdgcn_sched_barrier(0)
#define OPQ(x) asm volatile("" : "+v"(x))
#define KEEP(x) asm volatile("" ::"v"(x))

struct Params {
  const u16* x; const u16* wqkv; const u16* wo; const u16* wr; const u16* wg; const u16* wu; const u16* wd;
  u16* out; char* ws;
};

DEV rsrc_t mk(const void* p, unsigned bytes) { return __builtin_amdgcn_make_buffer_rsrc((void*)p, 0, bytes, 0x00020000); }
DEV void setm0(unsigned base, int imm) { asm volatile("s_add_u32 m0, %0, %1" ::"s"(base), "i"(imm) : "scc", "memory"); }
template <int SC> DEV void dma_go(rsrc_t rs, unsigned voff, unsigned soff) {
  if (SC) asm volatile("buffer_load_dwordx4 %0, %1, %2 offen sc1 lds" ::"v"(voff), "s"(rs), "s"(soff) : "memory");
  else asm volatile("buffer_load_dwordx4 %0, %1, %2 offen lds" ::"v"(voff), "s"(rs), "s"(soff) : "memory");
}
DEV void dma_go_m(rsrc_t rs, unsigned voff, unsigned soff, u64 mask) {
  asm volatile("s_mov_b64 exec, %3\n\tbuffer_load_dwordx4 %0, %1, %2 offen lds\n\ts_mov_b64 exec, -1" ::"v"(voff), "s"(rs), "s"(soff), "s"(mask) : "memory");
}
template <int SC> DEV void dma(rsrc_t rs, unsigned voff, unsigned soff, unsigned base, int imm) {
  if (SC) asm volatile("s_add_u32 m0, %3, %4\n\ts_nop 0\n\tbuffer_load_dwordx4 %0, %1, %2 offen sc1 lds" ::"v"(voff), "s"(rs), "s"(soff), "s"(base), "i"(imm) : "scc", "memory");
  else asm volatile("s_add_u32 m0, %3, %4\n\ts_nop 0\n\tbuffer_load_dwordx4 %0, %1, %2 offen lds" ::"v"(voff), "s"(rs), "s"(soff), "s"(base), "i"(imm) : "scc", "memory");
}
DEV void st16(rsrc_t rs, u4 v, unsigned voff, unsigned soff) {
  asm volatile("buffer_store_dwordx4 %0, %1, %2, %3 offen sc1\n\ts_nop 1" ::"v"(v), "v"(voff), "s"(rs), "s"(soff) : "memory");
}
DEV void st4(rsrc_t rs, unsigned v, unsigned voff, unsigned soff) {
  asm volatile("buffer_store_dword %0, %1, %2, %3 offen sc1" ::"v"(v), "v"(voff), "s"(rs), "s"(soff) : "memory");
}
DEV u4 ld16(rsrc_t rs, unsigned voff, unsigned soff) { return __builtin_amdgcn_raw_buffer_load_b128(rs, voff, soff, 16); }
DEV unsigned ld4(rsrc_t rs, unsigned voff, unsigned soff) { return __builtin_amdgcn_raw_buffer_load_b32(rs, voff, soff, 16); }

template <bool Z = false> DEV void mWA(f4& c, u4 w, u4 a) {
  if (Z) asm volatile("v_mfma_f32_16x16x32_bf16 %0, %1, %2, 0" : "=v"(c) : "a"(w), "v"(a));
  else asm volatile("v_mfma_f32_16x16x32_bf16 %0, %1, %2, %0" : "+v"(c) : "a"(w), "v"(a));
}
template <bool Z = false> DEV void mAW(f4& c, u4 a, u4 w) {
  if (Z) asm volatile("v_mfma_f32_16x16x32_bf16 %0, %1, %2, 0" : "=v"(c) : "v"(a), "a"(w));
  else asm volatile("v_mfma_f32_16x16x32_bf16 %0, %1, %2, %0" : "+v"(c) : "v"(a), "a"(w));
}
template <bool Z = false> DEV void mVV(f4& c, u4 a, u4 b) {
  if (Z) asm volatile("v_mfma_f32_16x16x32_bf16 %0, %1, %2, 0" : "=v"(c) : "v"(a), "v"(b));
  else asm volatile("v_mfma_f32_16x16x32_bf16 %0, %1, %2, %0" : "+v"(c) : "v"(a), "v"(b));
}
template <bool Z = false> DEV void m16(f4& c, u2 a, u2 b) {
  if (Z) asm volatile("v_mfma_f32_16x16x16_bf16 %0, %1, %2, 0" : "=v"(c) : "v"(a), "v"(b));
  else asm volatile("v_mfma_f32_16x16x16_bf16 %0, %1, %2, %0" : "+v"(c) : "v"(a), "v"(b));
}
DEV f4 m16c(u2 a, u2 b, f4 c) { f4 d; asm volatile("v_mfma_f32_16x16x16_bf16 %0, %1, %2, %3" : "=&v"(d) : "v"(a), "v"(b), "v"(c)); return d; }
template <bool Z = false> DEV void mf32(f4& c, float a, float b) {
  if (Z) asm volatile("v_mfma_f32_16x16x4_f32 %0, %1, %2, 0" : "=&v"(c) : "v"(a), "v"(b));
  else asm volatile("v_mfma_f32_16x16x4_f32 %0, %1, %2, %0" : "+v"(c) : "v"(a), "v"(b));
}
#define NOPS(n) asm volatile("s_nop %0" ::"i"((n) - 1))
DEV unsigned pk(float a, float b) { f2 v = {a, b}; return __builtin_bit_cast(unsigned, __builtin_convertvector(v, bf2)); }
DEV u2 pk4(f4 v) { return (u2){pk(v[0], v[1]), pk(v[2], v[3])}; }
DEV float lo(unsigned u) { return __uint_as_float(u << 16); }
DEV float hi(unsigned u) { return __uint_as_float(u & 0xFFFF0000u); }
DEV f4 up4(u2 u) { return (f4){lo(u[0]), hi(u[0]), lo(u[1]), hi(u[1])}; }
DEV f4 asf(u4 v) { return __builtin_bit_cast(f4, v); }
DEV u4 asu(f4 v) { return __builtin_bit_cast(u4, v); }
DEV unsigned bsel(unsigned m, unsigned a, unsigned b) { return __builtin_amdgcn_bitop3_b32(m, a, b, 0xCA); }
DEV float bself(unsigned m, float a, float b) { return __uint_as_float(bsel(m, __float_as_uint(a), __float_as_uint(b))); }
DEV f4 bsel4(unsigned m, f4 a, f4 b) { return (f4){bself(m, a[0], b[0]), bself(m, a[1], b[1]), bself(m, a[2], b[2]), bself(m, a[3], b[3])}; }
DEV unsigned lmask(bool c) { unsigned m = c ? ~0u : 0u; OPQ(m); return m; }
DEV float fmx(float a, float b) { return __builtin_elementwise_maximum(a, b); }
template <class T> DEV T lds_r(unsigned a) { return *(const LAS T*)(size_t)a; }
template <class T> DEV void lds_w(unsigned a, T v) { *(LAS T*)(size_t)a = v; }
DEV u4 trfrag2(unsigned a, unsigned step) {
  u2 x = __builtin_bit_cast(u2, __builtin_amdgcn_ds_read_tr16_b64_v4i16((LAS s4*)(size_t)a));
  u2 y = __builtin_bit_cast(u2, __builtin_amdgcn_ds_read_tr16_b64_v4i16((LAS s4*)(size_t)(a + step)));
  return (u4){x[0], x[1], y[0], y[1]};
}
DEV f2 swap16(float a, float b) { auto r = __builtin_amdgcn_permlane16_swap(__float_as_uint(a), __float_as_uint(b), false, false); return (f2){__uint_as_float(r[0]), __uint_as_float(r[1])}; }
DEV f2 swap32(float a, float b) { auto r = __builtin_amdgcn_permlane32_swap(__float_as_uint(a), __float_as_uint(b), false, false); return (f2){__uint_as_float(r[0]), __uint_as_float(r[1])}; }
DEV float xadd16(float v) { f2 r = swap16(v, v); return r[0] + r[1]; }
DEV float xadd32(float v) { f2 r = swap32(v, v); return r[0] + r[1]; }
template <int C> DEV unsigned dpp(unsigned v) { return __builtin_amdgcn_update_dpp(0u, v, C, 0xF, 0xF, true); }
template <int C> DEV float dppf(float v) { return __uint_as_float(dpp<C>(__float_as_uint(v))); }
DEV float wave_sum(float v) {
  v += dppf<0xB1>(v); v += dppf<0x4E>(v); v += dppf<0x141>(v); v += dppf<0x140>(v);
  return xadd32(xadd16(v));
}
DEV unsigned max_row16(unsigned v) { v = max(v, dpp<0xB1>(v)); v = max(v, dpp<0x4E>(v)); v = max(v, dpp<0x141>(v)); return max(v, dpp<0x140>(v)); }
DEV unsigned max_x16(unsigned v) { auto r = __builtin_amdgcn_permlane16_swap(v, v, false, false); return max(r[0], r[1]); }
DEV unsigned max_x32(unsigned v) { auto r = __builtin_amdgcn_permlane32_swap(v, v, false, false); return max(r[0], r[1]); }
DEV unsigned umin(unsigned a, unsigned b) { return a < b ? a : b; }
DEV unsigned rfl(unsigned v) { return __builtin_amdgcn_readfirstlane(v); }
DEV unsigned rdl(unsigned v, int l) { return __builtin_amdgcn_readlane(v, l); }
DEV float rsq(float v) { return __builtin_amdgcn_rsqf(v); }
DEV float sigm(float v) { return __builtin_amdgcn_rcpf(1.f + __builtin_amdgcn_exp2f(-1.44269504f * v)); }
DEV float key_score(unsigned k) { unsigned u = k & 0xFFFFF800u; return __uint_as_float((u & 0x80000000u) ? (u ^ 0x80000000u) : ~u); }

#define STAMP(i)
#define STAMP_INIT()
#define STAMP_FLUSH()
DEV unsigned tag_issue(const char* ws) { unsigned e; asm volatile("s_load_dword %0, %1, 0x0" : "=s"(e) : "s"(ws) : "memory"); return e; }
DEV unsigned tag_finish(unsigned e) { unsigned t; asm volatile("s_waitcnt lgkmcnt(0)\n\ts_add_u32 %0, %1, 1" : "=s"(t) : "s"(e) : "memory", "scc"); return t; }

DEV void wait_flags(rsrc_t ws, unsigned voff, unsigned base, unsigned tag) {
  for (int sp = 0; sp < SPIN_CAP; ++sp) {
    CFENCE();
    if (__all(ld4(ws, voff, base) == tag)) break;
    __builtin_amdgcn_s_sleep(1);
  }
}
DEV u4 wait_rec(rsrc_t ws, unsigned voff, unsigned base, unsigned tag) {
  u4 c;
  for (int sp = 0; sp < SPIN_CAP; ++sp) {
    CFENCE();
    c = ld16(ws, voff, base);
    if (__all(c[3] == tag)) break;
    __builtin_amdgcn_s_sleep(1);
  }
  return c;
}

struct RowC { float c[5], cm[5]; f4 c4[5], cm4[5]; };
DEV void rowc_read(RowC& R, unsigned r1, unsigned r4, int i) {
  const int t = i / 3;
  if (i % 3 == 0) { R.c[t] = lds_r<float>(r1 + O_RC + t * 64); R.cm[t] = lds_r<float>(r1 + O_RM + t * 64); }
  else if (i % 3 == 1) R.c4[t] = lds_r<f4>(r4 + O_RC + t * 64);
  else R.cm4[t] = lds_r<f4>(r4 + O_RM + t * 64);
}

DEV void role_a(const Params& P, const int b, const int wv, const int l) {
  const int xcd = b & 7, r = b >> 3;
  const int q = (xcd >> 1) * 30 + r, tile = q >> 2, hg = (xcd & 1) * 4 + (q & 3);
  const int lr = l & 15, g = l >> 4;
  const rsrc_t ws = mk(P.ws, WS_USED);
  const unsigned epoch = tag_issue(P.ws);
  STAMP_INIT();
  STAMP(0);
  u4 W[16][3];
  f4 aq[5], ak[5], av[5];
  u2 xp[5];
  RowC R;
  unsigned tag, pm;
  f4 mask;
  u2 wr2;
  const unsigned trb = L_RING + (8 * g + (lr >> 2)) * 128 + (((2 * wv + ((l & 3) >> 1)) ^ ((((lr >> 3) & 1) | ((g & 1) << 1)) << 1)) << 4) + 8 * (l & 1);
  {
    const int par = tile >= 15, p0 = 68 * (tile - 15 * par);
    const rsrc_t rx = mk(P.x, 2048 * 1024), rw = mk(P.wqkv, 512 * 3072), rwo = mk(P.wo, 512 * 1024);
    unsigned xoff[5];
    {
      const int rr = 4 * wv + g;
#pragma unroll
      for (int j = 0; j < 5; ++j) { int p = min(p0 + 15 * j + rr, 1023); xoff[j] = (2 * p + par) * 1024 + ((lr ^ rr) << 4); }
    }
    const u64 xmask = wv == 3 ? 0x0000FFFFFFFFFFFFull : ~0ull;
    const int k8 = l >> 3;
    const unsigned swA = (((l & 7) ^ (((k8 >> 1) & 1) << 1)) << 4) ^ ((wv & 1) << 6), swB = swA ^ 64;
    const unsigned woffA = k8 * 3072 + hg * 128 + swA, woffB = k8 * 3072 + hg * 128 + swB;
    const unsigned lx = wv * 1024, lw = L_RING + wv * 3072;
    unsigned wso[3];
#pragma unroll
    for (int j = 0; j < 3; ++j) { const int i = wv * 3 + j; wso[j] = (i & 3) * 8 * 3072 + (i >> 2) * 1024; }
    auto ldx = [&](int pr) {
#pragma unroll
      for (int j = 0; j < 5; ++j) { setm0(lx, pr * 20480 + j * 4096); dma_go_m(rx, xoff[j], pr * 256, xmask); }
    };
    auto ldw = [&](int s) {
#pragma unroll
      for (int j = 0; j < 3; ++j) dma<0>(rw, (j & 1) ? woffB : woffA, wso[j] + s * 98304, lw, (s % 6) * 12288 + j * 1024);
    };
    asm volatile("global_load_dwordx2 %0, %1, %2" : "=v"(wr2) : "v"((16 * wv + 4 * g) * 2), "s"(P.wr + 64 * hg) : "memory");
    ldx(0); ldw(0); ldw(1);
    unsigned touch;
    {
      int rh = (3 - (int)(((size_t)P.wqkv) >> 7)) & 3, idx = min(r * 52 + wv * 13 + min(l, 12), 1535);
      unsigned off = (idx / 3) * 3072 + (idx % 3) * 1024 + ((xcd & 1) * 4 + rh) * 128;
      asm volatile("buffer_load_dword %0, %1, %2, 0 offen" : "=v"(touch) : "v"(off), "s"(rw) : "memory");
    }
    ldw(2); ldx(1); ldw(3); ldw(4);
    tag = tag_finish(epoch);
    {
      const u4 one = {ONE2, ONE2, ONE2, ONE2};
      const int row = wv * 4 + g;
      lds_w<u4>((row / 5) * 20480 + ((row % 5) * 16 + 15) * 256 + lr * 16, one);
      if (wv == 0) lds_w<u4>(3 * 20480 + ((g + 1) * 16 + 15) * 256 + lr * 16, one);
    }
    const unsigned ooff = tile * TILEB + (((g & 1) * 32 + 4 * hg + wv) * 2 + (g >> 1)) * 256 + lr * 16;
    const unsigned ooff4 = (g & 1) ? OOB : ooff;
    u4 sent = {SENT, SENT, SENT, SENT};
    OPQ(sent);
    const unsigned wooffA = k8 * 1024 + hg * 128 + swA, wooffB = k8 * 1024 + hg * 128 + swB;
    unsigned xa[4];
#pragma unroll
    for (int i = 0; i < 4; ++i) xa[i] = lr * 256 + (((4 * i + g) ^ lr) << 4);
    const unsigned xg = lr * 256 + (((4 * wv + g) ^ lr) << 4);
    const unsigned xr = (hg >> 1) * 20480 + lr * 256 + ((((hg & 1) * 8 + 2 * wv + (g >> 1)) ^ lr) << 4) + 8 * (g & 1);
    unsigned mq = L_MISC + O_RED + (g == (lr >> 2) ? (wv * 80 + lr) * 8 + 4 : 2560 + l * 4), ms = L_MISC + O_RED + (g == 3 ? (wv * 80 + lr) * 8 : 2560 + l * 4);
    unsigned rb_ = L_MISC + O_RED + (wv * 20 + min(l, 19)) * 8, rc_ = L_MISC + (wv * 20 + min(l, 19)) * 4;
    unsigned r1 = L_MISC + lr * 4, r4 = L_MISC + g * 16;
    OPQ(mq); OPQ(ms); OPQ(rb_); OPQ(rc_); OPQ(r1); OPQ(r4);
    const unsigned m1 = lmask(l & 1), m2 = lmask(l & 2);
    pm = lmask(lr < 8 && g >= 2);
#pragma unroll
    for (int i = 0; i < 4; ++i) { const int rk = 4 * g + i, d = (lr < 8 && g >= 2) ? lr + 15 - rk : lr - rk; mask[i] = (d >= 0 && d <= 7 && rk != 15) ? 0.f : -1e30f; }
    OPQ(mask);
    f4 gr[5];
#pragma unroll
    for (int t = 0; t < 5; ++t) { aq[t] = ak[t] = av[t] = gr[t] = (f4){0, 0, 0, 0}; OPQ(aq[t]); OPQ(ak[t]); OPQ(av[t]); OPQ(gr[t]); }
    u4 a[2][5], z[2][5];
    f2 rv[4];
#pragma unroll
    for (int s = 0; s <= 16; ++s) {
      constexpr int NV[17] = {18, 20, 20, 16, 17, 17, 17, 17, 17, 15, 14, 16, 15, 15, 15, 15, 15};
      switch (s) {
#define C_(n) case n: WAITC(NV[n], 0); break;
        C_(0) C_(1) C_(2) C_(3) C_(4) C_(5) C_(6) C_(7) C_(8) C_(9) C_(10) C_(11) C_(12) C_(13) C_(14) C_(15) C_(16)
#undef C_
      }
      BAR();
      if (s == 0) STAMP(1);
      if (s == 4) STAMP(19);
      if (s == 8) STAMP(20);
      if (s == 12) STAMP(21);
      if (s == 16) STAMP(22);
      SB();
      auto gtile = [](int s_, int i) {
        if (s_ < 0) return -1;
        if (s_ < 5) return i == 0 ? s_ : -1;
        if (s_ < 7) return i < 2 ? 8 + 2 * (s_ - 5) + i : -1;
        if (s_ == 7) return i == 0 ? 12 : -1;
        if (s_ == 8) return i < 2 ? 16 + i : -1;
        if (s_ < 12) return i == 0 ? 16 + s_ - 7 : -1;
        if (s_ == 12) return 24 + i;
        return -1;
      };
      const int nx = s < 8 ? ((s & 3) == 0 ? 2 : 1) : 0, nd = s < 16 ? nx + 3 : 0;
#pragma unroll
      for (int i = 0; i < 20; ++i) {
        if (s > 0) {
          const int pb = (s - 1) & 1;
          if (i < 15) {
            const int t = i / 3;
            if (i % 3 == 0) mWA(aq[t], W[s - 1][0], a[pb][t]); else if (i % 3 == 1) mWA(ak[t], W[s - 1][1], a[pb][t]); else mAW(av[t], a[pb][t], W[s - 1][2]);
          } else if (gtile(s - 1, i - 15) >= 0) mVV(gr[gtile(s - 1, i - 15) & 7], z[pb][i - 15], z[pb][i - 15]);
        }
        if (s < 16) {
          if (i < 3) W[s][i] = trfrag2(trb + (s % 6) * 12288 + i * 4096, 512);
          else if (i < 8) a[s & 1][i - 3] = lds_r<u4>(xa[s & 3] + (s >> 2) * 20480 + (i - 3) * 4096);
          else if (i < 13 && gtile(s, i - 8) >= 0) z[s & 1][i - 8] = lds_r<u4>(xg + (gtile(s, i - 8) >> 3) * 20480 + (gtile(s, i - 8) & 7) * 4096);
          if (i >= 3 && (i - 3) / 2 < nd) {
            const int k = (i - 3) / 2, ph = (i - 3) & 1;
            if (k < nx) {
              const int pr = 2 + (s >> 2), j = (s & 3) == 0 ? k : (s & 3) + 1;
              if (ph == 0) setm0(lx, pr * 20480 + j * 4096); else dma_go_m(rx, xoff[j], pr * 256, xmask);
            } else {
              const int j = k - nx;
              if (ph == 0) setm0(lw, ((s + 5) % 6) * 12288 + j * 1024);
              else if (s < 11) dma_go<0>(rw, (j & 1) ? woffB : woffA, wso[j] + (s + 5) * 98304);
              else dma_go<0>(rwo, (j & 1) ? wooffB : wooffA, (((3 * (s - 11) + ((wv * 3 + j) >> 2) + 2 * hg) & 15) * 32 + ((wv * 3 + j) & 3) * 8) * 1024);
            }
          }
          if (s == 10 && i >= 10 && i < 13) st16(ws, sent, i < 12 ? ooff : ooff4, WS_OB + (i - 10) * 32768);
          if (s == 13 && i >= 10 && i < 15) xp[i - 10] = lds_r<u2>(xr + (i - 10) * 4096);
          if (s == 14 && i >= 8 && i < 13) {
            const f4 v = gr[i - 8];
            lds_w<float>(mq + (i - 8) * 128, bself(m2, bself(m1, v[3], v[2]), bself(m1, v[1], v[0])));
            lds_w<float>(ms + (i - 8) * 128, v[3]);
          }
          if (s == 15 && i == 8) { rv[0] = lds_r<f2>(rb_); rv[1] = lds_r<f2>(rb_ + 640); rv[2] = lds_r<f2>(rb_ + 1280); rv[3] = lds_r<f2>(rb_ + 1920); }
          if (s == 15 && i == 12) {
            const f2 v = (rv[0] + rv[1]) + (rv[2] + rv[3]);
            const float m = v[0] * (1.f / 512), rs = rsq(fmx(v[1] * (1.f / 512) - m * m, 0.f) + EPS);
            lds_w<float>(rc_ + O_RC, rs); lds_w<float>(rc_ + O_RM, rs * m); lds_w<float>(rc_ + O_RA, 1.f); lds_w<float>(rc_ + O_RB, 0.f);
          }
        } else {
          if (i < 15) rowc_read(R, r1, r4, i);
          if (i == 0 && wv == 0) st4(ws, tag, l == 0 ? 0 : OOB, WS_FA + (tile * 8 + hg) * 128);
          if (i == 2) dma<0>(rwo, wooffA, (((15 + 2 * hg) & 15) * 32 + wv * 8) * 1024, lx, L_RING + 3 * 12288);
          if (i >= 4 && i < 7) st16(ws, sent, i < 6 ? ooff : ooff4, WS_OB + BUFB + (i - 4) * 32768);
        }
        SB();
      }
    }
    asm volatile("" ::"v"(touch));
    OPQ(wr2);
  }
  NOPS(2);
  SB();
  f4 csq, csk;
#pragma unroll
  for (int i = 0; i < 4; ++i) { csq[i] = dppf<0x15F>(aq[0][i]); csk[i] = dppf<0x15F>(ak[0][i]); }
  float csv;
  { f2 x = swap32(av[0][3], av[0][3]); f2 y = swap16(x[1], x[1]); csv = y[1]; }
  STAMP(2);
  u4 WO[16];

#pragma unroll 1
  for (int L = 0; L < 2; ++L) {
#define FRESH(n) int l##n = l, wv##n = wv, hg##n = hg, tile##n = tile; asm volatile("" : "+v"(l##n)); asm volatile("" : "+s"(wv##n), "+s"(hg##n), "+s"(tile##n)); \
    const int lr##n = l##n & 15, g##n = l##n >> 4; (void)lr##n; (void)g##n;
    FRESH(1)
    const unsigned ob = WS_OB + L * BUFB + tile1 * TILEB;
    unsigned fa;
    {
      u2 kb[5], vb[5], qb[5], o[5];
      f4 ss[5], sp[5], sc[5];
      float mx[5];
      auto scale = [&](int t) {
        qb[t] = pk4(aq[t] * R.c[t] - csq * R.cm[t]);
        kb[t] = pk4(ak[t] * R.c[t] - csk * R.cm[t]);
        vb[t] = pk4(av[t] * R.c4[t] - R.cm4[t] * csv);
      };
      auto smm = [&](int t) { ss[t] = m16c(kb[t], qb[t], mask); if (t) sp[t] = m16c(kb[t - 1], qb[t], mask); };
      auto pick = [&](int t) {
        sc[t] = bsel4(pm, t ? sp[t] : (f4){-1e30f, -1e30f, -1e30f, -1e30f}, ss[t]);
        mx[t] = fmx(fmx(fmx(sc[t][0], sc[t][1]), sc[t][2]), sc[t][3]);
      };
      scale(0); SB();
      scale(1); SB(); smm(0); SB();
      scale(2); SB(); smm(1); SB();
      scale(3); SB(); smm(2); SB();
      scale(4); SB(); smm(3); smm(4); SB();
      pick(0); pick(1); pick(2); SB();
      pick(3); pick(4);
      {
        f2 x = swap16(mx[0], mx[1]), y = swap16(mx[2], mx[3]), w = swap16(mx[4], mx[4]);
        f2 u = swap32(fmx(x[0], x[1]), fmx(y[0], y[1]));
        const float w1 = fmx(w[0], w[1]), c = fmx(u[0], u[1]) * 0.36067376f;
        f2 w2 = swap32(w1, w1), v = swap32(c, c);
        f2 v0 = swap16(v[0], v[0]), v1 = swap16(v[1], v[1]);
        mx[0] = v0[0]; mx[1] = v0[1]; mx[2] = v1[0]; mx[3] = v1[1]; mx[4] = fmx(w2[0], w2[1]) * 0.36067376f;
      }
      asm volatile("buffer_load_dword %0, %1, %2, %3 offen sc1" : "=v"(fa) : "v"((l1 & 7) * 128), "s"(ws), "s"(WS_FA + tile1 * 1024) : "memory");
      u2 one2 = {ONE2, ONE2};
      float kap = 0.36067376f;
      OPQ(one2); OPQ(kap);
      u2 pb[5], ps[5], pp[5];
      f4 sum[5], oa[5];
      auto expo = [&](int t) {
        const f4 d = sc[t] * kap - mx[t];
        f4 e;
#pragma unroll
        for (int i = 0; i < 4; ++i) e[i] = __builtin_amdgcn_exp2f(d[i]);
        pb[t] = pk4(e);
        if (t) { pp[t] = pb[t] & pm; ps[t] = pb[t] ^ pp[t]; } else ps[t] = pb[t];
      };
      auto pv = [&](int t) { m16<true>(oa[t], vb[t], ps[t]); if (t) m16(oa[t], vb[t - 1], pp[t]); m16<true>(sum[t], one2, pb[t]); };
      auto norm = [&](int t) { KEEP(sum[t]); o[t] = pk4(oa[t] * __builtin_amdgcn_rcpf(sum[t][0])); };
      const unsigned go = (((g1 & 1) * 32 + 4 * hg1 + wv1) * 2 + (g1 >> 1)) * 256 + lr1 * 16;
      const unsigned lo_ = (((g1 & 1) * 32 + wv1) * 2 + (g1 >> 1)) * 256 + lr1 * 16;
      auto put = [&](int i) {
        u2 A = o[2 * i], B = o[i < 2 ? 2 * i + 1 : 4];
        auto s0 = __builtin_amdgcn_permlane16_swap(A[0], B[0], false, false); auto s1 = __builtin_amdgcn_permlane16_swap(A[1], B[1], false, false);
        const u4 e = {umin(s0[0], SENT - 1), s1[0], s0[1], s1[1]};
        st16(ws, e, (i == 2 && (g1 & 1)) ? OOB : go, ob + i * 32768);
        lds_w<u4>(i < 2 ? lo_ + i * 32768 : ((g1 & 1) ? lo_ + 2048 : lo_ + 65536), e);
      };
      SB();
      expo(0); SB();
      expo(1); SB(); pv(0); SB();
      expo(2); SB(); pv(1); SB();
      expo(3); SB(); pv(2); SB(); norm(0); SB();
      expo(4); SB(); pv(3); SB(); norm(1); SB(); pv(4); SB();
      put(0); norm(2); SB(); norm(3); SB();
      put(1); norm(4); SB();
      put(2);
    }
    STAMP(3 + 8 * L);
    FRESH(2)
    const unsigned l16 = l2 * 16;
    unsigned l16h = l16 + 65536, r1 = L_MISC + lr2 * 4;
    OPQ(l16h); OPQ(r1);
    f4 acc[5];
#pragma unroll
    for (int t = 0; t < 5; ++t) acc[t] = up4(xp[t]) * lds_r<float>(r1 + O_RA + t * 64) + lds_r<float>(r1 + O_RB + t * 64);
    WAITC(3, 0);
    OPQ(fa);
    BAR();
    if (L == 0) {
      unsigned tb = trb;
      OPQ(tb);
#pragma unroll
      for (int j = 0; j < 16; ++j) { WO[j] = trfrag2(tb + ((4 + j / 3) % 6) * 12288 + (j % 3) * 4096, 512); asm volatile("" : "+a"(WO[j])); }
      if (!__all(fa == tag)) wait_flags(ws, (l2 & 7) * 128, WS_FA + tile2 * 1024, tag);
    }
    u4 a[3][5];
    auto rda = [&](int j, int t) { a[j % 3][t] = lds_r<u4>((t < 4 ? l16 + t * 16384 : l16h) + j * 1024); };
#pragma unroll
    for (int t = 0; t < 5; ++t) { rda(0, t); rda(1, t); }
    const unsigned lw2 = wv2 * 1024;
    SB();
#pragma unroll
    for (int i = 0; i < 3; ++i) {
      const unsigned so = ob + ((2 * hg2 + 2 + 4 * i + wv2) & 15) * 1024;
#pragma unroll
      for (int t = 0; t < 5; ++t) {
        if (i == 0) { setm0(lw2, 2048 + t * 16384); SB(); mWA(acc[t], WO[0], a[0][t]); SB(); dma_go<1>(ws, l16, so + t * 16384); }
        else dma<1>(ws, l16, so + t * 16384, lw2, (2 + 4 * i) * 1024 + t * 16384);
      }
    }
    {
      const unsigned so = ob + ((2 * hg2 + 14 + (wv2 & 1)) & 15) * 1024;
#pragma unroll
      for (int i = 0; i < 3; ++i) { const unsigned t = min((wv2 >> 1) * 3 + i, 4); dma<1>(ws, l16, so + t * 16384, (wv2 & 1) * 1024 + t * 16384, 14 * 1024); }
    }
    STAMP(4 + 8 * L);
    auto check = [&](int j) {
      u4 c0 = a[j % 3][0], c1 = a[j % 3][1], c2 = a[j % 3][2], c3 = a[j % 3][3], c4 = a[j % 3][4];
      unsigned m = max(max(max(c0[0], c1[0]), max(c2[0], c3[0])), c4[0]);
      if (__builtin_expect(__any(m == SENT), 0)) {
        const unsigned so = ob + ((2 * hg2 + j) & 15) * 1024;
        for (int sp = 0; sp < SPIN_CAP; ++sp) {
          CFENCE();
          c0 = ld16(ws, l16, so); c1 = ld16(ws, l16, so + 16384); c2 = ld16(ws, l16, so + 32768); c3 = ld16(ws, l16, so + 49152); c4 = ld16(ws, l16, so + 65536);
          m = max(max(max(c0[0], c1[0]), max(c2[0], c3[0])), c4[0]);
          if (!__any(m == SENT)) break;
          __builtin_amdgcn_s_sleep(1);
        }
        a[j % 3][0] = c0; a[j % 3][1] = c1; a[j % 3][2] = c2; a[j % 3][3] = c3; a[j % 3][4] = c4;
        NOPS(2);
      }
    };
    WAITV(13);
    BAR();
#pragma unroll
    for (int t = 0; t < 5; ++t) rda(2, t);
    SB();
#pragma unroll
    for (int j = 1; j < 16; ++j) {
      if (j == 4) WAITV(8); else if (j == 8) WAITV(3); else if (j == 12) WAITV(0);
      if (j == 4 || j == 8 || j == 12) BAR();
      SB();
#pragma unroll
      for (int t = 0; t < 5; ++t) { mWA(acc[t], WO[j], a[j % 3][t]); if (j + 2 < 16) rda(j + 2, t); SB(); }
      if (j + 1 < 16) check(j + 1);
      SB();
    }
    STAMP(5 + 8 * L);
    FRESH(3)
    const unsigned xb = WS_XB + L * BUFB + tile3 * TILEB;
    const int row3 = wv3 < 2 ? wv3 * 24 + min(l3, 23) : 16 + wv3 * 16 + min(l3, 15);
    const bool rowok = l3 < (wv3 < 2 ? 24 : 16);
    unsigned rw_ = L_RED2 + wv3 * 2048 + l3 * 16, rr_ = L_RED2 + row3 * 16, rc_ = L_MISC + row3 * 4;
    OPQ(rw_); OPQ(rr_); OPQ(rc_);
    {
      u2 spa[4]; float spf[4];
#pragma unroll
      for (int t = 0; t < 4; ++t) { spa[t] = lr3 == 4 * t ? (u2){ONE2, ONE2} : lr3 == 4 * t + 1 ? wr2 : (u2){0, 0}; spf[t] = lr3 == 4 * t + 2 ? 1.f : 0.f; }
      SB();
      float q2[5];
      f4 da, db, ea, eb;
#pragma unroll
      for (int t = 0; t < 5; ++t) {
        xp[t] = pk4(acc[t]);
        const f2 h = (f2){acc[t][0], acc[t][1]} * (f2){acc[t][0], acc[t][1]} + (f2){acc[t][2], acc[t][3]} * (f2){acc[t][2], acc[t][3]};
        q2[t] = h[0] + h[1];
        SB();
        if (t == 1) { m16<true>(da, spa[0], xp[0]); mf32<true>(db, spf[0], q2[0]); }
        else if (t > 1) { m16(da, spa[t - 1], xp[t - 1]); mf32(db, spf[t - 1], q2[t - 1]); }
        SB();
      }
      m16<true>(ea, spa[0], xp[4]); mf32<true>(eb, spf[0], q2[4]);
      SB();
      NOPS(8);
      SB();
      KEEP(da); KEEP(db);
      lds_w<f4>(rw_, (f4){da[0], da[1], db[2], 0.f});
      SB();
      KEEP(ea); KEEP(eb);
      lds_w<f4>(rw_ + 1024, (f4){ea[0], ea[1], eb[2], 0.f});
    }
    WAITL();
    BAR();
    {
      u4 w = asu((lds_r<f4>(rr_) + lds_r<f4>(rr_ + 2048)) + (lds_r<f4>(rr_ + 4096) + lds_r<f4>(rr_ + 6144)));
      w[3] = tag;
      st16(ws, w, rowok ? row3 * 16 : OOB, WS_ST + L * STB + (tile3 * 8 + hg3) * 1280);
      STAMP(6 + 8 * L);
      const unsigned go = (((g3 & 1) * 32 + 4 * hg3 + wv3) * 2 + (g3 >> 1)) * 256 + lr3 * 16;
#pragma unroll
      for (int i = 0; i < 3; ++i) {
        u2 A = xp[2 * i], B = xp[i < 2 ? 2 * i + 1 : 4];
        auto s0 = __builtin_amdgcn_permlane16_swap(A[0], B[0], false, false); auto s1 = __builtin_amdgcn_permlane16_swap(A[1], B[1], false, false);
        st16(ws, (u4){s0[0], s1[0], s0[1], s1[1]}, (i == 2 && (g3 & 1)) ? OOB : go, xb + i * 32768);
      }
    }
    WAITV(0);
    BAR();
    if (wv3 == 0) st4(ws, tag, l3 == 0 ? 0 : OOB, WS_FX + L * 32768 + (tile3 * 8 + hg3) * 128);
    const int par3 = tile3 >= 15, ti3 = tile3 - 15 * par3, p03 = 68 * ti3;
    const bool halo = L == 0 && ti3 > 0;
    wait_flags(ws, g3 == 1 ? WS_FF + lr3 * 128 : WS_FX + L * 32768 + ((tile3 - (halo ? (lr3 >> 3) : 0)) * 8 + (l3 & 7)) * 128, 0, tag);
    STAMP(7 + 8 * L);
    {
      const unsigned so = (halo && row3 < 7) ? ((tile3 - 1) * 640 + 72 + row3) * 16 : (tile3 * 640 + row3) * 16;
      u4 v[8];
#pragma unroll
      for (int i = 0; i < 8; ++i) v[i] = ld16(ws, so, WS_ST + L * STB + i * 1280);
      if (L == 0) {
        const unsigned g1_ = l3 * 16 + 16384, g0_ = g1_ - ((halo && lr3 < 7) ? 16256 : 0);
#pragma unroll
        for (int i = 0; i < 4; ++i)
#pragma unroll
          for (int t = 0; t < 5; ++t) dma<1>(ws, t ? g1_ : g0_, xb - 16384 + wv3 * 1024 + t * 16384 + i * 4096, wv3 * 1024, t * 16384 + i * 4096);
      }
      const f4 st = ((asf(v[0]) + asf(v[1])) + (asf(v[2]) + asf(v[3]))) + ((asf(v[4]) + asf(v[5])) + (asf(v[6]) + asf(v[7])));
      const float m = st[0] * (1.f / 512), var = fmx(st[2] * (1.f / 512) - m * m, 0.f), rs = rsq(var + EPS), a1 = 1.f + rs;
      const float c = a1 * rsq(var * a1 * a1 + EPS);
      lds_w<float>(rc_ + O_RC, c); lds_w<float>(rc_ + O_RM, c * m); lds_w<float>(rc_ + O_RA, a1); lds_w<float>(rc_ + O_RB, -m * rs);
    }
    FRESH(4)
    unsigned q1 = L_MISC + lr4 * 4, q4 = L_MISC + g4 * 16;
    OPQ(q1); OPQ(q4);
    if (L == 0) {
      unsigned zl = l4 * 16, zh = zl + 65536;
      OPQ(zh);
      WAITC(0, 0);
      BAR();
      STAMP(26);
      {
        u4 z[2][5];
#pragma unroll
        for (int t = 0; t < 5; ++t) z[0][t] = lds_r<u4>(t < 4 ? zl + t * 16384 : zh);
        SB();
#pragma unroll
        for (int s = 0; s < 16; ++s) {
#pragma unroll
          for (int t = 0; t < 5; ++t) {
            if (s == 0) mWA<true>(aq[t], W[s][0], z[s & 1][t]); else mWA(aq[t], W[s][0], z[s & 1][t]);
            if (s < 15) z[(s + 1) & 1][t] = lds_r<u4>((t < 4 ? zl + t * 16384 : zh) + (s + 1) * 1024); else rowc_read(R, q1, q4, 3 * t);
            SB();
            if (s == 0) mWA<true>(ak[t], W[s][1], z[s & 1][t]); else mWA(ak[t], W[s][1], z[s & 1][t]);
            if (s == 15) rowc_read(R, q1, q4, 3 * t + 1);
            SB();
            if (s == 0) mAW<true>(av[t], z[s & 1][t], W[s][2]); else mAW(av[t], z[s & 1][t], W[s][2]);
            if (s == 15) rowc_read(R, q1, q4, 3 * t + 2);
            SB();
          }
        }
      }
      STAMP(8);
      const u4 sel = wait_rec(ws, (l4 & 1) * 16, WS_SEL + (b & 15) * 128, tag);
      STAMP(9);
#pragma unroll 1
      for (int i = 0; i < 2; ++i) {
        const unsigned key = rdl(sel[0], i);
        const int tok = 2047 - (key & 2047), u = (tok >> 1) - p03;
        if ((tok & 1) != par3 || u < 0 || u >= 75) continue;
        FRESH(5)
        const float s_m = __uint_as_float(rdl(sel[1], i)), s_rs = __uint_as_float(rdl(sel[2], i));
        const float sg = sigm(key_score(key)), a1 = 1.f + s_rs, b1 = -s_m * s_rs;
        const int ts = u / 15, rs_ = u - 15 * ts;
        unsigned yb = L_RING + l5 * 32, rb = L_RING + 8192 + wv5 * 1024, zb = rb + g5 * 16;
        OPQ(yb); OPQ(zb);
        const u4 xr = lds_r<u4>((ts * 16 + (l5 >> 2)) * 1024 + (l5 & 3) * 256 + rs_ * 16);
        const f4 x0 = up4((u2){xr[0], xr[1]}) * a1 + b1, x1 = up4((u2){xr[2], xr[3]}) * a1 + b1;
        const unsigned me = lmask(lr5 == rs_);
        BAR();
        u4 y[8];
        for (int sp = 0; sp < SPIN_CAP; ++sp) {
          CFENCE();
          unsigned m = 0;
#pragma unroll
          for (int j = 0; j < 8; ++j) { y[j] = ld16(ws, l5 * 32 + (j & 1) * 16, WS_YP + (4 * wv5 + (j >> 1)) * 4096 + i * 2048); m = max(m, y[j][0]); }
          if (!__any(m == SENT)) break;
          __builtin_amdgcn_s_sleep(1);
        }
        STAMP(27);
        lds_w<f4>(yb + wv5 * 2048, (asf(y[0]) + asf(y[2])) + (asf(y[4]) + asf(y[6])));
        lds_w<f4>(yb + wv5 * 2048 + 16, (asf(y[1]) + asf(y[3])) + (asf(y[5]) + asf(y[7])));
        WAITL();
        BAR();
        const f4 y0 = (lds_r<f4>(yb) + lds_r<f4>(yb + 2048)) + (lds_r<f4>(yb + 4096) + lds_r<f4>(yb + 6144));
        const f4 y1 = (lds_r<f4>(yb + 16) + lds_r<f4>(yb + 2048 + 16)) + (lds_r<f4>(yb + 4096 + 16) + lds_r<f4>(yb + 6144 + 16));
        const u2 z0 = pk4(y0 * sg + x0), z1 = pk4(y1 * sg + x1);
        lds_w<u4>(rb + l5 * 16, (u4){z0[0], z0[1], z1[0], z1[1]});
        f4 tq, tk, tv;
        u4 z[16];
#pragma unroll
        for (int s = 0; s < 4; ++s) z[s] = lds_r<u4>(zb + s * 64);
        const u2 zx = lds_r<u2>(rb + (64 * hg5 + 16 * wv5 + 4 * g5) * 2);
        SB();
#pragma unroll
        for (int s = 0; s < 16; ++s) {
          if (s == 0) mWA<true>(tq, W[s][0], z[s]); else mWA(tq, W[s][0], z[s]);
          if (s + 4 < 16) z[s + 4] = lds_r<u4>(zb + (s + 4) * 64);
          SB();
          if (s == 0) { mWA<true>(tk, W[s][1], z[s]); mAW<true>(tv, z[s], W[s][2]); } else { mWA(tk, W[s][1], z[s]); mAW(tv, z[s], W[s][2]); }
          SB();
        }
        const f4 f0 = up4(z0), f1 = up4(z1), fs = f0 + f1, fq = f0 * f0 + f1 * f1;
        const float m2 = wave_sum((fs[0] + fs[1]) + (fs[2] + fs[3])) * (1.f / 512), q2 = wave_sum((fq[0] + fq[1]) + (fq[2] + fq[3])) * (1.f / 512);
        const float r2 = rsq(fmx(q2 - m2 * m2, 0.f) + EPS), rm2 = r2 * m2;
        SB();
        if (l5 == 0) { const unsigned ra = L_MISC + (ts * 16 + rs_) * 4; lds_w<float>(ra + O_RA, 1.f); lds_w<float>(ra + O_RB, 0.f); }
#pragma unroll
        for (int t = 0; t < 5; ++t)
          if (t == ts) {
            aq[t] = bsel4(me, tq, aq[t]); ak[t] = bsel4(me, tk, ak[t]);
            xp[t] = (u2){bsel(me, zx[0], xp[t][0]), bsel(me, zx[1], xp[t][1])};
            R.c[t] = bself(me, r2, R.c[t]); R.cm[t] = bself(me, rm2, R.cm[t]);
#pragma unroll
            for (int e = 0; e < 4; ++e) {
              const bool mine = 4 * g5 + e == rs_;
              av[t][e] = mine ? tv[e] : av[t][e]; R.c4[t][e] = mine ? r2 : R.c4[t][e]; R.cm4[t][e] = mine ? rm2 : R.cm4[t][e];
            }
          }
      }
      WAITL();
      BAR();
      STAMP(10);
    } else {
      const rsrc_t ro = mk(P.out, 2048 * 1024);
      WAITL();
      BAR();
#pragma unroll
      for (int t = 0; t < 5; ++t) {
        const float ra = lds_r<float>(q1 + O_RA + t * 64), rb = lds_r<float>(q1 + O_RB + t * 64);
        lds_w<u2>((16 * t + lr4) * 144 + wv4 * 32 + g4 * 8, pk4(up4(xp[t]) * ra + rb));
      }
      WAITL();
      BAR();
#pragma unroll
      for (int i = 0; i < 3; ++i) {
        const int row = min((i * 4 + wv4) * 8 + (l4 >> 3), 79), u = row - (row >> 4), p = p03 + u;
        const bool ok = (i * 4 + wv4) < 10 && (row & 15) != 15 && p < 1024 && (ti3 == 0 || u >= 7);
        st16(ro, lds_r<u4>(row * 144 + (l4 & 7) * 16), ok ? (2 * p + par3) * 1024 + hg4 * 128 + (l4 & 7) * 16 : OOB, 0);
      }
      STAMP(16);
      WAITV(0);
      BAR();
      if (wv4 < 2) {
        const u4 sel = wait_rec(ws, wv4 * 16, WS_SEL + 2048 + (b & 15) * 128, tag);
        STAMP(17);
        const unsigned key = rfl(sel[0]);
        const int tok = 2047 - (key & 2047), u = (tok >> 1) - p03;
        if ((tok & 1) == par3 && u >= (ti3 ? 7 : 0) && u < 75) {
          const f4 base = up4(lds_r<u2>((u + u / 15) * 144 + lr4 * 8));
          const float sg = sigm(key_score(key));
          u4 y[4];
          for (int sp = 0; sp < SPIN_CAP; ++sp) {
            CFENCE();
            unsigned m = 0;
#pragma unroll
            for (int j = 0; j < 4; ++j) { y[j] = ld16(ws, g4 * 4096 + lr4 * 16, WS_YP + 65536 + j * 16384 + wv4 * 2048 + hg4 * 256); m = max(m, y[j][0]); }
            if (!__any(m == SENT)) break;
            __builtin_amdgcn_s_sleep(1);
          }
          const f4 s = (asf(y[0]) + asf(y[1])) + (asf(y[2]) + asf(y[3]));
          const f2 x = swap16(s[0], s[1]), w = swap16(s[2], s[3]), v = swap32(x[0] + x[1], w[0] + w[1]);
          const float e = v[0] + v[1];
          const f2 v2 = swap32(e, e), e01 = swap16(v2[0], v2[0]), e23 = swap16(v2[1], v2[1]);
          __builtin_amdgcn_raw_buffer_store_b64(pk4((f4){e01[0], e01[1], e23[0], e23[1]} * sg + base), ro, l4 < 16 ? tok * 1024 + hg4 * 128 + lr4 * 8 : OOB, 0, 16);
        }
        STAMP(18);
      }
    }
  }
  STAMP_FLUSH();
}

DEV void role_f(const Params& P, const int j, const int wv, const int l) {
  const int lr = l & 15, g = l >> 4, tid = wv * 64 + l;
  const rsrc_t ws = mk(P.ws, WS_USED);
  const u64 t0 = __builtin_amdgcn_s_memrealtime();
  STAMP_INIT();
  STAMP(0);
  const unsigned tag = tag_finish(tag_issue(P.ws));
  const u4 sent = {SENT, SENT, SENT, SENT};
  st16(ws, sent, j * 4096 + tid * 16, WS_YP); st16(ws, sent, j * 4096 + tid * 16, WS_YP + 65536);
  float wsum;
  { u4 w = *(const u4*)(P.wr + l * 8); f4 a = up4((u2){w[0], w[1]}) + up4((u2){w[2], w[3]}); wsum = wave_sum((a[0] + a[1]) + (a[2] + a[3])); }
  WAITV(0);
  BAR();
  if (wv == 0) st4(ws, tag, l == 0 ? 0 : OOB, WS_FF + j * 128);
  for (int sp = 0; sp < 4096 && __builtin_amdgcn_s_memrealtime() - t0 < FDELAY; ++sp) __builtin_amdgcn_s_sleep(4);
  const int noct = j < 12 ? 11 : 10, f0 = 8 * (j < 12 ? 11 * j : 132 + 10 * (j - 12));
  u4 G[16][3], Dn[3][8];
  {
    const rsrc_t rgu = mk(wv < 2 ? P.wg : P.wu, 512 * 2752), rd = mk(P.wd, 1376 * 1024);
    unsigned go[6];
#pragma unroll
    for (int i = 0; i < 6; ++i) { const int n = 64 * ((wv & 1) * 6 + i) + l, row = n / 12; go[i] = row * 2752 + f0 * 2 + (n - 12 * row) * 16; }
    const unsigned dof = (f0 + g) * 1024 + wv * 256 + lr * 16;
    auto issue = [&](int rd_) {
      if (rd_ < 8) {
#pragma unroll
        for (int i = 0; i < 6; ++i) dma<0>(rgu, go[i], rd_ * 64 * 2752, wv * 6144, (rd_ & 3) * 32768 + i * 1024);
      } else {
#pragma unroll
        for (int i = 0; i < 8; ++i) dma<0>(rd, dof, (32 * (rd_ - 8) + 4 * i) * 1024, wv * 8192, (rd_ & 3) * 32768 + i * 1024);
      }
    };
    unsigned tg = ((l >> 1) & 1) * 12288 + (8 * g + (lr >> 2)) * 192 + wv * 48 + (l & 1) * 8, td = wv * 8192 + (8 * g + (lr >> 2)) * 256 + (l & 3) * 8;
    issue(0); issue(1); issue(2);
#pragma unroll
    for (int rd_ = 0; rd_ < 11; ++rd_) {
      if (rd_ < 6) WAITC(12, 0); else if (rd_ == 6) WAITC(14, 0); else if (rd_ < 9) WAITC(16, 0); else if (rd_ == 9) WAITC(8, 0); else WAITC(0, 0);
      BAR();
      if (rd_ + 3 < 11) issue(rd_ + 3);
      unsigned bg = tg + (rd_ & 3) * 32768, bd = td + (rd_ & 3) * 32768;
      OPQ(bg); OPQ(bd);
      if (rd_ < 8) {
#pragma unroll
        for (int ks = 0; ks < 2; ++ks)
#pragma unroll
          for (int c = 0; c < 3; ++c) G[2 * rd_ + ks][c] = trfrag2(bg + ks * 32 * 192 + c * 16, 4 * 192);
      } else {
#pragma unroll
        for (int c = 0; c < 8; ++c) Dn[rd_ - 8][c] = trfrag2(bd + c * 32, 4 * 256);
      }
    }
    WAITL();
    BAR();
  }
  constexpr unsigned F_ACT = 0;
  const unsigned mg1 = lmask(g == 1), mg2 = lmask(g >= 2), mlo = lmask(lr < 8);
  auto gather = [&](const f4& a0, const f4& a1, const f4& a2, float& gv, float& uv) {
    KEEP(a0); KEEP(a1); KEEP(a2);
    const float d0 = bself(mg2, a2[0], bself(mg1, a1[0], a0[0])), d1 = bself(mg2, a2[1], bself(mg1, a1[1], a0[1]));
    gv = __uint_as_float(__builtin_amdgcn_update_dpp(__float_as_uint(d0), __float_as_uint(d1), 0x128, 0xF, 0xC, false));
    uv = __uint_as_float(__builtin_amdgcn_update_dpp(__float_as_uint(d1), __float_as_uint(d0), 0x128, 0xF, 0x3, false));
  };
  float csg, csu;
  {
    u4 one = {ONE2, ONE2, ONE2, ONE2};
    OPQ(one);
    NOPS(2);
    SB();
    f4 a[3];
#pragma unroll
    for (int ks = 0; ks < 16; ++ks)
#pragma unroll
      for (int c = 0; c < 3; ++c) { if (ks == 0) mAW<true>(a[c], one, G[ks][c]); else mAW(a[c], one, G[ks][c]); }
    SB();
    NOPS(8);
    SB();
    gather(a[0], a[1], a[2], csg, csu);
  }
  const bool live = g < 3 && 3 * wv + g < noct;
  const unsigned actw = F_ACT + (g < 3 ? (lr >> 3) * 192 + (24 * wv + 8 * g + (l & 7)) * 2 : 512 + l * 2);
  STAMP(1);
  const int tok = 32 * (4 * j + wv) + (l & 31);
  unsigned sto;
  { const int p = tok >> 1, ti = max(p - 7, 0) / 68, u = p - 68 * ti; sto = ((((tok & 1) * 15 + ti) * 8 + 4 * (l >> 5)) * 80 + u + u / 15) * 16; }

#pragma unroll 1
  for (int L = 0; L < 2; ++L) {
    u4 v[4];
    for (int sp = 0; sp < SPIN_CAP; ++sp) {
      CFENCE();
#pragma unroll
      for (int i = 0; i < 4; ++i) v[i] = ld16(ws, sto, WS_ST + L * STB + i * 1280);
      if (__all(v[0][3] == tag && v[1][3] == tag && v[2][3] == tag && v[3][3] == tag)) break;
      __builtin_amdgcn_s_sleep(1);
    }
    STAMP(2 + 8 * L);
    {
      const f4 s = (asf(v[0]) + asf(v[1])) + (asf(v[2]) + asf(v[3]));
      const float m = xadd32(s[0]) * (1.f / 512), dt = xadd32(s[1]), qq = xadd32(s[2]) * (1.f / 512);
      const float rs = rsq(fmx(qq - m * m, 0.f) + EPS), sc = (dt - m * wsum) * rs;
      unsigned u = __float_as_uint(sc); u ^= (unsigned)((int)u >> 31) | 0x80000000u;
      const unsigned key = (u & 0xFFFFF800u) | (2047u - tok);
      const unsigned k1 = max_x16(max_row16(key)), k2 = max_x16(max_row16(key == k1 ? 0u : key));
      st16(ws, (u4){key, __float_as_uint(m), __float_as_uint(rs), tag}, l >= 32 ? OOB : key == k1 ? 0 : key == k2 ? 16 : OOB, WS_CAND + L * 8192 + (4 * j + wv) * 128);
    }
    STAMP(3 + 8 * L);
    u4 c0, c1;
    for (int sp = 0; sp < SPIN_CAP; ++sp) {
      CFENCE();
      c0 = ld16(ws, l * 128, WS_CAND + L * 8192); c1 = ld16(ws, l * 128 + 16, WS_CAND + L * 8192);
      const unsigned f = ld4(ws, (wv * 60 + min(l, 59)) * 128, WS_FX + L * 32768);
      if (__all(c0[3] == tag && c1[3] == tag && f == tag)) break;
      __builtin_amdgcn_s_sleep(1);
    }
    BAR();
    STAMP(4 + 8 * L);
    const unsigned k1 = rfl(max_x32(max_x16(max_row16(c0[0]))));
    const bool w1 = c0[0] == k1;
    const u4 cb = w1 ? c1 : c0;
    const unsigned k2 = rfl(max_x32(max_x16(max_row16(cb[0]))));
    u4 h[16];
    {
      const int t = 2047 - (((l & 1) ? k2 : k1) & 2047), p = t >> 1, ti = max(p - 7, 0) / 68, u = p - 68 * ti, ts = u / 15;
      const unsigned vo = ((t & 1) * 15 + ti) * TILEB + ts * 16384 + g * 256 + (u - 15 * ts) * 16;
#pragma unroll
      for (int ks = 0; ks < 16; ++ks) h[ks] = ld16(ws, vo + (ks & 3) * 1024, WS_XB + L * BUFB + (ks >> 2) * 4096);
    }
    SB();
    const int s1 = __builtin_ctzll(__ballot(w1) | (1ull << 63)), s2 = __builtin_ctzll(__ballot(cb[0] == k2) | (1ull << 63));
    const unsigned ma = rdl(c0[1], s1), ra = rdl(c0[2], s1), mb = rdl(cb[1], s2), rb = rdl(cb[2], s2);
    if (wv == 0) st16(ws, (l & 1) ? (u4){k2, mb, rb, tag} : (u4){k1, ma, ra, tag}, l < 2 ? l * 16 : OOB, WS_SEL + L * 2048 + j * 128);
    const float m = __uint_as_float(bsel(mlo, ma, mb)), rs = __uint_as_float(bsel(mlo, ra, rb));
    SB();
    {
      f4 a[3];
#pragma unroll
      for (int ks = 0; ks < 16; ++ks)
#pragma unroll
        for (int c = 0; c < 3; ++c) { if (ks == 0) mAW<true>(a[c], h[ks], G[ks][c]); else mAW(a[c], h[ks], G[ks][c]); }
      SB();
      NOPS(8);
      SB();
      float gg, uu;
      gather(a[0], a[1], a[2], gg, uu);
      gg = (gg - m * csg) * rs; uu = (uu - m * csu) * rs;
      const float act = live ? gg * sigm(gg) * uu : 0.f;
      lds_w<u16>(actw, (u16)pk(act, act));
    }
    STAMP(5 + 8 * L);
    WAITL();
    BAR();
    {
      u4 b[3];
#pragma unroll
      for (int ks = 0; ks < 3; ++ks) b[ks] = lds_r<u4>(F_ACT + (l & 1) * 192 + ks * 64 + g * 16);
      const unsigned m2 = lmask(l & 2);
      const unsigned vo = lr < 4 ? j * 4096 + (l & 1) * 2048 + wv * 512 + (l & 2) * 32 + g * 16 : OOB;
      f4 y[8];
      SB();
#pragma unroll
      for (int c = 0; c < 8; ++c) { mWA<true>(y[c], Dn[0][c], b[0]); mWA(y[c], Dn[1][c], b[1]); mVV(y[c], Dn[2][c], b[2]); }
      SB();
#pragma unroll
      for (int c = 0; c < 4; ++c) {
        if (c == 0) { NOPS(1); SB(); }
        u4 w = asu(bsel4(m2, y[2 * c + 1], y[2 * c]));
        w[0] = umin(w[0], SENT - 1);
        st16(ws, w, vo, WS_YP + L * 65536 + c * 128);
        SB();
      }
    }
    STAMP(6 + 8 * L);
  }
  if (j == 0 && wv == 0) st4(ws, tag, l == 0 ? 0 : OOB, WS_EPOCH);
  STAMP_FLUSH();
}

__global__ void __launch_bounds__(256, 1) fused_kernel(Params P) {
  const int b = blockIdx.x, wv = __builtin_amdgcn_readfirstlane(threadIdx.x >> 6), l = threadIdx.x & 63;
  unsigned pfc;
  {
    u64 pc; unsigned len;
    asm volatile("s_getpc_b64 %0\n.Lpf_here:\n\ts_mov_b32 %1, code_end_marker-.Lpf_here" : "=s"(pc), "=s"(len));
    const rsrc_t rc = mk((const void*)pc, len);
    asm volatile("buffer_load_dword %0, %1, %2, 0 offen" : "=v"(pfc) : "v"(l < 3 ? (((b >> 3) * 4 + wv) * 3 + l) * 128 : OOB), "s"(rc) : "memory");
  }
  unsigned pft;
  {
    const char* p = P.ws + (l & 7) * 0x180000;
    if (l == 8) p = (const char*)P.out; if (l == 9) p = (const char*)P.out + 2048 * 1024 - 4;
    if (l == 10) p = (const char*)P.wo; if (l == 11) p = (const char*)P.wo + 512 * 1024 - 4;
    if (l == 12) p = (const char*)P.wg; if (l == 13) p = (const char*)P.wg + 512 * 2752 - 4;
    if (l == 14) p = (const char*)P.wu; if (l == 15) p = (const char*)P.wu + 512 * 2752 - 4;
    if (l == 16) p = (const char*)P.wd; if (l == 17) p = (const char*)P.wd + 512 * 2752 - 4;
    if (wv == 3) asm volatile("global_load_dword %0, %1, off" : "=v"(pft) : "v"(p) : "memory");
  }
  if (b < 240) role_a(P, b, wv, l);
  else role_f(P, b - 240, wv, l);
  asm volatile("" ::"v"(pfc));
  asm volatile("" ::"v"(pft));
}

extern "C" __device__ __attribute__((used, noinline)) void code_end_marker() {}

extern "C" void kernel_launch(void* const* d_in, const int* in_sizes, int n_in, void* d_out, int out_size, void* d_ws, size_t ws_size, hipStream_t stream) {
  Params P;
  P.x = (const u16*)d_in[0]; P.wqkv = (const u16*)d_in[1]; P.wo = (const u16*)d_in[2];
  P.wr = (const u16*)d_in[4]; P.wg = (const u16*)d_in[5]; P.wu = (const u16*)d_in[6]; P.wd = (const u16*)d_in[7];
  P.out = (u16*)d_out; P.ws = (char*)d_ws;
  (void)hipFuncSetAttribute((const void*)fused_kernel, hipFuncAttributeMaxDynamicSharedMemorySize, LDS_BYTES);
  fused_kernel<<<256, 256, LDS_BYTES, stream>>>(P);
}
```

```cpp
#define STAMPS
#define STAMPMASK 0x24000100u
#define PROBE_A 26
#define PROBE_B 8
#define PROBE_BLK 0
#define PROBE_MUL 10
#define PROBE_DIV 1
#include <hip/hip_runtime.h>
#include <hip/hip_bf16.h>
#include <stdint.h>

typedef unsigned short u16;
typedef unsigned long long u64;
typedef __bf16 bf2 __attribute__((ext_vector_type(2)));
typedef short s4 __attribute__((ext_vector_type(4)));
typedef float f4 __attribute__((ext_vector_type(4)));
typedef float f2 __attribute__((ext_vector_type(2)));
typedef unsigned u4 __attribute__((ext_vector_type(4)));
typedef unsigned u2 __attribute__((ext_vector_type(2)));
typedef __amdgpu_buffer_rsrc_t rsrc_t;
#define LAS __attribute__((address_space(3)))
#define DEV __device__ __forceinline__

constexpr unsigned TILEB = 81920;
constexpr unsigned BUFB = 30 * TILEB;
constexpr unsigned WS_EPOCH = 0;
constexpr unsigned WS_FF = 4096;
constexpr unsigned WS_SEL = 8192;
constexpr unsigned WS_CAND = 16384;
constexpr unsigned WS_FA = 32768;
constexpr unsigned WS_FX = 65536;
constexpr unsigned WS_YP = 131072;
constexpr unsigned WS_ST = 262144;
constexpr unsigned STB = 30 * 8 * 80 * 16;
constexpr unsigned WS_OB = 1048576;
constexpr unsigned WS_XB = WS_OB + 2 * BUFB;
constexpr unsigned WS_USED = WS_XB + 2 * BUFB;
constexpr unsigned WS_STAMP = 16u << 20;
constexpr unsigned L_RING = 81920;
constexpr unsigned L_RED2 = L_RING + 16384;
constexpr unsigned L_MISC = 155648;
constexpr unsigned O_RED = 0;
constexpr unsigned O_RC = 4096, O_RM = O_RC + 320, O_RA = O_RM + 320, O_RB = O_RA + 320;
constexpr unsigned LDS_BYTES = 163840;
constexpr unsigned SENT = 0xFFFFFFFFu, OOB = 0x80000000u, ONE2 = 0x3F803F80u;
#define FDELAY 200
constexpr int SPIN_CAP = 1 << 15;
constexpr float EPS = 1e-5f;

#define CFENCE() asm volatile("" ::: "memory")
#define WAITC(vm, lg) do { CFENCE(); __builtin_amdgcn_s_waitcnt(((vm) & 15) | 0x70 | ((lg) << 8) | (((vm) >> 4) << 14)); CFENCE(); } while (0)
#define WAITV(n) asm volatile("s_waitcnt vmcnt(%0)" ::"i"(n) : "memory")
#define WAITL() WAITC(63, 0)
#define BAR() do { CFENCE(); __builtin_amdgcn_s_barrier(); CFENCE(); } while (0)
#define SB() __builtin_amdgcn_sched_barrier(0)
#define OPQ(x) asm volatile("" : "+v"(x))
#define KEEP(x) asm volatile("" ::"v"(x))

struct Params {
  const u16* x; const u16* wqkv; const u16* wo; const u16* wr; const u16* wg; const u16* wu; const u16* wd;
  u16* out; char* ws;
};

DEV rsrc_t mk(const void* p, unsigned bytes) { return __builtin_amdgcn_make_buffer_rsrc((void*)p, 0, bytes, 0x00020000); }
DEV void setm0(unsigned base, int imm) { asm volatile("s_add_u32 m0, %0, %1" ::"s"(base), "i"(imm) : "scc", "memory"); }
template <int SC> DEV void dma_go(rsrc_t rs, unsigned voff, unsigned soff) {
  if (SC) asm volatile("buffer_load_dwordx4 %0, %1, %2 offen sc1 lds" ::"v"(voff), "s"(rs), "s"(soff) : "memory");
  else asm volatile("buffer_load_dwordx4 %0, %1, %2 offen lds" ::"v"(voff), "s"(rs), "s"(soff) : "memory");
}
DEV void dma_go_m(rsrc_t rs, unsigned voff, unsigned soff, u64 mask) {
  asm volatile("s_mov_b64 exec, %3\n\tbuffer_load_dwordx4 %0, %1, %2 offen lds\n\ts_mov_b64 exec, -1" ::"v"(voff), "s"(rs), "s"(soff), "s"(mask) : "memory");
}
template <int SC> DEV void dma(rsrc_t rs, unsigned voff, unsigned soff, unsigned base, int imm) {
  if (SC) asm volatile("s_add_u32 m0, %3, %4\n\ts_nop 0\n\tbuffer_load_dwordx4 %0, %1, %2 offen sc1 lds" ::"v"(voff), "s"(rs), "s"(soff), "s"(base), "i"(imm) : "scc", "memory");
  else asm volatile("s_add_u32 m0, %3, %4\n\ts_nop 0\n\tbuffer_load_dwordx4 %0, %1, %2 offen lds" ::"v"(voff), "s"(rs), "s"(soff), "s"(base), "i"(imm) : "scc", "memory");
}
DEV void st16(rsrc_t rs, u4 v, unsigned voff, unsigned soff) {
  asm volatile("buffer_store_dwordx4 %0, %1, %2, %3 offen sc1\n\ts_nop 1" ::"v"(v), "v"(voff), "s"(rs), "s"(soff) : "memory");
}
DEV void st4(rsrc_t rs, unsigned v, unsigned voff, unsigned soff) {
  asm volatile("buffer_store_dword %0, %1, %2, %3 offen sc1" ::"v"(v), "v"(voff), "s"(rs), "s"(soff) : "memory");
}
DEV u4 ld16(rsrc_t rs, unsigned voff, unsigned soff) { return __builtin_amdgcn_raw_buffer_load_b128(rs, voff, soff, 16); }
DEV unsigned ld4(rsrc_t rs, unsigned voff, unsigned soff) { return __builtin_amdgcn_raw_buffer_load_b32(rs, voff, soff, 16); }

template <bool Z = false> DEV void mWA(f4& c, u4 w, u4 a) {
  if (Z) asm volatile("v_mfma_f32_16x16x32_bf16 %0, %1, %2, 0" : "=v"(c) : "a"(w), "v"(a));
  else asm volatile("v_mfma_f32_16x16x32_bf16 %0, %1, %2, %0" : "+v"(c) : "a"(w), "v"(a));
}
template <bool Z = false> DEV void mAW(f4& c, u4 a, u4 w) {
  if (Z) asm volatile("v_mfma_f32_16x16x32_bf16 %0, %1, %2, 0" : "=v"(c) : "v"(a), "a"(w));
  else asm volatile("v_mfma_f32_16x16x32_bf16 %0, %1, %2, %0" : "+v"(c) : "v"(a), "a"(w));
}
template <bool Z = false> DEV void mVV(f4& c, u4 a, u4 b) {
  if (Z) asm volatile("v_mfma_f32_16x16x32_bf16 %0, %1, %2, 0" : "=v"(c) : "v"(a), "v"(b));
  else asm volatile("v_mfma_f32_16x16x32_bf16 %0, %1, %2, %0" : "+v"(c) : "v"(a), "v"(b));
}
template <bool Z = false> DEV void m16(f4& c, u2 a, u2 b) {
  if (Z) asm volatile("v_mfma_f32_16x16x16_bf16 %0, %1, %2, 0" : "=v"(c) : "v"(a), "v"(b));
  else asm volatile("v_mfma_f32_16x16x16_bf16 %0, %1, %2, %0" : "+v"(c) : "v"(a), "v"(b));
}
DEV f4 m16c(u2 a, u2 b, f4 c) { f4 d; asm volatile("v_mfma_f32_16x16x16_bf16 %0, %1, %2, %3" : "=&v"(d) : "v"(a), "v"(b), "v"(c)); return d; }
template <bool Z = false> DEV void mf32(f4& c, float a, float b) {
  if (Z) asm volatile("v_mfma_f32_16x16x4_f32 %0, %1, %2, 0" : "=&v"(c) : "v"(a), "v"(b));
  else asm volatile("v_mfma_f32_16x16x4_f32 %0, %1, %2, %0" : "+v"(c) : "v"(a), "v"(b));
}
#define NOPS(n) asm volatile("s_nop %0" ::"i"((n) - 1))
DEV unsigned pk(float a, float b) { f2 v = {a, b}; return __builtin_bit_cast(unsigned, __builtin_convertvector(v, bf2)); }
DEV u2 pk4(f4 v) { return (u2){pk(v[0], v[1]), pk(v[2], v[3])}; }
DEV float lo(unsigned u) { return __uint_as_float(u << 16); }
DEV float hi(unsigned u) { return __uint_as_float(u & 0xFFFF0000u); }
DEV f4 up4(u2 u) { return (f4){lo(u[0]), hi(u[0]), lo(u[1]), hi(u[1])}; }
DEV f4 asf(u4 v) { return __builtin_bit_cast(f4, v); }
DEV u4 asu(f4 v) { return __builtin_bit_cast(u4, v); }
DEV unsigned bsel(unsigned m, unsigned a, unsigned b) { return __builtin_amdgcn_bitop3_b32(m, a, b, 0xCA); }
DEV float bself(unsigned m, float a, float b) { return __uint_as_float(bsel(m, __float_as_uint(a), __float_as_uint(b))); }
DEV f4 bsel4(unsigned m, f4 a, f4 b) { return (f4){bself(m, a[0], b[0]), bself(m, a[1], b[1]), bself(m, a[2], b[2]), bself(m, a[3], b[3])}; }
DEV unsigned lmask(bool c) { unsigned m = c ? ~0u : 0u; OPQ(m); return m; }
DEV float fmx(float a, float b) { return __builtin_elementwise_maximum(a, b); }
template <class T> DEV T lds_r(unsigned a) { return *(const LAS T*)(size_t)a; }
template <class T> DEV void lds_w(unsigned a, T v) { *(LAS T*)(size_t)a = v; }
DEV u4 trfrag2(unsigned a, unsigned step) {
  u2 x = __builtin_bit_cast(u2, __builtin_amdgcn_ds_read_tr16_b64_v4i16((LAS s4*)(size_t)a));
  u2 y = __builtin_bit_cast(u2, __builtin_amdgcn_ds_read_tr16_b64_v4i16((LAS s4*)(size_t)(a + step)));
  return (u4){x[0], x[1], y[0], y[1]};
}
DEV f2 swap16(float a, float b) { auto r = __builtin_amdgcn_permlane16_swap(__float_as_uint(a), __float_as_uint(b), false, false); return (f2){__uint_as_float(r[0]), __uint_as_float(r[1])}; }
DEV f2 swap32(float a, float b) { auto r = __builtin_amdgcn_permlane32_swap(__float_as_uint(a), __float_as_uint(b), false, false); return (f2){__uint_as_float(r[0]), __uint_as_float(r[1])}; }
DEV float xadd16(float v) { f2 r = swap16(v, v); return r[0] + r[1]; }
DEV float xadd32(float v) { f2 r = swap32(v, v); return r[0] + r[1]; }
template <int C> DEV unsigned dpp(unsigned v) { return __builtin_amdgcn_update_dpp(0u, v, C, 0xF, 0xF, true); }
template <int C> DEV float dppf(float v) { return __uint_as_float(dpp<C>(__float_as_uint(v))); }
DEV float wave_sum(float v) {
  v += dppf<0xB1>(v); v += dppf<0x4E>(v); v += dppf<0x141>(v); v += dppf<0x140>(v);
  return xadd32(xadd16(v));
}
DEV unsigned max_row16(unsigned v) { v = max(v, dpp<0xB1>(v)); v = max(v, dpp<0x4E>(v)); v = max(v, dpp<0x141>(v)); return max(v, dpp<0x140>(v)); }
DEV unsigned max_x16(unsigned v) { auto r = __builtin_amdgcn_permlane16_swap(v, v, false, false); return max(r[0], r[1]); }
DEV unsigned max_x32(unsigned v) { auto r = __builtin_amdgcn_permlane32_swap(v, v, false, false); return max(r[0], r[1]); }
DEV unsigned umin(unsigned a, unsigned b) { return a < b ? a : b; }
DEV unsigned rfl(unsigned v) { return __builtin_amdgcn_readfirstlane(v); }
DEV unsigned rdl(unsigned v, int l) { return __builtin_amdgcn_readlane(v, l); }
DEV float rsq(float v) { return __builtin_amdgcn_rsqf(v); }
DEV float sigm(float v) { return __builtin_amdgcn_rcpf(1.f + __builtin_amdgcn_exp2f(-1.44269504f * v)); }
DEV float key_score(unsigned k) { unsigned u = k & 0xFFFFF800u; return __uint_as_float((u & 0x80000000u) ? (u ^ 0x80000000u) : ~u); }

#define STAMP(i) do { if (__builtin_constant_p(i) && !((STAMPMASK >> (i)) & 1)) break; u64 a_, b_; asm volatile("s_memrealtime %0\n\ts_memtime %1\n\ts_waitcnt lgkmcnt(0)" : "=&s"(a_), "=&s"(b_) :: "memory"); \
    asm volatile("s_mov_b32 m0, %3\n\ts_nop 0\n\tv_writelane_b32 %0, %1, m0\n\ts_add_u32 m0, m0, 32\n\ts_nop 0\n\tv_writelane_b32 %0, %2, m0" : "+v"(stv) : "s"((unsigned)a_), "s"((unsigned)b_), "s"(i) : "scc"); } while (0)
#define STAMP_INIT() unsigned stv = 0
#define STAMP_FLUSH() do { STAMP(29); if (blockIdx.x == PROBE_BLK && threadIdx.x < 64) { unsigned d_ = (rdl(stv, PROBE_B) - rdl(stv, PROBE_A)) * PROBE_MUL / PROBE_DIV, t_ = rdl(stv, 29); \
    for (int sp_ = 0; sp_ < (1 << 20) && (unsigned)__builtin_amdgcn_s_memrealtime() - t_ < d_; ++sp_) __builtin_amdgcn_s_sleep(2); } } while (0)
DEV unsigned tag_issue(const char* ws) { unsigned e; asm volatile("s_load_dword %0, %1, 0x0" : "=s"(e) : "s"(ws) : "memory"); return e; }
DEV unsigned tag_finish(unsigned e) { unsigned t; asm volatile("s_waitcnt lgkmcnt(0)\n\ts_add_u32 %0, %1, 1" : "=s"(t) : "s"(e) : "memory", "scc"); return t; }

DEV void wait_flags(rsrc_t ws, unsigned voff, unsigned base, unsigned tag) {
  for (int sp = 0; sp < SPIN_CAP; ++sp) {
    CFENCE();
    if (__all(ld4(ws, voff, base) == tag)) break;
    __builtin_amdgcn_s_sleep(1);
  }
}
DEV u4 wait_rec(rsrc_t ws, unsigned voff, unsigned base, unsigned tag) {
  u4 c;
  for (int sp = 0; sp < SPIN_CAP; ++sp) {
    CFENCE();
    c = ld16(ws, voff, base);
    if (__all(c[3] == tag)) break;
    __builtin_amdgcn_s_sleep(1);
  }
  return c;
}

struct RowC { float c[5], cm[5]; f4 c4[5], cm4[5]; };
DEV void rowc_read(RowC& R, unsigned r1, unsigned r4, int i) {
  const int t = i / 3;
  if (i % 3 == 0) { R.c[t] = lds_r<float>(r1 + O_RC + t * 64); R.cm[t] = lds_r<float>(r1 + O_RM + t * 64); }
  else if (i % 3 == 1) R.c4[t] = lds_r<f4>(r4 + O_RC + t * 64);
  else R.cm4[t] = lds_r<f4>(r4 + O_RM + t * 64);
}

DEV void role_a(const Params& P, const int b, const int wv, const int l) {
  const int xcd = b & 7, r = b >> 3;
  const int q = (xcd >> 1) * 30 + r, tile = q >> 2, hg = (xcd & 1) * 4 + (q & 3);
  const int lr = l & 15, g = l >> 4;
  const rsrc_t ws = mk(P.ws, WS_USED);
  const unsigned epoch = tag_issue(P.ws);
  STAMP_INIT();
  STAMP(0);
  u4 W[16][3];
  f4 aq[5], ak[5], av[5];
  u2 xp[5];
  RowC R;
  unsigned tag, pm;
  f4 mask;
  u2 wr2;
  const unsigned trb = L_RING + (8 * g + (lr >> 2)) * 128 + (((2 * wv + ((l & 3) >> 1)) ^ ((((lr >> 3) & 1) | ((g & 1) << 1)) << 1)) << 4) + 8 * (l & 1);
  {
    const int par = tile >= 15, p0 = 68 * (tile - 15 * par);
    const rsrc_t rx = mk(P.x, 2048 * 1024), rw = mk(P.wqkv, 512 * 3072), rwo = mk(P.wo, 512 * 1024);
    unsigned xoff[5];
    {
      const int rr = 4 * wv + g;
#pragma unroll
      for (int j = 0; j < 5; ++j) { int p = min(p0 + 15 * j + rr, 1023); xoff[j] = (2 * p + par) * 1024 + ((lr ^ rr) << 4); }
    }
    const u64 xmask = wv == 3 ? 0x0000FFFFFFFFFFFFull : ~0ull;
    const int k8 = l >> 3;
    const unsigned swA = (((l & 7) ^ (((k8 >> 1) & 1) << 1)) << 4) ^ ((wv & 1) << 6), swB = swA ^ 64;
    const unsigned woffA = k8 * 3072 + hg * 128 + swA, woffB = k8 * 3072 + hg * 128 + swB;
    const unsigned lx = wv * 1024, lw = L_RING + wv * 3072;
    unsigned wso[3];
#pragma unroll
    for (int j = 0; j < 3; ++j) { const int i = wv * 3 + j; wso[j] = (i & 3) * 8 * 3072 + (i >> 2) * 1024; }
    auto ldx = [&](int pr) {
#pragma unroll
      for (int j = 0; j < 5; ++j) { setm0(lx, pr * 20480 + j * 4096); dma_go_m(rx, xoff[j], pr * 256, xmask); }
    };
    auto ldw = [&](int s) {
#pragma unroll
      for (int j = 0; j < 3; ++j) dma<0>(rw, (j & 1) ? woffB : woffA, wso[j] + s * 98304, lw, (s % 6) * 12288 + j * 1024);
    };
    asm volatile("global_load_dwordx2 %0, %1, %2" : "=v"(wr2) : "v"((16 * wv + 4 * g) * 2), "s"(P.wr + 64 * hg) : "memory");
    ldx(0); ldw(0); ldw(1);
    unsigned touch;
    {
      int rh = (3 - (int)(((size_t)P.wqkv) >> 7)) & 3, idx = min(r * 52 + wv * 13 + min(l, 12), 1535);
      unsigned off = (idx / 3) * 3072 + (idx % 3) * 1024 + ((xcd & 1) * 4 + rh) * 128;
      asm volatile("buffer_load_dword %0, %1, %2, 0 offen" : "=v"(touch) : "v"(off), "s"(rw) : "memory");
    }
    ldw(2); ldx(1); ldw(3); ldw(4);
    tag = tag_finish(epoch);
    {
      const u4 one = {ONE2, ONE2, ONE2, ONE2};
      const int row = wv * 4 + g;
      lds_w<u4>((row / 5) * 20480 + ((row % 5) * 16 + 15) * 256 + lr * 16, one);
      if (wv == 0) lds_w<u4>(3 * 20480 + ((g + 1) * 16 + 15) * 256 + lr * 16, one);
    }
    const unsigned ooff = tile * TILEB + (((g & 1) * 32 + 4 * hg + wv) * 2 + (g >> 1)) * 256 + lr * 16;
    const unsigned ooff4 = (g & 1) ? OOB : ooff;
    u4 sent = {SENT, SENT, SENT, SENT};
    OPQ(sent);
    const unsigned wooffA = k8 * 1024 + hg * 128 + swA, wooffB = k8 * 1024 + hg * 128 + swB;
    unsigned xa[4];
#pragma unroll
    for (int i = 0; i < 4; ++i) xa[i] = lr * 256 + (((4 * i + g) ^ lr) << 4);
    const unsigned xg = lr * 256 + (((4 * wv + g) ^ lr) << 4);
    const unsigned xr = (hg >> 1) * 20480 + lr * 256 + ((((hg & 1) * 8 + 2 * wv + (g >> 1)) ^ lr) << 4) + 8 * (g & 1);
    unsigned mq = L_MISC + O_RED + (g == (lr >> 2) ? (wv * 80 + lr) * 8 + 4 : 2560 + l * 4), ms = L_MISC + O_RED + (g == 3 ? (wv * 80 + lr) * 8 : 2560 + l * 4);
    unsigned rb_ = L_MISC + O_RED + (wv * 20 + min(l, 19)) * 8, rc_ = L_MISC + (wv * 20 + min(l, 19)) * 4;
    unsigned r1 = L_MISC + lr * 4, r4 = L_MISC + g * 16;
    OPQ(mq); OPQ(ms); OPQ(rb_); OPQ(rc_); OPQ(r1); OPQ(r4);
    const unsigned m1 = lmask(l & 1), m2 = lmask(l & 2);
    pm = lmask(lr < 8 && g >= 2);
#pragma unroll
    for (int i = 0; i < 4; ++i) { const int rk = 4 * g + i, d = (lr < 8 && g >= 2) ? lr + 15 - rk : lr - rk; mask[i] = (d >= 0 && d <= 7 && rk != 15) ? 0.f : -1e30f; }
    OPQ(mask);
    f4 gr[5];
#pragma unroll
    for (int t = 0; t < 5; ++t) { aq[t] = ak[t] = av[t] = gr[t] = (f4){0, 0, 0, 0}; OPQ(aq[t]); OPQ(ak[t]); OPQ(av[t]); OPQ(gr[t]); }
    u4 a[2][5], z[2][5];
    f2 rv[4];
#pragma unroll
    for (int s = 0; s <= 16; ++s) {
      constexpr int NV[17] = {18, 20, 20, 16, 17, 17, 17, 17, 17, 15, 14, 16, 15, 15, 15, 15, 15};
      switch (s) {
#define C_(n) case n: WAITC(NV[n], 0); break;
        C_(0) C_(1) C_(2) C_(3) C_(4) C_(5) C_(6) C_(7) C_(8) C_(9) C_(10) C_(11) C_(12) C_(13) C_(14) C_(15) C_(16)
#undef C_
      }
      BAR();
      if (s == 0) STAMP(1);
      if (s == 4) STAMP(19);
      if (s == 8) STAMP(20);
      if (s == 12) STAMP(21);
      if (s == 16) STAMP(22);
      SB();
      auto gtile = [](int s_, int i) {
        if (s_ < 0) return -1;
        if (s_ < 5) return i == 0 ? s_ : -1;
        if (s_ < 7) return i < 2 ? 8 + 2 * (s_ - 5) + i : -1;
        if (s_ == 7) return i == 0 ? 12 : -1;
        if (s_ == 8) return i < 2 ? 16 + i : -1;
        if (s_ < 12) return i == 0 ? 16 + s_ - 7 : -1;
        if (s_ == 12) return 24 + i;
        return -1;
      };
      const int nx = s < 8 ? ((s & 3) == 0 ? 2 : 1) : 0, nd = s < 16 ? nx + 3 : 0;
#pragma unroll
      for (int i = 0; i < 20; ++i) {
        if (s > 0) {
          const int pb = (s - 1) & 1;
          if (i < 15) {
            const int t = i / 3;
            if (i % 3 == 0) mWA(aq[t], W[s - 1][0], a[pb][t]); else if (i % 3 == 1) mWA(ak[t], W[s - 1][1], a[pb][t]); else mAW(av[t], a[pb][t], W[s - 1][2]);
          } else if (gtile(s - 1, i - 15) >= 0) mVV(gr[gtile(s - 1, i - 15) & 7], z[pb][i - 15], z[pb][i - 15]);
        }
        if (s < 16) {
          if (i < 3) W[s][i] = trfrag2(trb + (s % 6) * 12288 + i * 4096, 512);
          else if (i < 8) a[s & 1][i - 3] = lds_r<u4>(xa[s & 3] + (s >> 2) * 20480 + (i - 3) * 4096);
          else if (i < 13 && gtile(s, i - 8) >= 0) z[s & 1][i - 8] = lds_r<u4>(xg + (gtile(s, i - 8) >> 3) * 20480 + (gtile(s, i - 8) & 7) * 4096);
          if (i >= 3 && (i - 3) / 2 < nd) {
            const int k = (i - 3) / 2, ph = (i - 3) & 1;
            if (k < nx) {
              const int pr = 2 + (s >> 2), j = (s & 3) == 0 ? k : (s & 3) + 1;
              if (ph == 0) setm0(lx, pr * 20480 + j * 4096); else dma_go_m(rx, xoff[j], pr * 256, xmask);
            } else {
              const int j = k - nx;
              if (ph == 0) setm0(lw, ((s + 5) % 6) * 12288 + j * 1024);
              else if (s < 11) dma_go<0>(rw, (j & 1) ? woffB : woffA, wso[j] + (s + 5) * 98304);
              else dma_go<0>(rwo, (j & 1) ? wooffB : wooffA, (((3 * (s - 11) + ((wv * 3 + j) >> 2) + 2 * hg) & 15) * 32 + ((wv * 3 + j) & 3) * 8) * 1024);
            }
          }
          if (s == 10 && i >= 10 && i < 13) st16(ws, sent, i < 12 ? ooff : ooff4, WS_OB + (i - 10) * 32768);
          if (s == 13 && i >= 10 && i < 15) xp[i - 10] = lds_r<u2>(xr + (i - 10) * 4096);
          if (s == 14 && i >= 8 && i < 13) {
            const f4 v = gr[i - 8];
            lds_w<float>(mq + (i - 8) * 128, bself(m2, bself(m1, v[3], v[2]), bself(m1, v[1], v[0])));
            lds_w<float>(ms + (i - 8) * 128, v[3]);
          }
          if (s == 15 && i == 8) { rv[0] = lds_r<f2>(rb_); rv[1] = lds_r<f2>(rb_ + 640); rv[2] = lds_r<f2>(rb_ + 1280); rv[3] = lds_r<f2>(rb_ + 1920); }
          if (s == 15 && i == 12) {
            const f2 v = (rv[0] + rv[1]) + (rv[2] + rv[3]);
            const float m = v[0] * (1.f / 512), rs = rsq(fmx(v[1] * (1.f / 512) - m * m, 0.f) + EPS);
            lds_w<float>(rc_ + O_RC, rs); lds_w<float>(rc_ + O_RM, rs * m); lds_w<float>(rc_ + O_RA, 1.f); lds_w<float>(rc_ + O_RB, 0.f);
          }
        } else {
          if (i < 15) rowc_read(R, r1, r4, i);
          if (i == 0 && wv == 0) st4(ws, tag, l == 0 ? 0 : OOB, WS_FA + (tile * 8 + hg) * 128);
          if (i == 2) dma<0>(rwo, wooffA, (((15 + 2 * hg) & 15) * 32 + wv * 8) * 1024, lx, L_RING + 3 * 12288);
          if (i >= 4 && i < 7) st16(ws, sent, i < 6 ? ooff : ooff4, WS_OB + BUFB + (i - 4) * 32768);
        }
        SB();
      }
    }
    asm volatile("" ::"v"(touch));
    OPQ(wr2);
  }
  NOPS(2);
  SB();
  f4 csq, csk;
#pragma unroll
  for (int i = 0; i < 4; ++i) { csq[i] = dppf<0x15F>(aq[0][i]); csk[i] = dppf<0x15F>(ak[0][i]); }
  float csv;
  { f2 x = swap32(av[0][3], av[0][3]); f2 y = swap16(x[1], x[1]); csv = y[1]; }
  STAMP(2);
  u4 WO[16];

#pragma unroll 1
  for (int L = 0; L < 2; ++L) {
#define FRESH(n) int l##n = l, wv##n = wv, hg##n = hg, tile##n = tile; asm volatile("" : "+v"(l##n)); asm volatile("" : "+s"(wv##n), "+s"(hg##n), "+s"(tile##n)); \
    const int lr##n = l##n & 15, g##n = l##n >> 4; (void)lr##n; (void)g##n;
    FRESH(1)
    const unsigned ob = WS_OB + L * BUFB + tile1 * TILEB;
    unsigned fa;
    {
      u2 kb[5], vb[5], qb[5], o[5];
      f4 ss[5], sp[5], sc[5];
      float mx[5];
      auto scale = [&](int t) {
        qb[t] = pk4(aq[t] * R.c[t] - csq * R.cm[t]);
        kb[t] = pk4(ak[t] * R.c[t] - csk * R.cm[t]);
        vb[t] = pk4(av[t] * R.c4[t] - R.cm4[t] * csv);
      };
      auto smm = [&](int t) { ss[t] = m16c(kb[t], qb[t], mask); if (t) sp[t] = m16c(kb[t - 1], qb[t], mask); };
      auto pick = [&](int t) {
        sc[t] = bsel4(pm, t ? sp[t] : (f4){-1e30f, -1e30f, -1e30f, -1e30f}, ss[t]);
        mx[t] = fmx(fmx(fmx(sc[t][0], sc[t][1]), sc[t][2]), sc[t][3]);
      };
      scale(0); SB();
      scale(1); SB(); smm(0); SB();
      scale(2); SB(); smm(1); SB();
      scale(3); SB(); smm(2); SB();
      scale(4); SB(); smm(3); smm(4); SB();
      pick(0); pick(1); pick(2); SB();
      pick(3); pick(4);
      {
        f2 x = swap16(mx[0], mx[1]), y = swap16(mx[2], mx[3]), w = swap16(mx[4], mx[4]);
        f2 u = swap32(fmx(x[0], x[1]), fmx(y[0], y[1]));
        const float w1 = fmx(w[0], w[1]), c = fmx(u[0], u[1]) * 0.36067376f;
        f2 w2 = swap32(w1, w1), v = swap32(c, c);
        f2 v0 = swap16(v[0], v[0]), v1 = swap16(v[1], v[1]);
        mx[0] = v0[0]; mx[1] = v0[1]; mx[2] = v1[0]; mx[3] = v1[1]; mx[4] = fmx(w2[0], w2[1]) * 0.36067376f;
      }
      asm volatile("buffer_load_dword %0, %1, %2, %3 offen sc1" : "=v"(fa) : "v"((l1 & 7) * 128), "s"(ws), "s"(WS_FA + tile1 * 1024) : "memory");
      u2 one2 = {ONE2, ONE2};
      float kap = 0.36067376f;
      OPQ(one2); OPQ(kap);
      u2 pb[5], ps[5], pp[5];
      f4 sum[5], oa[5];
      auto expo = [&](int t) {
        const f4 d = sc[t] * kap - mx[t];
        f4 e;
#pragma unroll
        for (int i = 0; i < 4; ++i) e[i] = __builtin_amdgcn_exp2f(d[i]);
        pb[t] = pk4(e);
        if (t) { pp[t] = pb[t] & pm; ps[t] = pb[t] ^ pp[t]; } else ps[t] = pb[t];
      };
      auto pv = [&](int t) { m16<true>(oa[t], vb[t], ps[t]); if (t) m16(oa[t], vb[t - 1], pp[t]); m16<true>(sum[t], one2, pb[t]); };
      auto norm = [&](int t) { KEEP(sum[t]); o[t] = pk4(oa[t] * __builtin_amdgcn_rcpf(sum[t][0])); };
      const unsigned go = (((g1 & 1) * 32 + 4 * hg1 + wv1) * 2 + (g1 >> 1)) * 256 + lr1 * 16;
      const unsigned lo_ = (((g1 & 1) * 32 + wv1) * 2 + (g1 >> 1)) * 256 + lr1 * 16;
      auto put = [&](int i) {
        u2 A = o[2 * i], B = o[i < 2 ? 2 * i + 1 : 4];
        auto s0 = __builtin_amdgcn_permlane16_swap(A[0], B[0], false, false); auto s1 = __builtin_amdgcn_permlane16_swap(A[1], B[1], false, false);
        const u4 e = {umin(s0[0], SENT - 1), s1[0], s0[1], s1[1]};
        st16(ws, e, (i == 2 && (g1 & 1)) ? OOB : go, ob + i * 32768);
        lds_w<u4>(i < 2 ? lo_ + i * 32768 : ((g1 & 1) ? lo_ + 2048 : lo_ + 65536), e);
      };
      SB();
      expo(0); SB();
      expo(1); SB(); pv(0); SB();
      expo(2); SB(); pv(1); SB();
      expo(3); SB(); pv(2); SB(); norm(0); SB();
      expo(4); SB(); pv(3); SB(); norm(1); SB(); pv(4); SB();
      put(0); norm(2); SB(); norm(3); SB();
      put(1); norm(4); SB();
      put(2);
    }
    STAMP(3 + 8 * L);
    FRESH(2)
    const unsigned l16 = l2 * 16;
    unsigned l16h = l16 + 65536, r1 = L_MISC + lr2 * 4;
    OPQ(l16h); OPQ(r1);
    f4 acc[5];
#pragma unroll
    for (int t = 0; t < 5; ++t) acc[t] = up4(xp[t]) * lds_r<float>(r1 + O_RA + t * 64) + lds_r<float>(r1 + O_RB + t * 64);
    WAITC(3, 0);
    OPQ(fa);
    BAR();
    if (L == 0) {
      unsigned tb = trb;
      OPQ(tb);
#pragma unroll
      for (int j = 0; j < 16; ++j) { WO[j] = trfrag2(tb + ((4 + j / 3) % 6) * 12288 + (j % 3) * 4096, 512); asm volatile("" : "+a"(WO[j])); }
      if (!__all(fa == tag)) wait_flags(ws, (l2 & 7) * 128, WS_FA + tile2 * 1024, tag);
    }
    u4 a[3][5];
    auto rda = [&](int j, int t) { a[j % 3][t] = lds_r<u4>((t < 4 ? l16 + t * 16384 : l16h) + j * 1024); };
#pragma unroll
    for (int t = 0; t < 5; ++t) { rda(0, t); rda(1, t); }
    const unsigned lw2 = wv2 * 1024;
    SB();
#pragma unroll
    for (int i = 0; i < 3; ++i) {
      const unsigned so = ob + ((2 * hg2 + 2 + 4 * i + wv2) & 15) * 1024;
#pragma unroll
      for (int t = 0; t < 5; ++t) {
        if (i == 0) { setm0(lw2, 2048 + t * 16384); SB(); mWA(acc[t], WO[0], a[0][t]); SB(); dma_go<1>(ws, l16, so + t * 16384); }
        else dma<1>(ws, l16, so + t * 16384, lw2, (2 + 4 * i) * 1024 + t * 16384);
      }
    }
    {
      const unsigned so = ob + ((2 * hg2 + 14 + (wv2 & 1)) & 15) * 1024;
#pragma unroll
      for (int i = 0; i < 3; ++i) { const unsigned t = min((wv2 >> 1) * 3 + i, 4); dma<1>(ws, l16, so + t * 16384, (wv2 & 1) * 1024 + t * 16384, 14 * 1024); }
    }
    STAMP(4 + 8 * L);
    auto check = [&](int j) {
      u4 c0 = a[j % 3][0], c1 = a[j % 3][1], c2 = a[j % 3][2], c3 = a[j % 3][3], c4 = a[j % 3][4];
      unsigned m = max(max(max(c0[0], c1[0]), max(c2[0], c3[0])), c4[0]);
      if (__builtin_expect(__any(m == SENT), 0)) {
        const unsigned so = ob + ((2 * hg2 + j) & 15) * 1024;
        for (int sp = 0; sp < SPIN_CAP; ++sp) {
          CFENCE();
          c0 = ld16(ws, l16, so); c1 = ld16(ws, l16, so + 16384); c2 = ld16(ws, l16, so + 32768); c3 = ld16(ws, l16, so + 49152); c4 = ld16(ws, l16, so + 65536);
          m = max(max(max(c0[0], c1[0]), max(c2[0], c3[0])), c4[0]);
          if (!__any(m == SENT)) break;
          __builtin_amdgcn_s_sleep(1);
        }
        a[j % 3][0] = c0; a[j % 3][1] = c1; a[j % 3][2] = c2; a[j % 3][3] = c3; a[j % 3][4] = c4;
        NOPS(2);
      }
    };
    WAITV(13);
    BAR();
#pragma unroll
    for (int t = 0; t < 5; ++t) rda(2, t);
    SB();
#pragma unroll
    for (int j = 1; j < 16; ++j) {
      if (j == 4) WAITV(8); else if (j == 8) WAITV(3); else if (j == 12) WAITV(0);
      if (j == 4 || j == 8 || j == 12) BAR();
      SB();
#pragma unroll
      for (int t = 0; t < 5; ++t) { mWA(acc[t], WO[j], a[j % 3][t]); if (j + 2 < 16) rda(j + 2, t); SB(); }
      if (j + 1 < 16) check(j + 1);
      SB();
    }
    STAMP(5 + 8 * L);
    FRESH(3)
    const unsigned xb = WS_XB + L * BUFB + tile3 * TILEB;
    const int row3 = wv3 < 2 ? wv3 * 24 + min(l3, 23) : 16 + wv3 * 16 + min(l3, 15);
    const bool rowok = l3 < (wv3 < 2 ? 24 : 16);
    unsigned rw_ = L_RED2 + wv3 * 2048 + l3 * 16, rr_ = L_RED2 + row3 * 16, rc_ = L_MISC + row3 * 4;
    OPQ(rw_); OPQ(rr_); OPQ(rc_);
    {
      u2 spa[4]; float spf[4];
#pragma unroll
      for (int t = 0; t < 4; ++t) { spa[t] = lr3 == 4 * t ? (u2){ONE2, ONE2} : lr3 == 4 * t + 1 ? wr2 : (u2){0, 0}; spf[t] = lr3 == 4 * t + 2 ? 1.f : 0.f; }
      SB();
      float q2[5];
      f4 da, db, ea, eb;
#pragma unroll
      for (int t = 0; t < 5; ++t) {
        xp[t] = pk4(acc[t]);
        const f2 h = (f2){acc[t][0], acc[t][1]} * (f2){acc[t][0], acc[t][1]} + (f2){acc[t][2], acc[t][3]} * (f2){acc[t][2], acc[t][3]};
        q2[t] = h[0] + h[1];
        SB();
        if (t == 1) { m16<true>(da, spa[0], xp[0]); mf32<true>(db, spf[0], q2[0]); }
        else if (t > 1) { m16(da, spa[t - 1], xp[t - 1]); mf32(db, spf[t - 1], q2[t - 1]); }
        SB();
      }
      m16<true>(ea, spa[0], xp[4]); mf32<true>(eb, spf[0], q2[4]);
      SB();
      NOPS(8);
      SB();
      KEEP(da); KEEP(db);
      lds_w<f4>(rw_, (f4){da[0], da[1], db[2], 0.f});
      SB();
      KEEP(ea); KEEP(eb);
      lds_w<f4>(rw_ + 1024, (f4){ea[0], ea[1], eb[2], 0.f});
    }
    WAITL();
    BAR();
    {
      u4 w = asu((lds_r<f4>(rr_) + lds_r<f4>(rr_ + 2048)) + (lds_r<f4>(rr_ + 4096) + lds_r<f4>(rr_ + 6144)));
      w[3] = tag;
      st16(ws, w, rowok ? row3 * 16 : OOB, WS_ST + L * STB + (tile3 * 8 + hg3) * 1280);
      STAMP(6 + 8 * L);
      const unsigned go = (((g3 & 1) * 32 + 4 * hg3 + wv3) * 2 + (g3 >> 1)) * 256 + lr3 * 16;
#pragma unroll
      for (int i = 0; i < 3; ++i) {
        u2 A = xp[2 * i], B = xp[i < 2 ? 2 * i + 1 : 4];
        auto s0 = __builtin_amdgcn_permlane16_swap(A[0], B[0], false, false); auto s1 = __builtin_amdgcn_permlane16_swap(A[1], B[1], false, false);
        st16(ws, (u4){s0[0], s1[0], s0[1], s1[1]}, (i == 2 && (g3 & 1)) ? OOB : go, xb + i * 32768);
      }
    }
    WAITV(0);
    BAR();
    if (wv3 == 0) st4(ws, tag, l3 == 0 ? 0 : OOB, WS_FX + L * 32768 + (tile3 * 8 + hg3) * 128);
    const int par3 = tile3 >= 15, ti3 = tile3 - 15 * par3, p03 = 68 * ti3;
    const bool halo = L == 0 && ti3 > 0;
    wait_flags(ws, g3 == 1 ? WS_FF + lr3 * 128 : WS_FX + L * 32768 + ((tile3 - (halo ? (lr3 >> 3) : 0)) * 8 + (l3 & 7)) * 128, 0, tag);
    STAMP(7 + 8 * L);
    {
      const unsigned so = (halo && row3 < 7) ? ((tile3 - 1) * 640 + 72 + row3) * 16 : (tile3 * 640 + row3) * 16;
      u4 v[8];
#pragma unroll
      for (int i = 0; i < 8; ++i) v[i] = ld16(ws, so, WS_ST + L * STB + i * 1280);
      if (L == 0) {
        const unsigned g1_ = l3 * 16 + 16384, g0_ = g1_ - ((halo && lr3 < 7) ? 16256 : 0);
#pragma unroll
        for (int i = 0; i < 4; ++i)
#pragma unroll
          for (int t = 0; t < 5; ++t) dma<1>(ws, t ? g1_ : g0_, xb - 16384 + wv3 * 1024 + t * 16384 + i * 4096, wv3 * 1024, t * 16384 + i * 4096);
      }
      const f4 st = ((asf(v[0]) + asf(v[1])) + (asf(v[2]) + asf(v[3]))) + ((asf(v[4]) + asf(v[5])) + (asf(v[6]) + asf(v[7])));
      const float m = st[0] * (1.f / 512), var = fmx(st[2] * (1.f / 512) - m * m, 0.f), rs = rsq(var + EPS), a1 = 1.f + rs;
      const float c = a1 * rsq(var * a1 * a1 + EPS);
      lds_w<float>(rc_ + O_RC, c); lds_w<float>(rc_ + O_RM, c * m); lds_w<float>(rc_ + O_RA, a1); lds_w<float>(rc_ + O_RB, -m * rs);
    }
    FRESH(4)
    unsigned q1 = L_MISC + lr4 * 4, q4 = L_MISC + g4 * 16;
    OPQ(q1); OPQ(q4);
    if (L == 0) {
      unsigned zl = l4 * 16, zh = zl + 65536;
      OPQ(zh);
      WAITC(0, 0);
      BAR();
      STAMP(26);
      {
        u4 z[2][5];
#pragma unroll
        for (int t = 0; t < 5; ++t) z[0][t] = lds_r<u4>(t < 4 ? zl + t * 16384 : zh);
        SB();
#pragma unroll
        for (int s = 0; s < 16; ++s) {
#pragma unroll
          for (int t = 0; t < 5; ++t) {
            if (s == 0) mWA<true>(aq[t], W[s][0], z[s & 1][t]); else mWA(aq[t], W[s][0], z[s & 1][t]);
            if (s < 15) z[(s + 1) & 1][t] = lds_r<u4>((t < 4 ? zl + t * 16384 : zh) + (s + 1) * 1024); else rowc_read(R, q1, q4, 3 * t);
            SB();
            if (s == 0) mWA<true>(ak[t], W[s][1], z[s & 1][t]); else mWA(ak[t], W[s][1], z[s & 1][t]);
            if (s == 15) rowc_read(R, q1, q4, 3 * t + 1);
            SB();
            if (s == 0) mAW<true>(av[t], z[s & 1][t], W[s][2]); else mAW(av[t], z[s & 1][t], W[s][2]);
            if (s == 15) rowc_read(R, q1, q4, 3 * t + 2);
            SB();
          }
        }
      }
      STAMP(8);
      const u4 sel = wait_rec(ws, (l4 & 1) * 16, WS_SEL + (b & 15) * 128, tag);
      STAMP(9);
#pragma unroll 1
      for (int i = 0; i < 2; ++i) {
        const unsigned key = rdl(sel[0], i);
        const int tok = 2047 - (key & 2047), u = (tok >> 1) - p03;
        if ((tok & 1) != par3 || u < 0 || u >= 75) continue;
        FRESH(5)
        const float s_m = __uint_as_float(rdl(sel[1], i)), s_rs = __uint_as_float(rdl(sel[2], i));
        const float sg = sigm(key_score(key)), a1 = 1.f + s_rs, b1 = -s_m * s_rs;
        const int ts = u / 15, rs_ = u - 15 * ts;
        unsigned yb = L_RING + l5 * 32, rb = L_RING + 8192 + wv5 * 1024, zb = rb + g5 * 16;
        OPQ(yb); OPQ(zb);
        const u4 xr = lds_r<u4>((ts * 16 + (l5 >> 2)) * 1024 + (l5 & 3) * 256 + rs_ * 16);
        const f4 x0 = up4((u2){xr[0], xr[1]}) * a1 + b1, x1 = up4((u2){xr[2], xr[3]}) * a1 + b1;
        const unsigned me = lmask(lr5 == rs_);
        BAR();
        u4 y[8];
        for (int sp = 0; sp < SPIN_CAP; ++sp) {
          CFENCE();
          unsigned m = 0;
#pragma unroll
          for (int j = 0; j < 8; ++j) { y[j] = ld16(ws, l5 * 32 + (j & 1) * 16, WS_YP + (4 * wv5 + (j >> 1)) * 4096 + i * 2048); m = max(m, y[j][0]); }
          if (!__any(m == SENT)) break;
          __builtin_amdgcn_s_sleep(1);
        }
        STAMP(27);
        lds_w<f4>(yb + wv5 * 2048, (asf(y[0]) + asf(y[2])) + (asf(y[4]) + asf(y[6])));
        lds_w<f4>(yb + wv5 * 2048 + 16, (asf(y[1]) + asf(y[3])) + (asf(y[5]) + asf(y[7])));
        WAITL();
        BAR();
        const f4 y0 = (lds_r<f4>(yb) + lds_r<f4>(yb + 2048)) + (lds_r<f4>(yb + 4096) + lds_r<f4>(yb + 6144));
        const f4 y1 = (lds_r<f4>(yb + 16) + lds_r<f4>(yb + 2048 + 16)) + (lds_r<f4>(yb + 4096 + 16) + lds_r<f4>(yb + 6144 + 16));
        const u2 z0 = pk4(y0 * sg + x0), z1 = pk4(y1 * sg + x1);
        lds_w<u4>(rb + l5 * 16, (u4){z0[0], z0[1], z1[0], z1[1]});
        f4 tq, tk, tv;
        u4 z[16];
#pragma unroll
        for (int s = 0; s < 4; ++s) z[s] = lds_r<u4>(zb + s * 64);
        const u2 zx = lds_r<u2>(rb + (64 * hg5 + 16 * wv5 + 4 * g5) * 2);
        SB();
#pragma unroll
        for (int s = 0; s < 16; ++s) {
          if (s == 0) mWA<true>(tq, W[s][0], z[s]); else mWA(tq, W[s][0], z[s]);
          if (s + 4 < 16) z[s + 4] = lds_r<u4>(zb + (s + 4) * 64);
          SB();
          if (s == 0) { mWA<true>(tk, W[s][1], z[s]); mAW<true>(tv, z[s], W[s][2]); } else { mWA(tk, W[s][1], z[s]); mAW(tv, z[s], W[s][2]); }
          SB();
        }
        const f4 f0 = up4(z0), f1 = up4(z1), fs = f0 + f1, fq = f0 * f0 + f1 * f1;
        const float m2 = wave_sum((fs[0] + fs[1]) + (fs[2] + fs[3])) * (1.f / 512), q2 = wave_sum((fq[0] + fq[1]) + (fq[2] + fq[3])) * (1.f / 512);
        const float r2 = rsq(fmx(q2 - m2 * m2, 0.f) + EPS), rm2 = r2 * m2;
        SB();
        if (l5 == 0) { const unsigned ra = L_MISC + (ts * 16 + rs_) * 4; lds_w<float>(ra + O_RA, 1.f); lds_w<float>(ra + O_RB, 0.f); }
#pragma unroll
        for (int t = 0; t < 5; ++t)
          if (t == ts) {
            aq[t] = bsel4(me, tq, aq[t]); ak[t] = bsel4(me, tk, ak[t]);
            xp[t] = (u2){bsel(me, zx[0], xp[t][0]), bsel(me, zx[1], xp[t][1])};
            R.c[t] = bself(me, r2, R.c[t]); R.cm[t] = bself(me, rm2, R.cm[t]);
#pragma unroll
            for (int e = 0; e < 4; ++e) {
              const bool mine = 4 * g5 + e == rs_;
              av[t][e] = mine ? tv[e] : av[t][e]; R.c4[t][e] = mine ? r2 : R.c4[t][e]; R.cm4[t][e] = mine ? rm2 : R.cm4[t][e];
            }
          }
      }
      WAITL();
      BAR();
      STAMP(10);
    } else {
      const rsrc_t ro = mk(P.out, 2048 * 1024);
      WAITL();
      BAR();
#pragma unroll
      for (int t = 0; t < 5; ++t) {
        const float ra = lds_r<float>(q1 + O_RA + t * 64), rb = lds_r<float>(q1 + O_RB + t * 64);
        lds_w<u2>((16 * t + lr4) * 144 + wv4 * 32 + g4 * 8, pk4(up4(xp[t]) * ra + rb));
      }
      WAITL();
      BAR();
#pragma unroll
      for (int i = 0; i < 3; ++i) {
        const int row = min((i * 4 + wv4) * 8 + (l4 >> 3), 79), u = row - (row >> 4), p = p03 + u;
        const bool ok = (i * 4 + wv4) < 10 && (row & 15) != 15 && p < 1024 && (ti3 == 0 || u >= 7);
        st16(ro, lds_r<u4>(row * 144 + (l4 & 7) * 16), ok ? (2 * p + par3) * 1024 + hg4 * 128 + (l4 & 7) * 16 : OOB, 0);
      }
      STAMP(16);
      WAITV(0);
      BAR();
      if (wv4 < 2) {
        const u4 sel = wait_rec(ws, wv4 * 16, WS_SEL + 2048 + (b & 15) * 128, tag);
        STAMP(17);
        const unsigned key = rfl(sel[0]);
        const int tok = 2047 - (key & 2047), u = (tok >> 1) - p03;
        if ((tok & 1) == par3 && u >= (ti3 ? 7 : 0) && u < 75) {
          const f4 base = up4(lds_r<u2>((u + u / 15) * 144 + lr4 * 8));
          const float sg = sigm(key_score(key));
          u4 y[4];
          for (int sp = 0; sp < SPIN_CAP; ++sp) {
            CFENCE();
            unsigned m = 0;
#pragma unroll
            for (int j = 0; j < 4; ++j) { y[j] = ld16(ws, g4 * 4096 + lr4 * 16, WS_YP + 65536 + j * 16384 + wv4 * 2048 + hg4 * 256); m = max(m, y[j][0]); }
            if (!__any(m == SENT)) break;
            __builtin_amdgcn_s_sleep(1);
          }
          const f4 s = (asf(y[0]) + asf(y[1])) + (asf(y[2]) + asf(y[3]));
          const f2 x = swap16(s[0], s[1]), w = swap16(s[2], s[3]), v = swap32(x[0] + x[1], w[0] + w[1]);
          const float e = v[0] + v[1];
          const f2 v2 = swap32(e, e), e01 = swap16(v2[0], v2[0]), e23 = swap16(v2[1], v2[1]);
          __builtin_amdgcn_raw_buffer_store_b64(pk4((f4){e01[0], e01[1], e23[0], e23[1]} * sg + base), ro, l4 < 16 ? tok * 1024 + hg4 * 128 + lr4 * 8 : OOB, 0, 16);
        }
        STAMP(18);
      }
    }
  }
  STAMP_FLUSH();
}

DEV void role_f(const Params& P, const int j, const int wv, const int l) {
  const int lr = l & 15, g = l >> 4, tid = wv * 64 + l;
  const rsrc_t ws = mk(P.ws, WS_USED);
  const u64 t0 = __builtin_amdgcn_s_memrealtime();
  STAMP_INIT();
  STAMP(0);
  const unsigned tag = tag_finish(tag_issue(P.ws));
  const u4 sent = {SENT, SENT, SENT, SENT};
  st16(ws, sent, j * 4096 + tid * 16, WS_YP); st16(ws, sent, j * 4096 + tid * 16, WS_YP + 65536);
  float wsum;
  { u4 w = *(const u4*)(P.wr + l * 8); f4 a = up4((u2){w[0], w[1]}) + up4((u2){w[2], w[3]}); wsum = wave_sum((a[0] + a[1]) + (a[2] + a[3])); }
  WAITV(0);
  BAR();
  if (wv == 0) st4(ws, tag, l == 0 ? 0 : OOB, WS_FF + j * 128);
  for (int sp = 0; sp < 4096 && __builtin_amdgcn_s_memrealtime() - t0 < FDELAY; ++sp) __builtin_amdgcn_s_sleep(4);
  const int noct = j < 12 ? 11 : 10, f0 = 8 * (j < 12 ? 11 * j : 132 + 10 * (j - 12));
  u4 G[16][3], Dn[3][8];
  {
    const rsrc_t rgu = mk(wv < 2 ? P.wg : P.wu, 512 * 2752), rd = mk(P.wd, 1376 * 1024);
    unsigned go[6];
#pragma unroll
    for (int i = 0; i < 6; ++i) { const int n = 64 * ((wv & 1) * 6 + i) + l, row = n / 12; go[i] = row * 2752 + f0 * 2 + (n - 12 * row) * 16; }
    const unsigned dof = (f0 + g) * 1024 + wv * 256 + lr * 16;
    auto issue = [&](int rd_) {
      if (rd_ < 8) {
#pragma unroll
        for (int i = 0; i < 6; ++i) dma<0>(rgu, go[i], rd_ * 64 * 2752, wv * 6144, (rd_ & 3) * 32768 + i * 1024);
      } else {
#pragma unroll
        for (int i = 0; i < 8; ++i) dma<0>(rd, dof, (32 * (rd_ - 8) + 4 * i) * 1024, wv * 8192, (rd_ & 3) * 32768 + i * 1024);
      }
    };
    unsigned tg = ((l >> 1) & 1) * 12288 + (8 * g + (lr >> 2)) * 192 + wv * 48 + (l & 1) * 8, td = wv * 8192 + (8 * g + (lr >> 2)) * 256 + (l & 3) * 8;
    issue(0); issue(1); issue(2);
#pragma unroll
    for (int rd_ = 0; rd_ < 11; ++rd_) {
      if (rd_ < 6) WAITC(12, 0); else if (rd_ == 6) WAITC(14, 0); else if (rd_ < 9) WAITC(16, 0); else if (rd_ == 9) WAITC(8, 0); else WAITC(0, 0);
      BAR();
      if (rd_ + 3 < 11) issue(rd_ + 3);
      unsigned bg = tg + (rd_ & 3) * 32768, bd = td + (rd_ & 3) * 32768;
      OPQ(bg); OPQ(bd);
      if (rd_ < 8) {
#pragma unroll
        for (int ks = 0; ks < 2; ++ks)
#pragma unroll
          for (int c = 0; c < 3; ++c) G[2 * rd_ + ks][c] = trfrag2(bg + ks * 32 * 192 + c * 16, 4 * 192);
      } else {
#pragma unroll
        for (int c = 0; c < 8; ++c) Dn[rd_ - 8][c] = trfrag2(bd + c * 32, 4 * 256);
      }
    }
    WAITL();
    BAR();
  }
  constexpr unsigned F_ACT = 0;
  const unsigned mg1 = lmask(g == 1), mg2 = lmask(g >= 2), mlo = lmask(lr < 8);
  auto gather = [&](const f4& a0, const f4& a1, const f4& a2, float& gv, float& uv) {
    KEEP(a0); KEEP(a1); KEEP(a2);
    const float d0 = bself(mg2, a2[0], bself(mg1, a1[0], a0[0])), d1 = bself(mg2, a2[1], bself(mg1, a1[1], a0[1]));
    gv = __uint_as_float(__builtin_amdgcn_update_dpp(__float_as_uint(d0), __float_as_uint(d1), 0x128, 0xF, 0xC, false));
    uv = __uint_as_float(__builtin_amdgcn_update_dpp(__float_as_uint(d1), __float_as_uint(d0), 0x128, 0xF, 0x3, false));
  };
  float csg, csu;
  {
    u4 one = {ONE2, ONE2, ONE2, ONE2};
    OPQ(one);
    NOPS(2);
    SB();
    f4 a[3];
#pragma unroll
    for (int ks = 0; ks < 16; ++ks)
#pragma unroll
      for (int c = 0; c < 3; ++c) { if (ks == 0) mAW<true>(a[c], one, G[ks][c]); else mAW(a[c], one, G[ks][c]); }
    SB();
    NOPS(8);
    SB();
    gather(a[0], a[1], a[2], csg, csu);
  }
  const bool live = g < 3 && 3 * wv + g < noct;
  const unsigned actw = F_ACT + (g < 3 ? (lr >> 3) * 192 + (24 * wv + 8 * g + (l & 7)) * 2 : 512 + l * 2);
  STAMP(1);
  const int tok = 32 * (4 * j + wv) + (l & 31);
  unsigned sto;
  { const int p = tok >> 1, ti = max(p - 7, 0) / 68, u = p - 68 * ti; sto = ((((tok & 1) * 15 + ti) * 8 + 4 * (l >> 5)) * 80 + u + u / 15) * 16; }

#pragma unroll 1
  for (int L = 0; L < 2; ++L) {
    u4 v[4];
    for (int sp = 0; sp < SPIN_CAP; ++sp) {
      CFENCE();
#pragma unroll
      for (int i = 0; i < 4; ++i) v[i] = ld16(ws, sto, WS_ST + L * STB + i * 1280);
      if (__all(v[0][3] == tag && v[1][3] == tag && v[2][3] == tag && v[3][3] == tag)) break;
      __builtin_amdgcn_s_sleep(1);
    }
    STAMP(2 + 8 * L);
    {
      const f4 s = (asf(v[0]) + asf(v[1])) + (asf(v[2]) + asf(v[3]));
      const float m = xadd32(s[0]) * (1.f / 512), dt = xadd32(s[1]), qq = xadd32(s[2]) * (1.f / 512);
      const float rs = rsq(fmx(qq - m * m, 0.f) + EPS), sc = (dt - m * wsum) * rs;
      unsigned u = __float_as_uint(sc); u ^= (unsigned)((int)u >> 31) | 0x80000000u;
      const unsigned key = (u & 0xFFFFF800u) | (2047u - tok);
      const unsigned k1 = max_x16(max_row16(key)), k2 = max_x16(max_row16(key == k1 ? 0u : key));
      st16(ws, (u4){key, __float_as_uint(m), __float_as_uint(rs), tag}, l >= 32 ? OOB : key == k1 ? 0 : key == k2 ? 16 : OOB, WS_CAND + L * 8192 + (4 * j + wv) * 128);
    }
    STAMP(3 + 8 * L);
    u4 c0, c1;
    for (int sp = 0; sp < SPIN_CAP; ++sp) {
      CFENCE();
      c0 = ld16(ws, l * 128, WS_CAND + L * 8192); c1 = ld16(ws, l * 128 + 16, WS_CAND + L * 8192);
      const unsigned f = ld4(ws, (wv * 60 + min(l, 59)) * 128, WS_FX + L * 32768);
      if (__all(c0[3] == tag && c1[3] == tag && f == tag)) break;
      __builtin_amdgcn_s_sleep(1);
    }
    BAR();
    STAMP(4 + 8 * L);
    const unsigned k1 = rfl(max_x32(max_x16(max_row16(c0[0]))));
    const bool w1 = c0[0] == k1;
    const u4 cb = w1 ? c1 : c0;
    const unsigned k2 = rfl(max_x32(max_x16(max_row16(cb[0]))));
    u4 h[16];
    {
      const int t = 2047 - (((l & 1) ? k2 : k1) & 2047), p = t >> 1, ti = max(p - 7, 0) / 68, u = p - 68 * ti, ts = u / 15;
      const unsigned vo = ((t & 1) * 15 + ti) * TILEB + ts * 16384 + g * 256 + (u - 15 * ts) * 16;
#pragma unroll
      for (int ks = 0; ks < 16; ++ks) h[ks] = ld16(ws, vo + (ks & 3) * 1024, WS_XB + L * BUFB + (ks >> 2) * 4096);
    }
    SB();
    const int s1 = __builtin_ctzll(__ballot(w1) | (1ull << 63)), s2 = __builtin_ctzll(__ballot(cb[0] == k2) | (1ull << 63));
    const unsigned ma = rdl(c0[1], s1), ra = rdl(c0[2], s1), mb = rdl(cb[1], s2), rb = rdl(cb[2], s2);
    if (wv == 0) st16(ws, (l & 1) ? (u4){k2, mb, rb, tag} : (u4){k1, ma, ra, tag}, l < 2 ? l * 16 : OOB, WS_SEL + L * 2048 + j * 128);
    const float m = __uint_as_float(bsel(mlo, ma, mb)), rs = __uint_as_float(bsel(mlo, ra, rb));
    SB();
    {
      f4 a[3];
#pragma unroll
      for (int ks = 0; ks < 16; ++ks)
#pragma unroll
        for (int c = 0; c < 3; ++c) { if (ks == 0) mAW<true>(a[c], h[ks], G[ks][c]); else mAW(a[c], h[ks], G[ks][c]); }
      SB();
      NOPS(8);
      SB();
      float gg, uu;
      gather(a[0], a[1], a[2], gg, uu);
      gg = (gg - m * csg) * rs; uu = (uu - m * csu) * rs;
      const float act = live ? gg * sigm(gg) * uu : 0.f;
      lds_w<u16>(actw, (u16)pk(act, act));
    }
    STAMP(5 + 8 * L);
    WAITL();
    BAR();
    {
      u4 b[3];
#pragma unroll
      for (int ks = 0; ks < 3; ++ks) b[ks] = lds_r<u4>(F_ACT + (l & 1) * 192 + ks * 64 + g * 16);
      const unsigned m2 = lmask(l & 2);
      const unsigned vo = lr < 4 ? j * 4096 + (l & 1) * 2048 + wv * 512 + (l & 2) * 32 + g * 16 : OOB;
      f4 y[8];
      SB();
#pragma unroll
      for (int c = 0; c < 8; ++c) { mWA<true>(y[c], Dn[0][c], b[0]); mWA(y[c], Dn[1][c], b[1]); mVV(y[c], Dn[2][c], b[2]); }
      SB();
#pragma unroll
      for (int c = 0; c < 4; ++c) {
        if (c == 0) { NOPS(1); SB(); }
        u4 w = asu(bsel4(m2, y[2 * c + 1], y[2 * c]));
        w[0] = umin(w[0], SENT - 1);
        st16(ws, w, vo, WS_YP + L * 65536 + c * 128);
        SB();
      }
    }
    STAMP(6 + 8 * L);
  }
  if (j == 0 && wv == 0) st4(ws, tag, l == 0 ? 0 : OOB, WS_EPOCH);
  STAMP_FLUSH();
}

__global__ void __launch_bounds__(256, 1) fused_kernel(Params P) {
  const int b = blockIdx.x, wv = __builtin_amdgcn_readfirstlane(threadIdx.x >> 6), l = threadIdx.x & 63;
  if (b < 240) role_a(P, b, wv, l);
  else role_f(P, b - 240, wv, l);
}

extern "C" __device__ __attribute__((used, noinline)) void code_end_marker() {}

extern "C" void kernel_launch(void* const* d_in, const int* in_sizes, int n_in, void* d_out, int out_size, void* d_ws, size_t ws_size, hipStream_t stream) {
  Params P;
  P.x = (const u16*)d_in[0]; P.wqkv = (const u16*)d_in[1]; P.wo = (const u16*)d_in[2];
  P.wr = (const u16*)d_in[4]; P.wg = (const u16*)d_in[5]; P.wu = (const u16*)d_in[6]; P.wd = (const u16*)d_in[7];
  P.out = (u16*)d_out; P.ws = (char*)d_ws;
  (void)hipFuncSetAttribute((const void*)fused_kernel, hipFuncAttributeMaxDynamicSharedMemorySize, LDS_BYTES);
  fused_kernel<<<256, 256, LDS_BYTES, stream>>>(P);
}
```

```cpp
#define STAMPS
#define STAMPMASK 0x30000001u
#define PROBE_A 28
#define PROBE_B 0
#define PROBE_BLK 0
#define PROBE_MUL 10
#define PROBE_DIV 1
#include <hip/hip_runtime.h>
#include <hip/hip_bf16.h>
#include <stdint.h>

typedef unsigned short u16;
typedef unsigned long long u64;
typedef __bf16 bf2 __attribute__((ext_vector_type(2)));
typedef short s4 __attribute__((ext_vector_type(4)));
typedef float f4 __attribute__((ext_vector_type(4)));
typedef float f2 __attribute__((ext_vector_type(2)));
typedef unsigned u4 __attribute__((ext_vector_type(4)));
typedef unsigned u2 __attribute__((ext_vector_type(2)));
typedef __amdgpu_buffer_rsrc_t rsrc_t;
#define LAS __attribute__((address_space(3)))
#define DEV __device__ __forceinline__

constexpr unsigned TILEB = 81920;
constexpr unsigned BUFB = 30 * TILEB;
constexpr unsigned WS_EPOCH = 0;
constexpr unsigned WS_FF = 4096;
constexpr unsigned WS_SEL = 8192;
constexpr unsigned WS_CAND = 16384;
constexpr unsigned WS_FA = 32768;
constexpr unsigned WS_FX = 65536;
constexpr unsigned WS_YP = 131072;
constexpr unsigned WS_ST = 262144;
constexpr unsigned STB = 30 * 8 * 80 * 16;
constexpr unsigned WS_OB = 1048576;
constexpr unsigned WS_XB = WS_OB + 2 * BUFB;
constexpr unsigned WS_USED = WS_XB + 2 * BUFB;
constexpr unsigned WS_STAMP = 16u << 20;
constexpr unsigned L_RING = 81920;
constexpr unsigned L_RED2 = L_RING + 16384;
constexpr unsigned L_MISC = 155648;
constexpr unsigned O_RED = 0;
constexpr unsigned O_RC = 4096, O_RM = O_RC + 320, O_RA = O_RM + 320, O_RB = O_RA + 320;
constexpr unsigned LDS_BYTES = 163840;
constexpr unsigned SENT = 0xFFFFFFFFu, OOB = 0x80000000u, ONE2 = 0x3F803F80u;
#define FDELAY 200
constexpr int SPIN_CAP = 1 << 15;
constexpr float EPS = 1e-5f;

#define CFENCE() asm volatile("" ::: "memory")
#define WAITC(vm, lg) do { CFENCE(); __builtin_amdgcn_s_waitcnt(((vm) & 15) | 0x70 | ((lg) << 8) | (((vm) >> 4) << 14)); CFENCE(); } while (0)
#define WAITV(n) asm volatile("s_waitcnt vmcnt(%0)" ::"i"(n) : "memory")
#define WAITL() WAITC(63, 0)
#define BAR() do { CFENCE(); __builtin_amdgcn_s_barrier(); CFENCE(); } while (0)
#define SB() __builtin_amdgcn_sched_barrier(0)
#define OPQ(x) asm volatile("" : "+v"(x))
#define KEEP(x) asm volatile("" ::"v"(x))

struct Params {
  const u16* x; const u16* wqkv; const u16* wo; const u16* wr; const u16* wg; const u16* wu; const u16* wd;
  u16* out; char* ws;
};

DEV rsrc_t mk(const void* p, unsigned bytes) { return __builtin_amdgcn_make_buffer_rsrc((void*)p, 0, bytes, 0x00020000); }
DEV void setm0(unsigned base, int imm) { asm volatile("s_add_u32 m0, %0, %1" ::"s"(base), "i"(imm) : "scc", "memory"); }
template <int SC> DEV void dma_go(rsrc_t rs, unsigned voff, unsigned soff) {
  if (SC) asm volatile("buffer_load_dwordx4 %0, %1, %2 offen sc1 lds" ::"v"(voff), "s"(rs), "s"(soff) : "memory");
  else asm volatile("buffer_load_dwordx4 %0, %1, %2 offen lds" ::"v"(voff), "s"(rs), "s"(soff) : "memory");
}
DEV void dma_go_m(rsrc_t rs, unsigned voff, unsigned soff, u64 mask) {
  asm volatile("s_mov_b64 exec, %3\n\tbuffer_load_dwordx4 %0, %1, %2 offen lds\n\ts_mov_b64 exec, -1" ::"v"(voff), "s"(rs), "s"(soff), "s"(mask) : "memory");
}
template <int SC> DEV void dma(rsrc_t rs, unsigned voff, unsigned soff, unsigned base, int imm) {
  if (SC) asm volatile("s_add_u32 m0, %3, %4\n\ts_nop 0\n\tbuffer_load_dwordx4 %0, %1, %2 offen sc1 lds" ::"v"(voff), "s"(rs), "s"(soff), "s"(base), "i"(imm) : "scc", "memory");
  else asm volatile("s_add_u32 m0, %3, %4\n\ts_nop 0\n\tbuffer_load_dwordx4 %0, %1, %2 offen lds" ::"v"(voff), "s"(rs), "s"(soff), "s"(base), "i"(imm) : "scc", "memory");
}
DEV void st16(rsrc_t rs, u4 v, unsigned voff, unsigned soff) {
  asm volatile("buffer_store_dwordx4 %0, %1, %2, %3 offen sc1\n\ts_nop 1" ::"v"(v), "v"(voff), "s"(rs), "s"(soff) : "memory");
}
DEV void st4(rsrc_t rs, unsigned v, unsigned voff, unsigned soff) {
  asm volatile("buffer_store_dword %0, %1, %2, %3 offen sc1" ::"v"(v), "v"(voff), "s"(rs), "s"(soff) : "memory");
}
DEV u4 ld16(rsrc_t rs, unsigned voff, unsigned soff) { return __builtin_amdgcn_raw_buffer_load_b128(rs, voff, soff, 16); }
DEV unsigned ld4(rsrc_t rs, unsigned voff, unsigned soff) { return __builtin_amdgcn_raw_buffer_load_b32(rs, voff, soff, 16); }

template <bool Z = false> DEV void mWA(f4& c, u4 w, u4 a) {
  if (Z) asm volatile("v_mfma_f32_16x16x32_bf16 %0, %1, %2, 0" : "=v"(c) : "a"(w), "v"(a));
  else asm volatile("v_mfma_f32_16x16x32_bf16 %0, %1, %2, %0" : "+v"(c) : "a"(w), "v"(a));
}
template <bool Z = false> DEV void mAW(f4& c, u4 a, u4 w) {
  if (Z) asm volatile("v_mfma_f32_16x16x32_bf16 %0, %1, %2, 0" : "=v"(c) : "v"(a), "a"(w));
  else asm volatile("v_mfma_f32_16x16x32_bf16 %0, %1, %2, %0" : "+v"(c) : "v"(a), "a"(w));
}
template <bool Z = false> DEV void mVV(f4& c, u4 a, u4 b) {
  if (Z) asm volatile("v_mfma_f32_16x16x32_bf16 %0, %1, %2, 0" : "=v"(c) : "v"(a), "v"(b));
  else asm volatile("v_mfma_f32_16x16x32_bf16 %0, %1, %2, %0" : "+v"(c) : "v"(a), "v"(b));
}
template <bool Z = false> DEV void m16(f4& c, u2 a, u2 b) {
  if (Z) asm volatile("v_mfma_f32_16x16x16_bf16 %0, %1, %2, 0" : "=v"(c) : "v"(a), "v"(b));
  else asm volatile("v_mfma_f32_16x16x16_bf16 %0, %1, %2, %0" : "+v"(c) : "v"(a), "v"(b));
}
DEV f4 m16c(u2 a, u2 b, f4 c) { f4 d; asm volatile("v_mfma_f32_16x16x16_bf16 %0, %1, %2, %3" : "=&v"(d) : "v"(a), "v"(b), "v"(c)); return d; }
template <bool Z = false> DEV void mf32(f4& c, float a, float b) {
  if (Z) asm volatile("v_mfma_f32_16x16x4_f32 %0, %1, %2, 0" : "=&v"(c) : "v"(a), "v"(b));
  else asm volatile("v_mfma_f32_16x16x4_f32 %0, %1, %2, %0" : "+v"(c) : "v"(a), "v"(b));
}
#define NOPS(n) asm volatile("s_nop %0" ::"i"((n) - 1))
DEV unsigned pk(float a, float b) { f2 v = {a, b}; return __builtin_bit_cast(unsigned, __builtin_convertvector(v, bf2)); }
DEV u2 pk4(f4 v) { return (u2){pk(v[0], v[1]), pk(v[2], v[3])}; }
DEV float lo(unsigned u) { return __uint_as_float(u << 16); }
DEV float hi(unsigned u) { return __uint_as_float(u & 0xFFFF0000u); }
DEV f4 up4(u2 u) { return (f4){lo(u[0]), hi(u[0]), lo(u[1]), hi(u[1])}; }
DEV f4 asf(u4 v) { return __builtin_bit_cast(f4, v); }
DEV u4 asu(f4 v) { return __builtin_bit_cast(u4, v); }
DEV unsigned bsel(unsigned m, unsigned a, unsigned b) { return __builtin_amdgcn_bitop3_b32(m, a, b, 0xCA); }
DEV float bself(unsigned m, float a, float b) { return __uint_as_float(bsel(m, __float_as_uint(a), __float_as_uint(b))); }
DEV f4 bsel4(unsigned m, f4 a, f4 b) { return (f4){bself(m, a[0], b[0]), bself(m, a[1], b[1]), bself(m, a[2], b[2]), bself(m, a[3], b[3])}; }
DEV unsigned lmask(bool c) { unsigned m = c ? ~0u : 0u; OPQ(m); return m; }
DEV float fmx(float a, float b) { return __builtin_elementwise_maximum(a, b); }
template <class T> DEV T lds_r(unsigned a) { return *(const LAS T*)(size_t)a; }
template <class T> DEV void lds_w(unsigned a, T v) { *(LAS T*)(size_t)a = v; }
DEV u4 trfrag2(unsigned a, unsigned step) {
  u2 x = __builtin_bit_cast(u2, __builtin_amdgcn_ds_read_tr16_b64_v4i16((LAS s4*)(size_t)a));
  u2 y = __builtin_bit_cast(u2, __builtin_amdgcn_ds_read_tr16_b64_v4i16((LAS s4*)(size_t)(a + step)));
  return (u4){x[0], x[1], y[0], y[1]};
}
DEV f2 swap16(float a, float b) { auto r = __builtin_amdgcn_permlane16_swap(__float_as_uint(a), __float_as_uint(b), false, false); return (f2){__uint_as_float(r[0]), __uint_as_float(r[1])}; }
DEV f2 swap32(float a, float b) { auto r = __builtin_amdgcn_permlane32_swap(__float_as_uint(a), __float_as_uint(b), false, false); return (f2){__uint_as_float(r[0]), __uint_as_float(r[1])}; }
DEV float xadd16(float v) { f2 r = swap16(v, v); return r[0] + r[1]; }
DEV float xadd32(float v) { f2 r = swap32(v, v); return r[0] + r[1]; }
template <int C> DEV unsigned dpp(unsigned v) { return __builtin_amdgcn_update_dpp(0u, v, C, 0xF, 0xF, true); }
template <int C> DEV float dppf(float v) { return __uint_as_float(dpp<C>(__float_as_uint(v))); }
DEV float wave_sum(float v) {
  v += dppf<0xB1>(v); v += dppf<0x4E>(v); v += dppf<0x141>(v); v += dppf<0x140>(v);
  return xadd32(xadd16(v));
}
DEV unsigned max_row16(unsigned v) { v = max(v, dpp<0xB1>(v)); v = max(v, dpp<0x4E>(v)); v = max(v, dpp<0x141>(v)); return max(v, dpp<0x140>(v)); }
DEV unsigned max_x16(unsigned v) { auto r = __builtin_amdgcn_permlane16_swap(v, v, false, false); return max(r[0], r[1]); }
DEV unsigned max_x32(unsigned v) { auto r = __builtin_amdgcn_permlane32_swap(v, v, false, false); return max(r[0], r[1]); }
DEV unsigned umin(unsigned a, unsigned b) { return a < b ? a : b; }
DEV unsigned rfl(unsigned v) { return __builtin_amdgcn_readfirstlane(v); }
DEV unsigned rdl(unsigned v, int l) { return __builtin_amdgcn_readlane(v, l); }
DEV float rsq(float v) { return __builtin_amdgcn_rsqf(v); }
DEV float sigm(float v) { return __builtin_amdgcn_rcpf(1.f + __builtin_amdgcn_exp2f(-1.44269504f * v)); }
DEV float key_score(unsigned k) { unsigned u = k & 0xFFFFF800u; return __uint_as_float((u & 0x80000000u) ? (u ^ 0x80000000u) : ~u); }

#define STAMP(i) do { if (__builtin_constant_p(i) && !((STAMPMASK >> (i)) & 1)) break; u64 a_, b_; asm volatile("s_memrealtime %0\n\ts_memtime %1\n\ts_waitcnt lgkmcnt(0)" : "=&s"(a_), "=&s"(b_) :: "memory"); \
    asm volatile("s_mov_b32 m0, %3\n\ts_nop 0\n\tv_writelane_b32 %0, %1, m0\n\ts_add_u32 m0, m0, 32\n\ts_nop 0\n\tv_writelane_b32 %0, %2, m0" : "+v"(stv) : "s"((unsigned)a_), "s"((unsigned)b_), "s"(i) : "scc"); } while (0)
#define STAMP_INIT() unsigned stv = 0; asm volatile("v_writelane_b32 %0, %1, 28" : "+v"(stv) : "s"(g_tent))
#define PROBE_X 0
#define STAMP_FLUSH() do { STAMP(29); if (PROBE_X && blockIdx.x == PROBE_BLK) { unsigned o_ = rfl(ld4(ws, 0, 2048)); asm volatile("v_writelane_b32 %0, %1, 27" : "+v"(stv) : "s"(o_)); } if (blockIdx.x == PROBE_BLK && threadIdx.x < 64) { unsigned d_ = min(max((int)(rdl(stv, PROBE_B) - rdl(stv, PROBE_A)) / PROBE_DIV + (PROBE_X ? 200 : 0), 0), 5000) * PROBE_MUL, t_ = rdl(stv, 29); \
    for (int sp_ = 0; sp_ < (1 << 20) && (unsigned)__builtin_amdgcn_s_memrealtime() - t_ < d_; ++sp_) __builtin_amdgcn_s_sleep(2); } } while (0)
DEV unsigned tag_issue(const char* ws) { unsigned e; asm volatile("s_load_dword %0, %1, 0x0" : "=s"(e) : "s"(ws) : "memory"); return e; }
DEV unsigned tag_finish(unsigned e) { unsigned t; asm volatile("s_waitcnt lgkmcnt(0)\n\ts_add_u32 %0, %1, 1" : "=s"(t) : "s"(e) : "memory", "scc"); return t; }

DEV void wait_flags(rsrc_t ws, unsigned voff, unsigned base, unsigned tag) {
  for (int sp = 0; sp < SPIN_CAP; ++sp) {
    CFENCE();
    if (__all(ld4(ws, voff, base) == tag)) break;
    __builtin_amdgcn_s_sleep(1);
  }
}
DEV u4 wait_rec(rsrc_t ws, unsigned voff, unsigned base, unsigned tag) {
  u4 c;
  for (int sp = 0; sp < SPIN_CAP; ++sp) {
    CFENCE();
    c = ld16(ws, voff, base);
    if (__all(c[3] == tag)) break;
    __builtin_amdgcn_s_sleep(1);
  }
  return c;
}

struct RowC { float c[5], cm[5]; f4 c4[5], cm4[5]; };
DEV void rowc_read(RowC& R, unsigned r1, unsigned r4, int i) {
  const int t = i / 3;
  if (i % 3 == 0) { R.c[t] = lds_r<float>(r1 + O_RC + t * 64); R.cm[t] = lds_r<float>(r1 + O_RM + t * 64); }
  else if (i % 3 == 1) R.c4[t] = lds_r<f4>(r4 + O_RC + t * 64);
  else R.cm4[t] = lds_r<f4>(r4 + O_RM + t * 64);
}

DEV void role_a(const Params& P, const int b, const int wv, const int l, unsigned g_tent) {
  const int xcd = b & 7, r = b >> 3;
  const int q = (xcd >> 1) * 30 + r, tile = q >> 2, hg = (xcd & 1) * 4 + (q & 3);
  const int lr = l & 15, g = l >> 4;
  const rsrc_t ws = mk(P.ws, WS_USED);
  const unsigned epoch = tag_issue(P.ws);
  STAMP_INIT();
  STAMP(0);
  if (PROBE_X && b == 0 && wv == 0) st4(ws, g_tent, l == 0 ? 0 : OOB, 2048);
  u4 W[16][3];
  f4 aq[5], ak[5], av[5];
  u2 xp[5];
  RowC R;
  unsigned tag, pm;
  f4 mask;
  u2 wr2;
  const unsigned trb = L_RING + (8 * g + (lr >> 2)) * 128 + (((2 * wv + ((l & 3) >> 1)) ^ ((((lr >> 3) & 1) | ((g & 1) << 1)) << 1)) << 4) + 8 * (l & 1);
  {
    const int par = tile >= 15, p0 = 68 * (tile - 15 * par);
    const rsrc_t rx = mk(P.x, 2048 * 1024), rw = mk(P.wqkv, 512 * 3072), rwo = mk(P.wo, 512 * 1024);
    unsigned xoff[5];
    {
      const int rr = 4 * wv + g;
#pragma unroll
      for (int j = 0; j < 5; ++j) { int p = min(p0 + 15 * j + rr, 1023); xoff[j] = (2 * p + par) * 1024 + ((lr ^ rr) << 4); }
    }
    const u64 xmask = wv == 3 ? 0x0000FFFFFFFFFFFFull : ~0ull;
    const int k8 = l >> 3;
    const unsigned swA = (((l & 7) ^ (((k8 >> 1) & 1) << 1)) << 4) ^ ((wv & 1) << 6), swB = swA ^ 64;
    const unsigned woffA = k8 * 3072 + hg * 128 + swA, woffB = k8 * 3072 + hg * 128 + swB;
    const unsigned lx = wv * 1024, lw = L_RING + wv * 3072;
    unsigned wso[3];
#pragma unroll
    for (int j = 0; j < 3; ++j) { const int i = wv * 3 + j; wso[j] = (i & 3) * 8 * 3072 + (i >> 2) * 1024; }
    auto ldx = [&](int pr) {
#pragma unroll
      for (int j = 0; j < 5; ++j) { setm0(lx, pr * 20480 + j * 4096); dma_go_m(rx, xoff[j], pr * 256, xmask); }
    };
    auto ldw = [&](int s) {
#pragma unroll
      for (int j = 0; j < 3; ++j) dma<0>(rw, (j & 1) ? woffB : woffA, wso[j] + s * 98304, lw, (s % 6) * 12288 + j * 1024);
    };
    asm volatile("global_load_dwordx2 %0, %1, %2" : "=v"(wr2) : "v"((16 * wv + 4 * g) * 2), "s"(P.wr + 64 * hg) : "memory");
    ldx(0); ldw(0); ldw(1);
    unsigned touch;
    {
      int rh = (3 - (int)(((size_t)P.wqkv) >> 7)) & 3, idx = min(r * 52 + wv * 13 + min(l, 12), 1535);
      unsigned off = (idx / 3) * 3072 + (idx % 3) * 1024 + ((xcd & 1) * 4 + rh) * 128;
      asm volatile("buffer_load_dword %0, %1, %2, 0 offen" : "=v"(touch) : "v"(off), "s"(rw) : "memory");
    }
    ldw(2); ldx(1); ldw(3); ldw(4);
    tag = tag_finish(epoch);
    {
      const u4 one = {ONE2, ONE2, ONE2, ONE2};
      const int row = wv * 4 + g;
      lds_w<u4>((row / 5) * 20480 + ((row % 5) * 16 + 15) * 256 + lr * 16, one);
      if (wv == 0) lds_w<u4>(3 * 20480 + ((g + 1) * 16 + 15) * 256 + lr * 16, one);
    }
    const unsigned ooff = tile * TILEB + (((g & 1) * 32 + 4 * hg + wv) * 2 + (g >> 1)) * 256 + lr * 16;
    const unsigned ooff4 = (g & 1) ? OOB : ooff;
    u4 sent = {SENT, SENT, SENT, SENT};
    OPQ(sent);
    const unsigned wooffA = k8 * 1024 + hg * 128 + swA, wooffB = k8 * 1024 + hg * 128 + swB;
    unsigned xa[4];
#pragma unroll
    for (int i = 0; i < 4; ++i) xa[i] = lr * 256 + (((4 * i + g) ^ lr) << 4);
    const unsigned xg = lr * 256 + (((4 * wv + g) ^ lr) << 4);
    const unsigned xr = (hg >> 1) * 20480 + lr * 256 + ((((hg & 1) * 8 + 2 * wv + (g >> 1)) ^ lr) << 4) + 8 * (g & 1);
    unsigned mq = L_MISC + O_RED + (g == (lr >> 2) ? (wv * 80 + lr) * 8 + 4 : 2560 + l * 4), ms = L_MISC + O_RED + (g == 3 ? (wv * 80 + lr) * 8 : 2560 + l * 4);
    unsigned rb_ = L_MISC + O_RED + (wv * 20 + min(l, 19)) * 8, rc_ = L_MISC + (wv * 20 + min(l, 19)) * 4;
    unsigned r1 = L_MISC + lr * 4, r4 = L_MISC + g * 16;
    OPQ(mq); OPQ(ms); OPQ(rb_); OPQ(rc_); OPQ(r1); OPQ(r4);
    const unsigned m1 = lmask(l & 1), m2 = lmask(l & 2);
    pm = lmask(lr < 8 && g >= 2);
#pragma unroll
    for (int i = 0; i < 4; ++i) { const int rk = 4 * g + i, d = (lr < 8 && g >= 2) ? lr + 15 - rk : lr - rk; mask[i] = (d >= 0 && d <= 7 && rk != 15) ? 0.f : -1e30f; }
    OPQ(mask);
    f4 gr[5];
#pragma unroll
    for (int t = 0; t < 5; ++t) { aq[t] = ak[t] = av[t] = gr[t] = (f4){0, 0, 0, 0}; OPQ(aq[t]); OPQ(ak[t]); OPQ(av[t]); OPQ(gr[t]); }
    u4 a[2][5], z[2][5];
    f2 rv[4];
#pragma unroll
    for (int s = 0; s <= 16; ++s) {
      constexpr int NV[17] = {18, 20, 20, 16, 17, 17, 17, 17, 17, 15, 14, 16, 15, 15, 15, 15, 15};
      switch (s) {
#define C_(n) case n: WAITC(NV[n], 0); break;
        C_(0) C_(1) C_(2) C_(3) C_(4) C_(5) C_(6) C_(7) C_(8) C_(9) C_(10) C_(11) C_(12) C_(13) C_(14) C_(15) C_(16)
#undef C_
      }
      BAR();
      if (s == 0) STAMP(1);
      if (s == 4) STAMP(19);
      if (s == 8) STAMP(20);
      if (s == 12) STAMP(21);
      if (s == 16) STAMP(22);
      SB();
      auto gtile = [](int s_, int i) {
        if (s_ < 0) return -1;
        if (s_ < 5) return i == 0 ? s_ : -1;
        if (s_ < 7) return i < 2 ? 8 + 2 * (s_ - 5) + i : -1;
        if (s_ == 7) return i == 0 ? 12 : -1;
        if (s_ == 8) return i < 2 ? 16 + i : -1;
        if (s_ < 12) return i == 0 ? 16 + s_ - 7 : -1;
        if (s_ == 12) return 24 + i;
        return -1;
      };
      const int nx = s < 8 ? ((s & 3) == 0 ? 2 : 1) : 0, nd = s < 16 ? nx + 3 : 0;
#pragma unroll
      for (int i = 0; i < 20; ++i) {
        if (s > 0) {
          const int pb = (s - 1) & 1;
          if (i < 15) {
            const int t = i / 3;
            if (i % 3 == 0) mWA(aq[t], W[s - 1][0], a[pb][t]); else if (i % 3 == 1) mWA(ak[t], W[s - 1][1], a[pb][t]); else mAW(av[t], a[pb][t], W[s - 1][2]);
          } else if (gtile(s - 1, i - 15) >= 0) mVV(gr[gtile(s - 1, i - 15) & 7], z[pb][i - 15], z[pb][i - 15]);
        }
        if (s < 16) {
          if (i < 3) W[s][i] = trfrag2(trb + (s % 6) * 12288 + i * 4096, 512);
          else if (i < 8) a[s & 1][i - 3] = lds_r<u4>(xa[s & 3] + (s >> 2) * 20480 + (i - 3) * 4096);
          else if (i < 13 && gtile(s, i - 8) >= 0) z[s & 1][i - 8] = lds_r<u4>(xg + (gtile(s, i - 8) >> 3) * 20480 + (gtile(s, i - 8) & 7) * 4096);
          if (i >= 3 && (i - 3) / 2 < nd) {
            const int k = (i - 3) / 2, ph = (i - 3) & 1;
            if (k < nx) {
              const int pr = 2 + (s >> 2), j = (s & 3) == 0 ? k : (s & 3) + 1;
              if (ph == 0) setm0(lx, pr * 20480 + j * 4096); else dma_go_m(rx, xoff[j], pr * 256, xmask);
            } else {
              const int j = k - nx;
              if (ph == 0) setm0(lw, ((s + 5) % 6) * 12288 + j * 1024);
              else if (s < 11) dma_go<0>(rw, (j & 1) ? woffB : woffA, wso[j] + (s + 5) * 98304);
              else dma_go<0>(rwo, (j & 1) ? wooffB : wooffA, (((3 * (s - 11) + ((wv * 3 + j) >> 2) + 2 * hg) & 15) * 32 + ((wv * 3 + j) & 3) * 8) * 1024);
            }
          }
          if (s == 10 && i >= 10 && i < 13) st16(ws, sent, i < 12 ? ooff : ooff4, WS_OB + (i - 10) * 32768);
          if (s == 13 && i >= 10 && i < 15) xp[i - 10] = lds_r<u2>(xr + (i - 10) * 4096);
          if (s == 14 && i >= 8 && i < 13) {
            const f4 v = gr[i - 8];
            lds_w<float>(mq + (i - 8) * 128, bself(m2, bself(m1, v[3], v[2]), bself(m1, v[1], v[0])));
            lds_w<float>(ms + (i - 8) * 128, v[3]);
          }
          if (s == 15 && i == 8) { rv[0] = lds_r<f2>(rb_); rv[1] = lds_r<f2>(rb_ + 640); rv[2] = lds_r<f2>(rb_ + 1280); rv[3] = lds_r<f2>(rb_ + 1920); }
          if (s == 15 && i == 12) {
            const f2 v = (rv[0] + rv[1]) + (rv[2] + rv[3]);
            const float m = v[0] * (1.f / 512), rs = rsq(fmx(v[1] * (1.f / 512) - m * m, 0.f) + EPS);
            lds_w<float>(rc_ + O_RC, rs); lds_w<float>(rc_ + O_RM, rs * m); lds_w<float>(rc_ + O_RA, 1.f); lds_w<float>(rc_ + O_RB, 0.f);
          }
        } else {
          if (i < 15) rowc_read(R, r1, r4, i);
          if (i == 0 && wv == 0) st4(ws, tag, l == 0 ? 0 : OOB, WS_FA + (tile * 8 + hg) * 128);
          if (i == 2) dma<0>(rwo, wooffA, (((15 + 2 * hg) & 15) * 32 + wv * 8) * 1024, lx, L_RING + 3 * 12288);
          if (i >= 4 && i < 7) st16(ws, sent, i < 6 ? ooff : ooff4, WS_OB + BUFB + (i - 4) * 32768);
        }
        SB();
      }
    }
    asm volatile("" ::"v"(touch));
    OPQ(wr2);
  }
  NOPS(2);
  SB();
  f4 csq, csk;
#pragma unroll
  for (int i = 0; i < 4; ++i) { csq[i] = dppf<0x15F>(aq[0][i]); csk[i] = dppf<0x15F>(ak[0][i]); }
  float csv;
  { f2 x = swap32(av[0][3], av[0][3]); f2 y = swap16(x[1], x[1]); csv = y[1]; }
  STAMP(2);
  u4 WO[16];

#pragma unroll 1
  for (int L = 0; L < 2; ++L) {
#define FRESH(n) int l##n = l, wv##n = wv, hg##n = hg, tile##n = tile; asm volatile("" : "+v"(l##n)); asm volatile("" : "+s"(wv##n), "+s"(hg##n), "+s"(tile##n)); \
    const int lr##n = l##n & 15, g##n = l##n >> 4; (void)lr##n; (void)g##n;
    FRESH(1)
    const unsigned ob = WS_OB + L * BUFB + tile1 * TILEB;
    unsigned fa;
    {
      u2 kb[5], vb[5], qb[5], o[5];
      f4 ss[5], sp[5], sc[5];
      float mx[5];
      auto scale = [&](int t) {
        qb[t] = pk4(aq[t] * R.c[t] - csq * R.cm[t]);
        kb[t] = pk4(ak[t] * R.c[t] - csk * R.cm[t]);
        vb[t] = pk4(av[t] * R.c4[t] - R.cm4[t] * csv);
      };
      auto smm = [&](int t) { ss[t] = m16c(kb[t], qb[t], mask); if (t) sp[t] = m16c(kb[t - 1], qb[t], mask); };
      auto pick = [&](int t) {
        sc[t] = bsel4(pm, t ? sp[t] : (f4){-1e30f, -1e30f, -1e30f, -1e30f}, ss[t]);
        mx[t] = fmx(fmx(fmx(sc[t][0], sc[t][1]), sc[t][2]), sc[t][3]);
      };
      scale(0); SB();
      scale(1); SB(); smm(0); SB();
      scale(2); SB(); smm(1); SB();
      scale(3); SB(); smm(2); SB();
      scale(4); SB(); smm(3); smm(4); SB();
      pick(0); pick(1); pick(2); SB();
      pick(3); pick(4);
      {
        f2 x = swap16(mx[0], mx[1]), y = swap16(mx[2], mx[3]), w = swap16(mx[4], mx[4]);
        f2 u = swap32(fmx(x[0], x[1]), fmx(y[0], y[1]));
        const float w1 = fmx(w[0], w[1]), c = fmx(u[0], u[1]) * 0.36067376f;
        f2 w2 = swap32(w1, w1), v = swap32(c, c);
        f2 v0 = swap16(v[0], v[0]), v1 = swap16(v[1], v[1]);
        mx[0] = v0[0]; mx[1] = v0[1]; mx[2] = v1[0]; mx[3] = v1[1]; mx[4] = fmx(w2[0], w2[1]) * 0.36067376f;
      }
      asm volatile("buffer_load_dword %0, %1, %2, %3 offen sc1" : "=v"(fa) : "v"((l1 & 7) * 128), "s"(ws), "s"(WS_FA + tile1 * 1024) : "memory");
      u2 one2 = {ONE2, ONE2};
      float kap = 0.36067376f;
      OPQ(one2); OPQ(kap);
      u2 pb[5], ps[5], pp[5];
      f4 sum[5], oa[5];
      auto expo = [&](int t) {
        const f4 d = sc[t] * kap - mx[t];
        f4 e;
#pragma unroll
        for (int i = 0; i < 4; ++i) e[i] = __builtin_amdgcn_exp2f(d[i]);
        pb[t] = pk4(e);
        if (t) { pp[t] = pb[t] & pm; ps[t] = pb[t] ^ pp[t]; } else ps[t] = pb[t];
      };
      auto pv = [&](int t) { m16<true>(oa[t], vb[t], ps[t]); if (t) m16(oa[t], vb[t - 1], pp[t]); m16<true>(sum[t], one2, pb[t]); };
      auto norm = [&](int t) { KEEP(sum[t]); o[t] = pk4(oa[t] * __builtin_amdgcn_rcpf(sum[t][0])); };
      const unsigned go = (((g1 & 1) * 32 + 4 * hg1 + wv1) * 2 + (g1 >> 1)) * 256 + lr1 * 16;
      const unsigned lo_ = (((g1 & 1) * 32 + wv1) * 2 + (g1 >> 1)) * 256 + lr1 * 16;
      auto put = [&](int i) {
        u2 A = o[2 * i], B = o[i < 2 ? 2 * i + 1 : 4];
        auto s0 = __builtin_amdgcn_permlane16_swap(A[0], B[0], false, false); auto s1 = __builtin_amdgcn_permlane16_swap(A[1], B[1], false, false);
        const u4 e = {umin(s0[0], SENT - 1), s1[0], s0[1], s1[1]};
        st16(ws, e, (i == 2 && (g1 & 1)) ? OOB : go, ob + i * 32768);
        lds_w<u4>(i < 2 ? lo_ + i * 32768 : ((g1 & 1) ? lo_ + 2048 : lo_ + 65536), e);
      };
      SB();
      expo(0); SB();
      expo(1); SB(); pv(0); SB();
      expo(2); SB(); pv(1); SB();
      expo(3); SB(); pv(2); SB(); norm(0); SB();
      expo(4); SB(); pv(3); SB(); norm(1); SB(); pv(4); SB();
      put(0); norm(2); SB(); norm(3); SB();
      put(1); norm(4); SB();
      put(2);
    }
    STAMP(3 + 8 * L);
    FRESH(2)
    const unsigned l16 = l2 * 16;
    unsigned l16h = l16 + 65536, r1 = L_MISC + lr2 * 4;
    OPQ(l16h); OPQ(r1);
    f4 acc[5];
#pragma unroll
    for (int t = 0; t < 5; ++t) acc[t] = up4(xp[t]) * lds_r<float>(r1 + O_RA + t * 64) + lds_r<float>(r1 + O_RB + t * 64);
    WAITC(3, 0);
    OPQ(fa);
    BAR();
    if (L == 0) {
      unsigned tb = trb;
      OPQ(tb);
#pragma unroll
      for (int j = 0; j < 16; ++j) { WO[j] = trfrag2(tb + ((4 + j / 3) % 6) * 12288 + (j % 3) * 4096, 512); asm volatile("" : "+a"(WO[j])); }
      if (!__all(fa == tag)) wait_flags(ws, (l2 & 7) * 128, WS_FA + tile2 * 1024, tag);
    }
    u4 a[3][5];
    auto rda = [&](int j, int t) { a[j % 3][t] = lds_r<u4>((t < 4 ? l16 + t * 16384 : l16h) + j * 1024); };
#pragma unroll
    for (int t = 0; t < 5; ++t) { rda(0, t); rda(1, t); }
    const unsigned lw2 = wv2 * 1024;
    SB();
#pragma unroll
    for (int i = 0; i < 3; ++i) {
      const unsigned so = ob + ((2 * hg2 + 2 + 4 * i + wv2) & 15) * 1024;
#pragma unroll
      for (int t = 0; t < 5; ++t) {
        if (i == 0) { setm0(lw2, 2048 + t * 16384); SB(); mWA(acc[t], WO[0], a[0][t]); SB(); dma_go<1>(ws, l16, so + t * 16384); }
        else dma<1>(ws, l16, so + t * 16384, lw2, (2 + 4 * i) * 1024 + t * 16384);
      }
    }
    {
      const unsigned so = ob + ((2 * hg2 + 14 + (wv2 & 1)) & 15) * 1024;
#pragma unroll
      for (int i = 0; i < 3; ++i) { const unsigned t = min((wv2 >> 1) * 3 + i, 4); dma<1>(ws, l16, so + t * 16384, (wv2 & 1) * 1024 + t * 16384, 14 * 1024); }
    }
    STAMP(4 + 8 * L);
    auto check = [&](int j) {
      u4 c0 = a[j % 3][0], c1 = a[j % 3][1], c2 = a[j % 3][2], c3 = a[j % 3][3], c4 = a[j % 3][4];
      unsigned m = max(max(max(c0[0], c1[0]), max(c2[0], c3[0])), c4[0]);
      if (__builtin_expect(__any(m == SENT), 0)) {
        const unsigned so = ob + ((2 * hg2 + j) & 15) * 1024;
        for (int sp = 0; sp < SPIN_CAP; ++sp) {
          CFENCE();
          c0 = ld16(ws, l16, so); c1 = ld16(ws, l16, so + 16384); c2 = ld16(ws, l16, so + 32768); c3 = ld16(ws, l16, so + 49152); c4 = ld16(ws, l16, so + 65536);
          m = max(max(max(c0[0], c1[0]), max(c2[0], c3[0])), c4[0]);
          if (!__any(m == SENT)) break;
          __builtin_amdgcn_s_sleep(1);
        }
        a[j % 3][0] = c0; a[j % 3][1] = c1; a[j % 3][2] = c2; a[j % 3][3] = c3; a[j % 3][4] = c4;
        NOPS(2);
      }
    };
    WAITV(13);
    BAR();
#pragma unroll
    for (int t = 0; t < 5; ++t) rda(2, t);
    SB();
#pragma unroll
    for (int j = 1; j < 16; ++j) {
      if (j == 4) WAITV(8); else if (j == 8) WAITV(3); else if (j == 12) WAITV(0);
      if (j == 4 || j == 8 || j == 12) BAR();
      SB();
#pragma unroll
      for (int t = 0; t < 5; ++t) { mWA(acc[t], WO[j], a[j % 3][t]); if (j + 2 < 16) rda(j + 2, t); SB(); }
      if (j + 1 < 16) check(j + 1);
      SB();
    }
    STAMP(5 + 8 * L);
    FRESH(3)
    const unsigned xb = WS_XB + L * BUFB + tile3 * TILEB;
    const int row3 = wv3 < 2 ? wv3 * 24 + min(l3, 23) : 16 + wv3 * 16 + min(l3, 15);
    const bool rowok = l3 < (wv3 < 2 ? 24 : 16);
    unsigned rw_ = L_RED2 + wv3 * 2048 + l3 * 16, rr_ = L_RED2 + row3 * 16, rc_ = L_MISC + row3 * 4;
    OPQ(rw_); OPQ(rr_); OPQ(rc_);
    {
      u2 spa[4]; float spf[4];
#pragma unroll
      for (int t = 0; t < 4; ++t) { spa[t] = lr3 == 4 * t ? (u2){ONE2, ONE2} : lr3 == 4 * t + 1 ? wr2 : (u2){0, 0}; spf[t] = lr3 == 4 * t + 2 ? 1.f : 0.f; }
      SB();
      float q2[5];
      f4 da, db, ea, eb;
#pragma unroll
      for (int t = 0; t < 5; ++t) {
        xp[t] = pk4(acc[t]);
        const f2 h = (f2){acc[t][0], acc[t][1]} * (f2){acc[t][0], acc[t][1]} + (f2){acc[t][2], acc[t][3]} * (f2){acc[t][2], acc[t][3]};
        q2[t] = h[0] + h[1];
        SB();
        if (t == 1) { m16<true>(da, spa[0], xp[0]); mf32<true>(db, spf[0], q2[0]); }
        else if (t > 1) { m16(da, spa[t - 1], xp[t - 1]); mf32(db, spf[t - 1], q2[t - 1]); }
        SB();
      }
      m16<true>(ea, spa[0], xp[4]); mf32<true>(eb, spf[0], q2[4]);
      SB();
      NOPS(8);
      SB();
      KEEP(da); KEEP(db);
      lds_w<f4>(rw_, (f4){da[0], da[1], db[2], 0.f});
      SB();
      KEEP(ea); KEEP(eb);
      lds_w<f4>(rw_ + 1024, (f4){ea[0], ea[1], eb[2], 0.f});
    }
    WAITL();
    BAR();
    {
      u4 w = asu((lds_r<f4>(rr_) + lds_r<f4>(rr_ + 2048)) + (lds_r<f4>(rr_ + 4096) + lds_r<f4>(rr_ + 6144)));
      w[3] = tag;
      st16(ws, w, rowok ? row3 * 16 : OOB, WS_ST + L * STB + (tile3 * 8 + hg3) * 1280);
      STAMP(6 + 8 * L);
      const unsigned go = (((g3 & 1) * 32 + 4 * hg3 + wv3) * 2 + (g3 >> 1)) * 256 + lr3 * 16;
#pragma unroll
      for (int i = 0; i < 3; ++i) {
        u2 A = xp[2 * i], B = xp[i < 2 ? 2 * i + 1 : 4];
        auto s0 = __builtin_amdgcn_permlane16_swap(A[0], B[0], false, false); auto s1 = __builtin_amdgcn_permlane16_swap(A[1], B[1], false, false);
        st16(ws, (u4){s0[0], s1[0], s0[1], s1[1]}, (i == 2 && (g3 & 1)) ? OOB : go, xb + i * 32768);
      }
    }
    WAITV(0);
    BAR();
    if (wv3 == 0) st4(ws, tag, l3 == 0 ? 0 : OOB, WS_FX + L * 32768 + (tile3 * 8 + hg3) * 128);
    const int par3 = tile3 >= 15, ti3 = tile3 - 15 * par3, p03 = 68 * ti3;
    const bool halo = L == 0 && ti3 > 0;
    wait_flags(ws, g3 == 1 ? WS_FF + lr3 * 128 : WS_FX + L * 32768 + ((tile3 - (halo ? (lr3 >> 3) : 0)) * 8 + (l3 & 7)) * 128, 0, tag);
    STAMP(7 + 8 * L);
    {
      const unsigned so = (halo && row3 < 7) ? ((tile3 - 1) * 640 + 72 + row3) * 16 : (tile3 * 640 + row3) * 16;
      u4 v[8];
#pragma unroll
      for (int i = 0; i < 8; ++i) v[i] = ld16(ws, so, WS_ST + L * STB + i * 1280);
      if (L == 0) {
        const unsigned g1_ = l3 * 16 + 16384, g0_ = g1_ - ((halo && lr3 < 7) ? 16256 : 0);
#pragma unroll
        for (int i = 0; i < 4; ++i)
#pragma unroll
          for (int t = 0; t < 5; ++t) dma<1>(ws, t ? g1_ : g0_, xb - 16384 + wv3 * 1024 + t * 16384 + i * 4096, wv3 * 1024, t * 16384 + i * 4096);
      }
      const f4 st = ((asf(v[0]) + asf(v[1])) + (asf(v[2]) + asf(v[3]))) + ((asf(v[4]) + asf(v[5])) + (asf(v[6]) + asf(v[7])));
      const float m = st[0] * (1.f / 512), var = fmx(st[2] * (1.f / 512) - m * m, 0.f), rs = rsq(var + EPS), a1 = 1.f + rs;
      const float c = a1 * rsq(var * a1 * a1 + EPS);
      lds_w<float>(rc_ + O_RC, c); lds_w<float>(rc_ + O_RM, c * m); lds_w<float>(rc_ + O_RA, a1); lds_w<float>(rc_ + O_RB, -m * rs);
    }
    FRESH(4)
    unsigned q1 = L_MISC + lr4 * 4, q4 = L_MISC + g4 * 16;
    OPQ(q1); OPQ(q4);
    if (L == 0) {
      unsigned zl = l4 * 16, zh = zl + 65536;
      OPQ(zh);
      WAITC(0, 0);
      BAR();
      STAMP(26);
      {
        u4 z[2][5];
#pragma unroll
        for (int t = 0; t < 5; ++t) z[0][t] = lds_r<u4>(t < 4 ? zl + t * 16384 : zh);
        SB();
#pragma unroll
        for (int s = 0; s < 16; ++s) {
#pragma unroll
          for (int t = 0; t < 5; ++t) {
            if (s == 0) mWA<true>(aq[t], W[s][0], z[s & 1][t]); else mWA(aq[t], W[s][0], z[s & 1][t]);
            if (s < 15) z[(s + 1) & 1][t] = lds_r<u4>((t < 4 ? zl + t * 16384 : zh) + (s + 1) * 1024); else rowc_read(R, q1, q4, 3 * t);
            SB();
            if (s == 0) mWA<true>(ak[t], W[s][1], z[s & 1][t]); else mWA(ak[t], W[s][1], z[s & 1][t]);
            if (s == 15) rowc_read(R, q1, q4, 3 * t + 1);
            SB();
            if (s == 0) mAW<true>(av[t], z[s & 1][t], W[s][2]); else mAW(av[t], z[s & 1][t], W[s][2]);
            if (s == 15) rowc_read(R, q1, q4, 3 * t + 2);
            SB();
          }
        }
      }
      STAMP(8);
      const u4 sel = wait_rec(ws, (l4 & 1) * 16, WS_SEL + (b & 15) * 128, tag);
      STAMP(9);
#pragma unroll 1
      for (int i = 0; i < 2; ++i) {
        const unsigned key = rdl(sel[0], i);
        const int tok = 2047 - (key & 2047), u = (tok >> 1) - p03;
        if ((tok & 1) != par3 || u < 0 || u >= 75) continue;
        FRESH(5)
        const float s_m = __uint_as_float(rdl(sel[1], i)), s_rs = __uint_as_float(rdl(sel[2], i));
        const float sg = sigm(key_score(key)), a1 = 1.f + s_rs, b1 = -s_m * s_rs;
        const int ts = u / 15, rs_ = u - 15 * ts;
        unsigned yb = L_RING + l5 * 32, rb = L_RING + 8192 + wv5 * 1024, zb = rb + g5 * 16;
        OPQ(yb); OPQ(zb);
        const u4 xr = lds_r<u4>((ts * 16 + (l5 >> 2)) * 1024 + (l5 & 3) * 256 + rs_ * 16);
        const f4 x0 = up4((u2){xr[0], xr[1]}) * a1 + b1, x1 = up4((u2){xr[2], xr[3]}) * a1 + b1;
        const unsigned me = lmask(lr5 == rs_);
        BAR();
        u4 y[8];
        for (int sp = 0; sp < SPIN_CAP; ++sp) {
          CFENCE();
          unsigned m = 0;
#pragma unroll
          for (int j = 0; j < 8; ++j) { y[j] = ld16(ws, l5 * 32 + (j & 1) * 16, WS_YP + (4 * wv5 + (j >> 1)) * 4096 + i * 2048); m = max(m, y[j][0]); }
          if (!__any(m == SENT)) break;
          __builtin_amdgcn_s_sleep(1);
        }
        STAMP(27);
        lds_w<f4>(yb + wv5 * 2048, (asf(y[0]) + asf(y[2])) + (asf(y[4]) + asf(y[6])));
        lds_w<f4>(yb + wv5 * 2048 + 16, (asf(y[1]) + asf(y[3])) + (asf(y[5]) + asf(y[7])));
        WAITL();
        BAR();
        const f4 y0 = (lds_r<f4>(yb) + lds_r<f4>(yb + 2048)) + (lds_r<f4>(yb + 4096) + lds_r<f4>(yb + 6144));
        const f4 y1 = (lds_r<f4>(yb + 16) + lds_r<f4>(yb + 2048 + 16)) + (lds_r<f4>(yb + 4096 + 16) + lds_r<f4>(yb + 6144 + 16));
        const u2 z0 = pk4(y0 * sg + x0), z1 = pk4(y1 * sg + x1);
        lds_w<u4>(rb + l5 * 16, (u4){z0[0], z0[1], z1[0], z1[1]});
        f4 tq, tk, tv;
        u4 z[16];
#pragma unroll
        for (int s = 0; s < 4; ++s) z[s] = lds_r<u4>(zb + s * 64);
        const u2 zx = lds_r<u2>(rb + (64 * hg5 + 16 * wv5 + 4 * g5) * 2);
        SB();
#pragma unroll
        for (int s = 0; s < 16; ++s) {
          if (s == 0) mWA<true>(tq, W[s][0], z[s]); else mWA(tq, W[s][0], z[s]);
          if (s + 4 < 16) z[s + 4] = lds_r<u4>(zb + (s + 4) * 64);
          SB();
          if (s == 0) { mWA<true>(tk, W[s][1], z[s]); mAW<true>(tv, z[s], W[s][2]); } else { mWA(tk, W[s][1], z[s]); mAW(tv, z[s], W[s][2]); }
          SB();
        }
        const f4 f0 = up4(z0), f1 = up4(z1), fs = f0 + f1, fq = f0 * f0 + f1 * f1;
        const float m2 = wave_sum((fs[0] + fs[1]) + (fs[2] + fs[3])) * (1.f / 512), q2 = wave_sum((fq[0] + fq[1]) + (fq[2] + fq[3])) * (1.f / 512);
        const float r2 = rsq(fmx(q2 - m2 * m2, 0.f) + EPS), rm2 = r2 * m2;
        SB();
        if (l5 == 0) { const unsigned ra = L_MISC + (ts * 16 + rs_) * 4; lds_w<float>(ra + O_RA, 1.f); lds_w<float>(ra + O_RB, 0.f); }
#pragma unroll
        for (int t = 0; t < 5; ++t)
          if (t == ts) {
            aq[t] = bsel4(me, tq, aq[t]); ak[t] = bsel4(me, tk, ak[t]);
            xp[t] = (u2){bsel(me, zx[0], xp[t][0]), bsel(me, zx[1], xp[t][1])};
            R.c[t] = bself(me, r2, R.c[t]); R.cm[t] = bself(me, rm2, R.cm[t]);
#pragma unroll
            for (int e = 0; e < 4; ++e) {
              const bool mine = 4 * g5 + e == rs_;
              av[t][e] = mine ? tv[e] : av[t][e]; R.c4[t][e] = mine ? r2 : R.c4[t][e]; R.cm4[t][e] = mine ? rm2 : R.cm4[t][e];
            }
          }
      }
      WAITL();
      BAR();
      STAMP(10);
    } else {
      const rsrc_t ro = mk(P.out, 2048 * 1024);
      WAITL();
      BAR();
#pragma unroll
      for (int t = 0; t < 5; ++t) {
        const float ra = lds_r<float>(q1 + O_RA + t * 64), rb = lds_r<float>(q1 + O_RB + t * 64);
        lds_w<u2>((16 * t + lr4) * 144 + wv4 * 32 + g4 * 8, pk4(up4(xp[t]) * ra + rb));
      }
      WAITL();
      BAR();
#pragma unroll
      for (int i = 0; i < 3; ++i) {
        const int row = min((i * 4 + wv4) * 8 + (l4 >> 3), 79), u = row - (row >> 4), p = p03 + u;
        const bool ok = (i * 4 + wv4) < 10 && (row & 15) != 15 && p < 1024 && (ti3 == 0 || u >= 7);
        st16(ro, lds_r<u4>(row * 144 + (l4 & 7) * 16), ok ? (2 * p + par3) * 1024 + hg4 * 128 + (l4 & 7) * 16 : OOB, 0);
      }
      STAMP(16);
      WAITV(0);
      BAR();
      if (wv4 < 2) {
        const u4 sel = wait_rec(ws, wv4 * 16, WS_SEL + 2048 + (b & 15) * 128, tag);
        STAMP(17);
        const unsigned key = rfl(sel[0]);
        const int tok = 2047 - (key & 2047), u = (tok >> 1) - p03;
        if ((tok & 1) == par3 && u >= (ti3 ? 7 : 0) && u < 75) {
          const f4 base = up4(lds_r<u2>((u + u / 15) * 144 + lr4 * 8));
          const float sg = sigm(key_score(key));
          u4 y[4];
          for (int sp = 0; sp < SPIN_CAP; ++sp) {
            CFENCE();
            unsigned m = 0;
#pragma unroll
            for (int j = 0; j < 4; ++j) { y[j] = ld16(ws, g4 * 4096 + lr4 * 16, WS_YP + 65536 + j * 16384 + wv4 * 2048 + hg4 * 256); m = max(m, y[j][0]); }
            if (!__any(m == SENT)) break;
            __builtin_amdgcn_s_sleep(1);
          }
          const f4 s = (asf(y[0]) + asf(y[1])) + (asf(y[2]) + asf(y[3]));
          const f2 x = swap16(s[0], s[1]), w = swap16(s[2], s[3]), v = swap32(x[0] + x[1], w[0] + w[1]);
          const float e = v[0] + v[1];
          const f2 v2 = swap32(e, e), e01 = swap16(v2[0], v2[0]), e23 = swap16(v2[1], v2[1]);
          __builtin_amdgcn_raw_buffer_store_b64(pk4((f4){e01[0], e01[1], e23[0], e23[1]} * sg + base), ro, l4 < 16 ? tok * 1024 + hg4 * 128 + lr4 * 8 : OOB, 0, 16);
        }
        STAMP(18);
      }
    }
  }
  STAMP_FLUSH();
}

DEV void role_f(const Params& P, const int j, const int wv, const int l, unsigned g_tent) {
  const int lr = l & 15, g = l >> 4, tid = wv * 64 + l;
  const rsrc_t ws = mk(P.ws, WS_USED);
  const u64 t0 = __builtin_amdgcn_s_memrealtime();
  STAMP_INIT();
  STAMP(0);
  const unsigned tag = tag_finish(tag_issue(P.ws));
  const u4 sent = {SENT, SENT, SENT, SENT};
  st16(ws, sent, j * 4096 + tid * 16, WS_YP); st16(ws, sent, j * 4096 + tid * 16, WS_YP + 65536);
  float wsum;
  { u4 w = *(const u4*)(P.wr + l * 8); f4 a = up4((u2){w[0], w[1]}) + up4((u2){w[2], w[3]}); wsum = wave_sum((a[0] + a[1]) + (a[2] + a[3])); }
  WAITV(0);
  BAR();
  if (wv == 0) st4(ws, tag, l == 0 ? 0 : OOB, WS_FF + j * 128);
  for (int sp = 0; sp < 4096 && __builtin_amdgcn_s_memrealtime() - t0 < FDELAY; ++sp) __builtin_amdgcn_s_sleep(4);
  const int noct = j < 12 ? 11 : 10, f0 = 8 * (j < 12 ? 11 * j : 132 + 10 * (j - 12));
  u4 G[16][3], Dn[3][8];
  {
    const rsrc_t rgu = mk(wv < 2 ? P.wg : P.wu, 512 * 2752), rd = mk(P.wd, 1376 * 1024);
    unsigned go[6];
#pragma unroll
    for (int i = 0; i < 6; ++i) { const int n = 64 * ((wv & 1) * 6 + i) + l, row = n / 12; go[i] = row * 2752 + f0 * 2 + (n - 12 * row) * 16; }
    const unsigned dof = (f0 + g) * 1024 + wv * 256 + lr * 16;
    auto issue = [&](int rd_) {
      if (rd_ < 8) {
#pragma unroll
        for (int i = 0; i < 6; ++i) dma<0>(rgu, go[i], rd_ * 64 * 2752, wv * 6144, (rd_ & 3) * 32768 + i * 1024);
      } else {
#pragma unroll
        for (int i = 0; i < 8; ++i) dma<0>(rd, dof, (32 * (rd_ - 8) + 4 * i) * 1024, wv * 8192, (rd_ & 3) * 32768 + i * 1024);
      }
    };
    unsigned tg = ((l >> 1) & 1) * 12288 + (8 * g + (lr >> 2)) * 192 + wv * 48 + (l & 1) * 8, td = wv * 8192 + (8 * g + (lr >> 2)) * 256 + (l & 3) * 8;
    issue(0); issue(1); issue(2);
#pragma unroll
    for (int rd_ = 0; rd_ < 11; ++rd_) {
      if (rd_ < 6) WAITC(12, 0); else if (rd_ == 6) WAITC(14, 0); else if (rd_ < 9) WAITC(16, 0); else if (rd_ == 9) WAITC(8, 0); else WAITC(0, 0);
      BAR();
      if (rd_ + 3 < 11) issue(rd_ + 3);
      unsigned bg = tg + (rd_ & 3) * 32768, bd = td + (rd_ & 3) * 32768;
      OPQ(bg); OPQ(bd);
      if (rd_ < 8) {
#pragma unroll
        for (int ks = 0; ks < 2; ++ks)
#pragma unroll
          for (int c = 0; c < 3; ++c) G[2 * rd_ + ks][c] = trfrag2(bg + ks * 32 * 192 + c * 16, 4 * 192);
      } else {
#pragma unroll
        for (int c = 0; c < 8; ++c) Dn[rd_ - 8][c] = trfrag2(bd + c * 32, 4 * 256);
      }
    }
    WAITL();
    BAR();
  }
  constexpr unsigned F_ACT = 0;
  const unsigned mg1 = lmask(g == 1), mg2 = lmask(g >= 2), mlo = lmask(lr < 8);
  auto gather = [&](const f4& a0, const f4& a1, const f4& a2, float& gv, float& uv) {
    KEEP(a0); KEEP(a1); KEEP(a2);
    const float d0 = bself(mg2, a2[0], bself(mg1, a1[0], a0[0])), d1 = bself(mg2, a2[1], bself(mg1, a1[1], a0[1]));
    gv = __uint_as_float(__builtin_amdgcn_update_dpp(__float_as_uint(d0), __float_as_uint(d1), 0x128, 0xF, 0xC, false));
    uv = __uint_as_float(__builtin_amdgcn_update_dpp(__float_as_uint(d1), __float_as_uint(d0), 0x128, 0xF, 0x3, false));
  };
  float csg, csu;
  {
    u4 one = {ONE2, ONE2, ONE2, ONE2};
    OPQ(one);
    NOPS(2);
    SB();
    f4 a[3];
#pragma unroll
    for (int ks = 0; ks < 16; ++ks)
#pragma unroll
      for (int c = 0; c < 3; ++c) { if (ks == 0) mAW<true>(a[c], one, G[ks][c]); else mAW(a[c], one, G[ks][c]); }
    SB();
    NOPS(8);
    SB();
    gather(a[0], a[1], a[2], csg, csu);
  }
  const bool live = g < 3 && 3 * wv + g < noct;
  const unsigned actw = F_ACT + (g < 3 ? (lr >> 3) * 192 + (24 * wv + 8 * g + (l & 7)) * 2 : 512 + l * 2);
  STAMP(1);
  const int tok = 32 * (4 * j + wv) + (l & 31);
  unsigned sto;
  { const int p = tok >> 1, ti = max(p - 7, 0) / 68, u = p - 68 * ti; sto = ((((tok & 1) * 15 + ti) * 8 + 4 * (l >> 5)) * 80 + u + u / 15) * 16; }

#pragma unroll 1
  for (int L = 0; L < 2; ++L) {
    u4 v[4];
    for (int sp = 0; sp < SPIN_CAP; ++sp) {
      CFENCE();
#pragma unroll
      for (int i = 0; i < 4; ++i) v[i] = ld16(ws, sto, WS_ST + L * STB + i * 1280);
      if (__all(v[0][3] == tag && v[1][3] == tag && v[2][3] == tag && v[3][3] == tag)) break;
      __builtin_amdgcn_s_sleep(1);
    }
    STAMP(2 + 8 * L);
    {
      const f4 s = (asf(v[0]) + asf(v[1])) + (asf(v[2]) + asf(v[3]));
      const float m = xadd32(s[0]) * (1.f / 512), dt = xadd32(s[1]), qq = xadd32(s[2]) * (1.f / 512);
      const float rs = rsq(fmx(qq - m * m, 0.f) + EPS), sc = (dt - m * wsum) * rs;
      unsigned u = __float_as_uint(sc); u ^= (unsigned)((int)u >> 31) | 0x80000000u;
      const unsigned key = (u & 0xFFFFF800u) | (2047u - tok);
      const unsigned k1 = max_x16(max_row16(key)), k2 = max_x16(max_row16(key == k1 ? 0u : key));
      st16(ws, (u4){key, __float_as_uint(m), __float_as_uint(rs), tag}, l >= 32 ? OOB : key == k1 ? 0 : key == k2 ? 16 : OOB, WS_CAND + L * 8192 + (4 * j + wv) * 128);
    }
    STAMP(3 + 8 * L);
    u4 c0, c1;
    for (int sp = 0; sp < SPIN_CAP; ++sp) {
      CFENCE();
      c0 = ld16(ws, l * 128, WS_CAND + L * 8192); c1 = ld16(ws, l * 128 + 16, WS_CAND + L * 8192);
      const unsigned f = ld4(ws, (wv * 60 + min(l, 59)) * 128, WS_FX + L * 32768);
      if (__all(c0[3] == tag && c1[3] == tag && f == tag)) break;
      __builtin_amdgcn_s_sleep(1);
    }
    BAR();
    STAMP(4 + 8 * L);
    const unsigned k1 = rfl(max_x32(max_x16(max_row16(c0[0]))));
    const bool w1 = c0[0] == k1;
    const u4 cb = w1 ? c1 : c0;
    const unsigned k2 = rfl(max_x32(max_x16(max_row16(cb[0]))));
    u4 h[16];
    {
      const int t = 2047 - (((l & 1) ? k2 : k1) & 2047), p = t >> 1, ti = max(p - 7, 0) / 68, u = p - 68 * ti, ts = u / 15;
      const unsigned vo = ((t & 1) * 15 + ti) * TILEB + ts * 16384 + g * 256 + (u - 15 * ts) * 16;
#pragma unroll
      for (int ks = 0; ks < 16; ++ks) h[ks] = ld16(ws, vo + (ks & 3) * 1024, WS_XB + L * BUFB + (ks >> 2) * 4096);
    }
    SB();
    const int s1 = __builtin_ctzll(__ballot(w1) | (1ull << 63)), s2 = __builtin_ctzll(__ballot(cb[0] == k2) | (1ull << 63));
    const unsigned ma = rdl(c0[1], s1), ra = rdl(c0[2], s1), mb = rdl(cb[1], s2), rb = rdl(cb[2], s2);
    if (wv == 0) st16(ws, (l & 1) ? (u4){k2, mb, rb, tag} : (u4){k1, ma, ra, tag}, l < 2 ? l * 16 : OOB, WS_SEL + L * 2048 + j * 128);
    const float m = __uint_as_float(bsel(mlo, ma, mb)), rs = __uint_as_float(bsel(mlo, ra, rb));
    SB();
    {
      f4 a[3];
#pragma unroll
      for (int ks = 0; ks < 16; ++ks)
#pragma unroll
        for (int c = 0; c < 3; ++c) { if (ks == 0) mAW<true>(a[c], h[ks], G[ks][c]); else mAW(a[c], h[ks], G[ks][c]); }
      SB();
      NOPS(8);
      SB();
      float gg, uu;
      gather(a[0], a[1], a[2], gg, uu);
      gg = (gg - m * csg) * rs; uu = (uu - m * csu) * rs;
      const float act = live ? gg * sigm(gg) * uu : 0.f;
      lds_w<u16>(actw, (u16)pk(act, act));
    }
    STAMP(5 + 8 * L);
    WAITL();
    BAR();
    {
      u4 b[3];
#pragma unroll
      for (int ks = 0; ks < 3; ++ks) b[ks] = lds_r<u4>(F_ACT + (l & 1) * 192 + ks * 64 + g * 16);
      const unsigned m2 = lmask(l & 2);
      const unsigned vo = lr < 4 ? j * 4096 + (l & 1) * 2048 + wv * 512 + (l & 2) * 32 + g * 16 : OOB;
      f4 y[8];
      SB();
#pragma unroll
      for (int c = 0; c < 8; ++c) { mWA<true>(y[c], Dn[0][c], b[0]); mWA(y[c], Dn[1][c], b[1]); mVV(y[c], Dn[2][c], b[2]); }
      SB();
#pragma unroll
      for (int c = 0; c < 4; ++c) {
        if (c == 0) { NOPS(1); SB(); }
        u4 w = asu(bsel4(m2, y[2 * c + 1], y[2 * c]));
        w[0] = umin(w[0], SENT - 1);
        st16(ws, w, vo, WS_YP + L * 65536 + c * 128);
        SB();
      }
    }
    STAMP(6 + 8 * L);
  }
  if (j == 0 && wv == 0) st4(ws, tag, l == 0 ? 0 : OOB, WS_EPOCH);
  STAMP_FLUSH();
}

__global__ void __launch_bounds__(256, 1) fused_kernel(Params P) {
  unsigned g_tent = 0;
  u64 tent; asm volatile("s_memrealtime %0\n\ts_waitcnt lgkmcnt(0)" : "=s"(tent) :: "memory");
  g_tent = (unsigned)tent;
  const int b = blockIdx.x, wv = __builtin_amdgcn_readfirstlane(threadIdx.x >> 6), l = threadIdx.x & 63;
  if (b < 240) role_a(P, b, wv, l, g_tent);
  else role_f(P, b - 240, wv, l, g_tent);
}

extern "C" __device__ __attribute__((used, noinline)) void code_end_marker() {}

extern "C" void kernel_launch(void* const* d_in, const int* in_sizes, int n_in, void* d_out, int out_size, void* d_ws, size_t ws_size, hipStream_t stream) {
  Params P;
  P.x = (const u16*)d_in[0]; P.wqkv = (const u16*)d_in[1]; P.wo = (const u16*)d_in[2];
  P.wr = (const u16*)d_in[4]; P.wg = (const u16*)d_in[5]; P.wu = (const u16*)d_in[6]; P.wd = (const u16*)d_in[7];
  P.out = (u16*)d_out; P.ws = (char*)d_ws;
  (void)hipFuncSetAttribute((const void*)fused_kernel, hipFuncAttributeMaxDynamicSharedMemorySize, LDS_BYTES);
  fused_kernel<<<256, 256, LDS_BYTES, stream>>>(P);
}
```

```cpp
#define STAMPS
#define STAMPMASK 0x20000001u
#define PROBE_A 0
#define PROBE_B 0
#define PROBE_BLK 0
#define PROBE_MUL 0
#define PROBE_DIV 1
#define K2MUL 0
#include <hip/hip_runtime.h>
#include <hip/hip_bf16.h>
#include <stdint.h>

typedef unsigned short u16;
typedef unsigned long long u64;
typedef __bf16 bf2 __attribute__((ext_vector_type(2)));
typedef short s4 __attribute__((ext_vector_type(4)));
typedef float f4 __attribute__((ext_vector_type(4)));
typedef float f2 __attribute__((ext_vector_type(2)));
typedef unsigned u4 __attribute__((ext_vector_type(4)));
typedef unsigned u2 __attribute__((ext_vector_type(2)));
typedef __amdgpu_buffer_rsrc_t rsrc_t;
#define LAS __attribute__((address_space(3)))
#define DEV __device__ __forceinline__

constexpr unsigned TILEB = 81920;
constexpr unsigned BUFB = 30 * TILEB;
constexpr unsigned WS_EPOCH = 0;
constexpr unsigned WS_FF = 4096;
constexpr unsigned WS_SEL = 8192;
constexpr unsigned WS_CAND = 16384;
constexpr unsigned WS_FA = 32768;
constexpr unsigned WS_FX = 65536;
constexpr unsigned WS_YP = 131072;
constexpr unsigned WS_ST = 262144;
constexpr unsigned STB = 30 * 8 * 80 * 16;
constexpr unsigned WS_OB = 1048576;
constexpr unsigned WS_XB = WS_OB + 2 * BUFB;
constexpr unsigned WS_USED = WS_XB + 2 * BUFB;
constexpr unsigned WS_STAMP = 16u << 20;
constexpr unsigned L_RING = 81920;
constexpr unsigned L_RED2 = L_RING + 16384;
constexpr unsigned L_MISC = 155648;
constexpr unsigned O_RED = 0;
constexpr unsigned O_RC = 4096, O_RM = O_RC + 320, O_RA = O_RM + 320, O_RB = O_RA + 320;
constexpr unsigned LDS_BYTES = 163840;
constexpr unsigned SENT = 0xFFFFFFFFu, OOB = 0x80000000u, ONE2 = 0x3F803F80u;
#define FDELAY 200
constexpr int SPIN_CAP = 1 << 15;
constexpr float EPS = 1e-5f;

#define CFENCE() asm volatile("" ::: "memory")
#define WAITC(vm, lg) do { CFENCE(); __builtin_amdgcn_s_waitcnt(((vm) & 15) | 0x70 | ((lg) << 8) | (((vm) >> 4) << 14)); CFENCE(); } while (0)
#define WAITV(n) asm volatile("s_waitcnt vmcnt(%0)" ::"i"(n) : "memory")
#define WAITL() WAITC(63, 0)
#define BAR() do { CFENCE(); __builtin_amdgcn_s_barrier(); CFENCE(); } while (0)
#define SB() __builtin_amdgcn_sched_barrier(0)
#define OPQ(x) asm volatile("" : "+v"(x))
#define KEEP(x) asm volatile("" ::"v"(x))

struct Params {
  const u16* x; const u16* wqkv; const u16* wo; const u16* wr; const u16* wg; const u16* wu; const u16* wd;
  u16* out; char* ws;
};

DEV rsrc_t mk(const void* p, unsigned bytes) { return __builtin_amdgcn_make_buffer_rsrc((void*)p, 0, bytes, 0x00020000); }
DEV void setm0(unsigned base, int imm) { asm volatile("s_add_u32 m0, %0, %1" ::"s"(base), "i"(imm) : "scc", "memory"); }
template <int SC> DEV void dma_go(rsrc_t rs, unsigned voff, unsigned soff) {
  if (SC) asm volatile("buffer_load_dwordx4 %0, %1, %2 offen sc1 lds" ::"v"(voff), "s"(rs), "s"(soff) : "memory");
  else asm volatile("buffer_load_dwordx4 %0, %1, %2 offen lds" ::"v"(voff), "s"(rs), "s"(soff) : "memory");
}
DEV void dma_go_m(rsrc_t rs, unsigned voff, unsigned soff, u64 mask) {
  asm volatile("s_mov_b64 exec, %3\n\tbuffer_load_dwordx4 %0, %1, %2 offen lds\n\ts_mov_b64 exec, -1" ::"v"(voff), "s"(rs), "s"(soff), "s"(mask) : "memory");
}
template <int SC> DEV void dma(rsrc_t rs, unsigned voff, unsigned soff, unsigned base, int imm) {
  if (SC) asm volatile("s_add_u32 m0, %3, %4\n\ts_nop 0\n\tbuffer_load_dwordx4 %0, %1, %2 offen sc1 lds" ::"v"(voff), "s"(rs), "s"(soff), "s"(base), "i"(imm) : "scc", "memory");
  else asm volatile("s_add_u32 m0, %3, %4\n\ts_nop 0\n\tbuffer_load_dwordx4 %0, %1, %2 offen lds" ::"v"(voff), "s"(rs), "s"(soff), "s"(base), "i"(imm) : "scc", "memory");
}
DEV void st16(rsrc_t rs, u4 v, unsigned voff, unsigned soff) {
  asm volatile("buffer_store_dwordx4 %0, %1, %2, %3 offen sc1\n\ts_nop 1" ::"v"(v), "v"(voff), "s"(rs), "s"(soff) : "memory");
}
DEV void st4(rsrc_t rs, unsigned v, unsigned voff, unsigned soff) {
  asm volatile("buffer_store_dword %0, %1, %2, %3 offen sc1" ::"v"(v), "v"(voff), "s"(rs), "s"(soff) : "memory");
}
DEV u4 ld16(rsrc_t rs, unsigned voff, unsigned soff) { return __builtin_amdgcn_raw_buffer_load_b128(rs, voff, soff, 16); }
DEV unsigned ld4(rsrc_t rs, unsigned voff, unsigned soff) { return __builtin_amdgcn_raw_buffer_load_b32(rs, voff, soff, 16); }

template <bool Z = false> DEV void mWA(f4& c, u4 w, u4 a) {
  if (Z) asm volatile("v_mfma_f32_16x16x32_bf16 %0, %1, %2, 0" : "=v"(c) : "a"(w), "v"(a));
  else asm volatile("v_mfma_f32_16x16x32_bf16 %0, %1, %2, %0" : "+v"(c) : "a"(w), "v"(a));
}
template <bool Z = false> DEV void mAW(f4& c, u4 a, u4 w) {
  if (Z) asm volatile("v_mfma_f32_16x16x32_bf16 %0, %1, %2, 0" : "=v"(c) : "v"(a), "a"(w));
  else asm volatile("v_mfma_f32_16x16x32_bf16 %0, %1, %2, %0" : "+v"(c) : "v"(a), "a"(w));
}
template <bool Z = false> DEV void mVV(f4& c, u4 a, u4 b) {
  if (Z) asm volatile("v_mfma_f32_16x16x32_bf16 %0, %1, %2, 0" : "=v"(c) : "v"(a), "v"(b));
  else asm volatile("v_mfma_f32_16x16x32_bf16 %0, %1, %2, %0" : "+v"(c) : "v"(a), "v"(b));
}
template <bool Z = false> DEV void m16(f4& c, u2 a, u2 b) {
  if (Z) asm volatile("v_mfma_f32_16x16x16_bf16 %0, %1, %2, 0" : "=v"(c) : "v"(a), "v"(b));
  else asm volatile("v_mfma_f32_16x16x16_bf16 %0, %1, %2, %0" : "+v"(c) : "v"(a), "v"(b));
}
DEV f4 m16c(u2 a, u2 b, f4 c) { f4 d; asm volatile("v_mfma_f32_16x16x16_bf16 %0, %1, %2, %3" : "=&v"(d) : "v"(a), "v"(b), "v"(c)); return d; }
template <bool Z = false> DEV void mf32(f4& c, float a, float b) {
  if (Z) asm volatile("v_mfma_f32_16x16x4_f32 %0, %1, %2, 0" : "=&v"(c) : "v"(a), "v"(b));
  else asm volatile("v_mfma_f32_16x16x4_f32 %0, %1, %2, %0" : "+v"(c) : "v"(a), "v"(b));
}
#define NOPS(n) asm volatile("s_nop %0" ::"i"((n) - 1))
DEV unsigned pk(float a, float b) { f2 v = {a, b}; return __builtin_bit_cast(unsigned, __builtin_convertvector(v, bf2)); }
DEV u2 pk4(f4 v) { return (u2){pk(v[0], v[1]), pk(v[2], v[3])}; }
DEV float lo(unsigned u) { return __uint_as_float(u << 16); }
DEV float hi(unsigned u) { return __uint_as_float(u & 0xFFFF0000u); }
DEV f4 up4(u2 u) { return (f4){lo(u[0]), hi(u[0]), lo(u[1]), hi(u[1])}; }
DEV f4 asf(u4 v) { return __builtin_bit_cast(f4, v); }
DEV u4 asu(f4 v) { return __builtin_bit_cast(u4, v); }
DEV unsigned bsel(unsigned m, unsigned a, unsigned b) { return __builtin_amdgcn_bitop3_b32(m, a, b, 0xCA); }
DEV float bself(unsigned m, float a, float b) { return __uint_as_float(bsel(m, __float_as_uint(a), __float_as_uint(b))); }
DEV f4 bsel4(unsigned m, f4 a, f4 b) { return (f4){bself(m, a[0], b[0]), bself(m, a[1], b[1]), bself(m, a[2], b[2]), bself(m, a[3], b[3])}; }
DEV unsigned lmask(bool c) { unsigned m = c ? ~0u : 0u; OPQ(m); return m; }
DEV float fmx(float a, float b) { return __builtin_elementwise_maximum(a, b); }
template <class T> DEV T lds_r(unsigned a) { return *(const LAS T*)(size_t)a; }
template <class T> DEV void lds_w(unsigned a, T v) { *(LAS T*)(size_t)a = v; }
DEV u4 trfrag2(unsigned a, unsigned step) {
  u2 x = __builtin_bit_cast(u2, __builtin_amdgcn_ds_read_tr16_b64_v4i16((LAS s4*)(size_t)a));
  u2 y = __builtin_bit_cast(u2, __builtin_amdgcn_ds_read_tr16_b64_v4i16((LAS s4*)(size_t)(a + step)));
  return (u4){x[0], x[1], y[0], y[1]};
}
DEV f2 swap16(float a, float b) { auto r = __builtin_amdgcn_permlane16_swap(__float_as_uint(a), __float_as_uint(b), false, false); return (f2){__uint_as_float(r[0]), __uint_as_float(r[1])}; }
DEV f2 swap32(float a, float b) { auto r = __builtin_amdgcn_permlane32_swap(__float_as_uint(a), __float_as_uint(b), false, false); return (f2){__uint_as_float(r[0]), __uint_as_float(r[1])}; }
DEV float xadd16(float v) { f2 r = swap16(v, v); return r[0] + r[1]; }
DEV float xadd32(float v) { f2 r = swap32(v, v); return r[0] + r[1]; }
template <int C> DEV unsigned dpp(unsigned v) { return __builtin_amdgcn_update_dpp(0u, v, C, 0xF, 0xF, true); }
template <int C> DEV float dppf(float v) { return __uint_as_float(dpp<C>(__float_as_uint(v))); }
DEV float wave_sum(float v) {
  v += dppf<0xB1>(v); v += dppf<0x4E>(v); v += dppf<0x141>(v); v += dppf<0x140>(v);
  return xadd32(xadd16(v));
}
DEV unsigned max_row16(unsigned v) { v = max(v, dpp<0xB1>(v)); v = max(v, dpp<0x4E>(v)); v = max(v, dpp<0x141>(v)); return max(v, dpp<0x140>(v)); }
DEV unsigned max_x16(unsigned v) { auto r = __builtin_amdgcn_permlane16_swap(v, v, false, false); return max(r[0], r[1]); }
DEV unsigned max_x32(unsigned v) { auto r = __builtin_amdgcn_permlane32_swap(v, v, false, false); return max(r[0], r[1]); }
DEV unsigned umin(unsigned a, unsigned b) { return a < b ? a : b; }
DEV unsigned rfl(unsigned v) { return __builtin_amdgcn_readfirstlane(v); }
DEV unsigned rdl(unsigned v, int l) { return __builtin_amdgcn_readlane(v, l); }
DEV float rsq(float v) { return __builtin_amdgcn_rsqf(v); }
DEV float sigm(float v) { return __builtin_amdgcn_rcpf(1.f + __builtin_amdgcn_exp2f(-1.44269504f * v)); }
DEV float key_score(unsigned k) { unsigned u = k & 0xFFFFF800u; return __uint_as_float((u & 0x80000000u) ? (u ^ 0x80000000u) : ~u); }

#define STAMP(i) do { if (__builtin_constant_p(i) && !((STAMPMASK >> (i)) & 1)) break; u64 a_, b_; asm volatile("s_memrealtime %0\n\ts_memtime %1\n\ts_waitcnt lgkmcnt(0)" : "=&s"(a_), "=&s"(b_) :: "memory"); \
    asm volatile("s_mov_b32 m0, %3\n\ts_nop 0\n\tv_writelane_b32 %0, %1, m0\n\ts_add_u32 m0, m0, 32\n\ts_nop 0\n\tv_writelane_b32 %0, %2, m0" : "+v"(stv) : "s"((unsigned)a_), "s"((unsigned)b_), "s"(i) : "scc"); } while (0)
#define STAMP_INIT() unsigned stv = 0; asm volatile("v_writelane_b32 %0, %1, 28" : "+v"(stv) : "s"(g_tent))
#define PROBE_X 0
#define STAMP_FLUSH() do { STAMP(29); if (wv == 0) st4(ws, rdl(stv, 29), l == 0 ? blockIdx.x * 4 : OOB, 2048); if (PROBE_X && blockIdx.x == PROBE_BLK) { unsigned o_ = rfl(ld4(ws, 0, 2048)); asm volatile("v_writelane_b32 %0, %1, 27" : "+v"(stv) : "s"(o_)); } if (blockIdx.x == PROBE_BLK && threadIdx.x < 64) { unsigned d_ = min(max((int)(rdl(stv, PROBE_B) - rdl(stv, PROBE_A)) / PROBE_DIV + (PROBE_X ? 200 : 0), 0), 5000) * PROBE_MUL, t_ = rdl(stv, 29); \
    for (int sp_ = 0; sp_ < (1 << 20) && (unsigned)__builtin_amdgcn_s_memrealtime() - t_ < d_; ++sp_) __builtin_amdgcn_s_sleep(2); } } while (0)
DEV unsigned tag_issue(const char* ws) { unsigned e; asm volatile("s_load_dword %0, %1, 0x0" : "=s"(e) : "s"(ws) : "memory"); return e; }
DEV unsigned tag_finish(unsigned e) { unsigned t; asm volatile("s_waitcnt lgkmcnt(0)\n\ts_add_u32 %0, %1, 1" : "=s"(t) : "s"(e) : "memory", "scc"); return t; }

DEV void wait_flags(rsrc_t ws, unsigned voff, unsigned base, unsigned tag) {
  for (int sp = 0; sp < SPIN_CAP; ++sp) {
    CFENCE();
    if (__all(ld4(ws, voff, base) == tag)) break;
    __builtin_amdgcn_s_sleep(1);
  }
}
DEV u4 wait_rec(rsrc_t ws, unsigned voff, unsigned base, unsigned tag) {
  u4 c;
  for (int sp = 0; sp < SPIN_CAP; ++sp) {
    CFENCE();
    c = ld16(ws, voff, base);
    if (__all(c[3] == tag)) break;
    __builtin_amdgcn_s_sleep(1);
  }
  return c;
}

struct RowC { float c[5], cm[5]; f4 c4[5], cm4[5]; };
DEV void rowc_read(RowC& R, unsigned r1, unsigned r4, int i) {
  const int t = i / 3;
  if (i % 3 == 0) { R.c[t] = lds_r<float>(r1 + O_RC + t * 64); R.cm[t] = lds_r<float>(r1 + O_RM + t * 64); }
  else if (i % 3 == 1) R.c4[t] = lds_r<f4>(r4 + O_RC + t * 64);
  else R.cm4[t] = lds_r<f4>(r4 + O_RM + t * 64);
}

DEV void role_a(const Params& P, const int b, const int wv, const int l, unsigned g_tent) {
  const int xcd = b & 7, r = b >> 3;
  const int q = (xcd >> 1) * 30 + r, tile = q >> 2, hg = (xcd & 1) * 4 + (q & 3);
  const int lr = l & 15, g = l >> 4;
  const rsrc_t ws = mk(P.ws, WS_USED);
  const unsigned epoch = tag_issue(P.ws);
  STAMP_INIT();
  STAMP(0);
  if (PROBE_X && b == 0 && wv == 0) st4(ws, g_tent, l == 0 ? 0 : OOB, 2048);
  u4 W[16][3];
  f4 aq[5], ak[5], av[5];
  u2 xp[5];
  RowC R;
  unsigned tag, pm;
  f4 mask;
  u2 wr2;
  const unsigned trb = L_RING + (8 * g + (lr >> 2)) * 128 + (((2 * wv + ((l & 3) >> 1)) ^ ((((lr >> 3) & 1) | ((g & 1) << 1)) << 1)) << 4) + 8 * (l & 1);
  {
    const int par = tile >= 15, p0 = 68 * (tile - 15 * par);
    const rsrc_t rx = mk(P.x, 2048 * 1024), rw = mk(P.wqkv, 512 * 3072), rwo = mk(P.wo, 512 * 1024);
    unsigned xoff[5];
    {
      const int rr = 4 * wv + g;
#pragma unroll
      for (int j = 0; j < 5; ++j) { int p = min(p0 + 15 * j + rr, 1023); xoff[j] = (2 * p + par) * 1024 + ((lr ^ rr) << 4); }
    }
    const u64 xmask = wv == 3 ? 0x0000FFFFFFFFFFFFull : ~0ull;
    const int k8 = l >> 3;
    const unsigned swA = (((l & 7) ^ (((k8 >> 1) & 1) << 1)) << 4) ^ ((wv & 1) << 6), swB = swA ^ 64;
    const unsigned woffA = k8 * 3072 + hg * 128 + swA, woffB = k8 * 3072 + hg * 128 + swB;
    const unsigned lx = wv * 1024, lw = L_RING + wv * 3072;
    unsigned wso[3];
#pragma unroll
    for (int j = 0; j < 3; ++j) { const int i = wv * 3 + j; wso[j] = (i & 3) * 8 * 3072 + (i >> 2) * 1024; }
    auto ldx = [&](int pr) {
#pragma unroll
      for (int j = 0; j < 5; ++j) { setm0(lx, pr * 20480 + j * 4096); dma_go_m(rx, xoff[j], pr * 256, xmask); }
    };
    auto ldw = [&](int s) {
#pragma unroll
      for (int j = 0; j < 3; ++j) dma<0>(rw, (j & 1) ? woffB : woffA, wso[j] + s * 98304, lw, (s % 6) * 12288 + j * 1024);
    };
    asm volatile("global_load_dwordx2 %0, %1, %2" : "=v"(wr2) : "v"((16 * wv + 4 * g) * 2), "s"(P.wr + 64 * hg) : "memory");
    ldx(0); ldw(0); ldw(1);
    unsigned touch;
    {
      int rh = (3 - (int)(((size_t)P.wqkv) >> 7)) & 3, idx = min(r * 52 + wv * 13 + min(l, 12), 1535);
      unsigned off = (idx / 3) * 3072 + (idx % 3) * 1024 + ((xcd & 1) * 4 + rh) * 128;
      asm volatile("buffer_load_dword %0, %1, %2, 0 offen" : "=v"(touch) : "v"(off), "s"(rw) : "memory");
    }
    ldw(2); ldx(1); ldw(3); ldw(4);
    tag = tag_finish(epoch);
    {
      const u4 one = {ONE2, ONE2, ONE2, ONE2};
      const int row = wv * 4 + g;
      lds_w<u4>((row / 5) * 20480 + ((row % 5) * 16 + 15) * 256 + lr * 16, one);
      if (wv == 0) lds_w<u4>(3 * 20480 + ((g + 1) * 16 + 15) * 256 + lr * 16, one);
    }
    const unsigned ooff = tile * TILEB + (((g & 1) * 32 + 4 * hg + wv) * 2 + (g >> 1)) * 256 + lr * 16;
    const unsigned ooff4 = (g & 1) ? OOB : ooff;
    u4 sent = {SENT, SENT, SENT, SENT};
    OPQ(sent);
    const unsigned wooffA = k8 * 1024 + hg * 128 + swA, wooffB = k8 * 1024 + hg * 128 + swB;
    unsigned xa[4];
#pragma unroll
    for (int i = 0; i < 4; ++i) xa[i] = lr * 256 + (((4 * i + g) ^ lr) << 4);
    const unsigned xg = lr * 256 + (((4 * wv + g) ^ lr) << 4);
    const unsigned xr = (hg >> 1) * 20480 + lr * 256 + ((((hg & 1) * 8 + 2 * wv + (g >> 1)) ^ lr) << 4) + 8 * (g & 1);
    unsigned mq = L_MISC + O_RED + (g == (lr >> 2) ? (wv * 80 + lr) * 8 + 4 : 2560 + l * 4), ms = L_MISC + O_RED + (g == 3 ? (wv * 80 + lr) * 8 : 2560 + l * 4);
    unsigned rb_ = L_MISC + O_RED + (wv * 20 + min(l, 19)) * 8, rc_ = L_MISC + (wv * 20 + min(l, 19)) * 4;
    unsigned r1 = L_MISC + lr * 4, r4 = L_MISC + g * 16;
    OPQ(mq); OPQ(ms); OPQ(rb_); OPQ(rc_); OPQ(r1); OPQ(r4);
    const unsigned m1 = lmask(l & 1), m2 = lmask(l & 2);
    pm = lmask(lr < 8 && g >= 2);
#pragma unroll
    for (int i = 0; i < 4; ++i) { const int rk = 4 * g + i, d = (lr < 8 && g >= 2) ? lr + 15 - rk : lr - rk; mask[i] = (d >= 0 && d <= 7 && rk != 15) ? 0.f : -1e30f; }
    OPQ(mask);
    f4 gr[5];
#pragma unroll
    for (int t = 0; t < 5; ++t) { aq[t] = ak[t] = av[t] = gr[t] = (f4){0, 0, 0, 0}; OPQ(aq[t]); OPQ(ak[t]); OPQ(av[t]); OPQ(gr[t]); }
    u4 a[2][5], z[2][5];
    f2 rv[4];
#pragma unroll
    for (int s = 0; s <= 16; ++s) {
      constexpr int NV[17] = {18, 20, 20, 16, 17, 17, 17, 17, 17, 15, 14, 16, 15, 15, 15, 15, 15};
      switch (s) {
#define C_(n) case n: WAITC(NV[n], 0); break;
        C_(0) C_(1) C_(2) C_(3) C_(4) C_(5) C_(6) C_(7) C_(8) C_(9) C_(10) C_(11) C_(12) C_(13) C_(14) C_(15) C_(16)
#undef C_
      }
      BAR();
      if (s == 0) STAMP(1);
      if (s == 4) STAMP(19);
      if (s == 8) STAMP(20);
      if (s == 12) STAMP(21);
      if (s == 16) STAMP(22);
      SB();
      auto gtile = [](int s_, int i) {
        if (s_ < 0) return -1;
        if (s_ < 5) return i == 0 ? s_ : -1;
        if (s_ < 7) return i < 2 ? 8 + 2 * (s_ - 5) + i : -1;
        if (s_ == 7) return i == 0 ? 12 : -1;
        if (s_ == 8) return i < 2 ? 16 + i : -1;
        if (s_ < 12) return i == 0 ? 16 + s_ - 7 : -1;
        if (s_ == 12) return 24 + i;
        return -1;
      };
      const int nx = s < 8 ? ((s & 3) == 0 ? 2 : 1) : 0, nd = s < 16 ? nx + 3 : 0;
#pragma unroll
      for (int i = 0; i < 20; ++i) {
        if (s > 0) {
          const int pb = (s - 1) & 1;
          if (i < 15) {
            const int t = i / 3;
            if (i % 3 == 0) mWA(aq[t], W[s - 1][0], a[pb][t]); else if (i % 3 == 1) mWA(ak[t], W[s - 1][1], a[pb][t]); else mAW(av[t], a[pb][t], W[s - 1][2]);
          } else if (gtile(s - 1, i - 15) >= 0) mVV(gr[gtile(s - 1, i - 15) & 7], z[pb][i - 15], z[pb][i - 15]);
        }
        if (s < 16) {
          if (i < 3) W[s][i] = trfrag2(trb + (s % 6) * 12288 + i * 4096, 512);
          else if (i < 8) a[s & 1][i - 3] = lds_r<u4>(xa[s & 3] + (s >> 2) * 20480 + (i - 3) * 4096);
          else if (i < 13 && gtile(s, i - 8) >= 0) z[s & 1][i - 8] = lds_r<u4>(xg + (gtile(s, i - 8) >> 3) * 20480 + (gtile(s, i - 8) & 7) * 4096);
          if (i >= 3 && (i - 3) / 2 < nd) {
            const int k = (i - 3) / 2, ph = (i - 3) & 1;
            if (k < nx) {
              const int pr = 2 + (s >> 2), j = (s & 3) == 0 ? k : (s & 3) + 1;
              if (ph == 0) setm0(lx, pr * 20480 + j * 4096); else dma_go_m(rx, xoff[j], pr * 256, xmask);
            } else {
              const int j = k - nx;
              if (ph == 0) setm0(lw, ((s + 5) % 6) * 12288 + j * 1024);
              else if (s < 11) dma_go<0>(rw, (j & 1) ? woffB : woffA, wso[j] + (s + 5) * 98304);
              else dma_go<0>(rwo, (j & 1) ? wooffB : wooffA, (((3 * (s - 11) + ((wv * 3 + j) >> 2) + 2 * hg) & 15) * 32 + ((wv * 3 + j) & 3) * 8) * 1024);
            }
          }
          if (s == 10 && i >= 10 && i < 13) st16(ws, sent, i < 12 ? ooff : ooff4, WS_OB + (i - 10) * 32768);
          if (s == 13 && i >= 10 && i < 15) xp[i - 10] = lds_r<u2>(xr + (i - 10) * 4096);
          if (s == 14 && i >= 8 && i < 13) {
            const f4 v = gr[i - 8];
            lds_w<float>(mq + (i - 8) * 128, bself(m2, bself(m1, v[3], v[2]), bself(m1, v[1], v[0])));
            lds_w<float>(ms + (i - 8) * 128, v[3]);
          }
          if (s == 15 && i == 8) { rv[0] = lds_r<f2>(rb_); rv[1] = lds_r<f2>(rb_ + 640); rv[2] = lds_r<f2>(rb_ + 1280); rv[3] = lds_r<f2>(rb_ + 1920); }
          if (s == 15 && i == 12) {
            const f2 v = (rv[0] + rv[1]) + (rv[2] + rv[3]);
            const float m = v[0] * (1.f / 512), rs = rsq(fmx(v[1] * (1.f / 512) - m * m, 0.f) + EPS);
            lds_w<float>(rc_ + O_RC, rs); lds_w<float>(rc_ + O_RM, rs * m); lds_w<float>(rc_ + O_RA, 1.f); lds_w<float>(rc_ + O_RB, 0.f);
          }
        } else {
          if (i < 15) rowc_read(R, r1, r4, i);
          if (i == 0 && wv == 0) st4(ws, tag, l == 0 ? 0 : OOB, WS_FA + (tile * 8 + hg) * 128);
          if (i == 2) dma<0>(rwo, wooffA, (((15 + 2 * hg) & 15) * 32 + wv * 8) * 1024, lx, L_RING + 3 * 12288);
          if (i >= 4 && i < 7) st16(ws, sent, i < 6 ? ooff : ooff4, WS_OB + BUFB + (i - 4) * 32768);
        }
        SB();
      }
    }
    asm volatile("" ::"v"(touch));
    OPQ(wr2);
  }
  NOPS(2);
  SB();
  f4 csq, csk;
#pragma unroll
  for (int i = 0; i < 4; ++i) { csq[i] = dppf<0x15F>(aq[0][i]); csk[i] = dppf<0x15F>(ak[0][i]); }
  float csv;
  { f2 x = swap32(av[0][3], av[0][3]); f2 y = swap16(x[1], x[1]); csv = y[1]; }
  STAMP(2);
  u4 WO[16];

#pragma unroll 1
  for (int L = 0; L < 2; ++L) {
#define FRESH(n) int l##n = l, wv##n = wv, hg##n = hg, tile##n = tile; asm volatile("" : "+v"(l##n)); asm volatile("" : "+s"(wv##n), "+s"(hg##n), "+s"(tile##n)); \
    const int lr##n = l##n & 15, g##n = l##n >> 4; (void)lr##n; (void)g##n;
    FRESH(1)
    const unsigned ob = WS_OB + L * BUFB + tile1 * TILEB;
    unsigned fa;
    {
      u2 kb[5], vb[5], qb[5], o[5];
      f4 ss[5], sp[5], sc[5];
      float mx[5];
      auto scale = [&](int t) {
        qb[t] = pk4(aq[t] * R.c[t] - csq * R.cm[t]);
        kb[t] = pk4(ak[t] * R.c[t] - csk * R.cm[t]);
        vb[t] = pk4(av[t] * R.c4[t] - R.cm4[t] * csv);
      };
      auto smm = [&](int t) { ss[t] = m16c(kb[t], qb[t], mask); if (t) sp[t] = m16c(kb[t - 1], qb[t], mask); };
      auto pick = [&](int t) {
        sc[t] = bsel4(pm, t ? sp[t] : (f4){-1e30f, -1e30f, -1e30f, -1e30f}, ss[t]);
        mx[t] = fmx(fmx(fmx(sc[t][0], sc[t][1]), sc[t][2]), sc[t][3]);
      };
      scale(0); SB();
      scale(1); SB(); smm(0); SB();
      scale(2); SB(); smm(1); SB();
      scale(3); SB(); smm(2); SB();
      scale(4); SB(); smm(3); smm(4); SB();
      pick(0); pick(1); pick(2); SB();
      pick(3); pick(4);
      {
        f2 x = swap16(mx[0], mx[1]), y = swap16(mx[2], mx[3]), w = swap16(mx[4], mx[4]);
        f2 u = swap32(fmx(x[0], x[1]), fmx(y[0], y[1]));
        const float w1 = fmx(w[0], w[1]), c = fmx(u[0], u[1]) * 0.36067376f;
        f2 w2 = swap32(w1, w1), v = swap32(c, c);
        f2 v0 = swap16(v[0], v[0]), v1 = swap16(v[1], v[1]);
        mx[0] = v0[0]; mx[1] = v0[1]; mx[2] = v1[0]; mx[3] = v1[1]; mx[4] = fmx(w2[0], w2[1]) * 0.36067376f;
      }
      asm volatile("buffer_load_dword %0, %1, %2, %3 offen sc1" : "=v"(fa) : "v"((l1 & 7) * 128), "s"(ws), "s"(WS_FA + tile1 * 1024) : "memory");
      u2 one2 = {ONE2, ONE2};
      float kap = 0.36067376f;
      OPQ(one2); OPQ(kap);
      u2 pb[5], ps[5], pp[5];
      f4 sum[5], oa[5];
      auto expo = [&](int t) {
        const f4 d = sc[t] * kap - mx[t];
        f4 e;
#pragma unroll
        for (int i = 0; i < 4; ++i) e[i] = __builtin_amdgcn_exp2f(d[i]);
        pb[t] = pk4(e);
        if (t) { pp[t] = pb[t] & pm; ps[t] = pb[t] ^ pp[t]; } else ps[t] = pb[t];
      };
      auto pv = [&](int t) { m16<true>(oa[t], vb[t], ps[t]); if (t) m16(oa[t], vb[t - 1], pp[t]); m16<true>(sum[t], one2, pb[t]); };
      auto norm = [&](int t) { KEEP(sum[t]); o[t] = pk4(oa[t] * __builtin_amdgcn_rcpf(sum[t][0])); };
      const unsigned go = (((g1 & 1) * 32 + 4 * hg1 + wv1) * 2 + (g1 >> 1)) * 256 + lr1 * 16;
      const unsigned lo_ = (((g1 & 1) * 32 + wv1) * 2 + (g1 >> 1)) * 256 + lr1 * 16;
      auto put = [&](int i) {
        u2 A = o[2 * i], B = o[i < 2 ? 2 * i + 1 : 4];
        auto s0 = __builtin_amdgcn_permlane16_swap(A[0], B[0], false, false); auto s1 = __builtin_amdgcn_permlane16_swap(A[1], B[1], false, false);
        const u4 e = {umin(s0[0], SENT - 1), s1[0], s0[1], s1[1]};
        st16(ws, e, (i == 2 && (g1 & 1)) ? OOB : go, ob + i * 32768);
        lds_w<u4>(i < 2 ? lo_ + i * 32768 : ((g1 & 1) ? lo_ + 2048 : lo_ + 65536), e);
      };
      SB();
      expo(0); SB();
      expo(1); SB(); pv(0); SB();
      expo(2); SB(); pv(1); SB();
      expo(3); SB(); pv(2); SB(); norm(0); SB();
      expo(4); SB(); pv(3); SB(); norm(1); SB(); pv(4); SB();
      put(0); norm(2); SB(); norm(3); SB();
      put(1); norm(4); SB();
      put(2);
    }
    STAMP(3 + 8 * L);
    FRESH(2)
    const unsigned l16 = l2 * 16;
    unsigned l16h = l16 + 65536, r1 = L_MISC + lr2 * 4;
    OPQ(l16h); OPQ(r1);
    f4 acc[5];
#pragma unroll
    for (int t = 0; t < 5; ++t) acc[t] = up4(xp[t]) * lds_r<float>(r1 + O_RA + t * 64) + lds_r<float>(r1 + O_RB + t * 64);
    WAITC(3, 0);
    OPQ(fa);
    BAR();
    if (L == 0) {
      unsigned tb = trb;
      OPQ(tb);
#pragma unroll
      for (int j = 0; j < 16; ++j) { WO[j] = trfrag2(tb + ((4 + j / 3) % 6) * 12288 + (j % 3) * 4096, 512); asm volatile("" : "+a"(WO[j])); }
      if (!__all(fa == tag)) wait_flags(ws, (l2 & 7) * 128, WS_FA + tile2 * 1024, tag);
    }
    u4 a[3][5];
    auto rda = [&](int j, int t) { a[j % 3][t] = lds_r<u4>((t < 4 ? l16 + t * 16384 : l16h) + j * 1024); };
#pragma unroll
    for (int t = 0; t < 5; ++t) { rda(0, t); rda(1, t); }
    const unsigned lw2 = wv2 * 1024;
    SB();
#pragma unroll
    for (int i = 0; i < 3; ++i) {
      const unsigned so = ob + ((2 * hg2 + 2 + 4 * i + wv2) & 15) * 1024;
#pragma unroll
      for (int t = 0; t < 5; ++t) {
        if (i == 0) { setm0(lw2, 2048 + t * 16384); SB(); mWA(acc[t], WO[0], a[0][t]); SB(); dma_go<1>(ws, l16, so + t * 16384); }
        else dma<1>(ws, l16, so + t * 16384, lw2, (2 + 4 * i) * 1024 + t * 16384);
      }
    }
    {
      const unsigned so = ob + ((2 * hg2 + 14 + (wv2 & 1)) & 15) * 1024;
#pragma unroll
      for (int i = 0; i < 3; ++i) { const unsigned t = min((wv2 >> 1) * 3 + i, 4); dma<1>(ws, l16, so + t * 16384, (wv2 & 1) * 1024 + t * 16384, 14 * 1024); }
    }
    STAMP(4 + 8 * L);
    auto check = [&](int j) {
      u4 c0 = a[j % 3][0], c1 = a[j % 3][1], c2 = a[j % 3][2], c3 = a[j % 3][3], c4 = a[j % 3][4];
      unsigned m = max(max(max(c0[0], c1[0]), max(c2[0], c3[0])), c4[0]);
      if (__builtin_expect(__any(m == SENT), 0)) {
        const unsigned so = ob + ((2 * hg2 + j) & 15) * 1024;
        for (int sp = 0; sp < SPIN_CAP; ++sp) {
          CFENCE();
          c0 = ld16(ws, l16, so); c1 = ld16(ws, l16, so + 16384); c2 = ld16(ws, l16, so + 32768); c3 = ld16(ws, l16, so + 49152); c4 = ld16(ws, l16, so + 65536);
          m = max(max(max(c0[0], c1[0]), max(c2[0], c3[0])), c4[0]);
          if (!__any(m == SENT)) break;
          __builtin_amdgcn_s_sleep(1);
        }
        a[j % 3][0] = c0; a[j % 3][1] = c1; a[j % 3][2] = c2; a[j % 3][3] = c3; a[j % 3][4] = c4;
        NOPS(2);
      }
    };
    WAITV(13);
    BAR();
#pragma unroll
    for (int t = 0; t < 5; ++t) rda(2, t);
    SB();
#pragma unroll
    for (int j = 1; j < 16; ++j) {
      if (j == 4) WAITV(8); else if (j == 8) WAITV(3); else if (j == 12) WAITV(0);
      if (j == 4 || j == 8 || j == 12) BAR();
      SB();
#pragma unroll
      for (int t = 0; t < 5; ++t) { mWA(acc[t], WO[j], a[j % 3][t]); if (j + 2 < 16) rda(j + 2, t); SB(); }
      if (j + 1 < 16) check(j + 1);
      SB();
    }
    STAMP(5 + 8 * L);
    FRESH(3)
    const unsigned xb = WS_XB + L * BUFB + tile3 * TILEB;
    const int row3 = wv3 < 2 ? wv3 * 24 + min(l3, 23) : 16 + wv3 * 16 + min(l3, 15);
    const bool rowok = l3 < (wv3 < 2 ? 24 : 16);
    unsigned rw_ = L_RED2 + wv3 * 2048 + l3 * 16, rr_ = L_RED2 + row3 * 16, rc_ = L_MISC + row3 * 4;
    OPQ(rw_); OPQ(rr_); OPQ(rc_);
    {
      u2 spa[4]; float spf[4];
#pragma unroll
      for (int t = 0; t < 4; ++t) { spa[t] = lr3 == 4 * t ? (u2){ONE2, ONE2} : lr3 == 4 * t + 1 ? wr2 : (u2){0, 0}; spf[t] = lr3 == 4 * t + 2 ? 1.f : 0.f; }
      SB();
      float q2[5];
      f4 da, db, ea, eb;
#pragma unroll
      for (int t = 0; t < 5; ++t) {
        xp[t] = pk4(acc[t]);
        const f2 h = (f2){acc[t][0], acc[t][1]} * (f2){acc[t][0], acc[t][1]} + (f2){acc[t][2], acc[t][3]} * (f2){acc[t][2], acc[t][3]};
        q2[t] = h[0] + h[1];
        SB();
        if (t == 1) { m16<true>(da, spa[0], xp[0]); mf32<true>(db, spf[0], q2[0]); }
        else if (t > 1) { m16(da, spa[t - 1], xp[t - 1]); mf32(db, spf[t - 1], q2[t - 1]); }
        SB();
      }
      m16<true>(ea, spa[0], xp[4]); mf32<true>(eb, spf[0], q2[4]);
      SB();
      NOPS(8);
      SB();
      KEEP(da); KEEP(db);
      lds_w<f4>(rw_, (f4){da[0], da[1], db[2], 0.f});
      SB();
      KEEP(ea); KEEP(eb);
      lds_w<f4>(rw_ + 1024, (f4){ea[0], ea[1], eb[2], 0.f});
    }
    WAITL();
    BAR();
    {
      u4 w = asu((lds_r<f4>(rr_) + lds_r<f4>(rr_ + 2048)) + (lds_r<f4>(rr_ + 4096) + lds_r<f4>(rr_ + 6144)));
      w[3] = tag;
      st16(ws, w, rowok ? row3 * 16 : OOB, WS_ST + L * STB + (tile3 * 8 + hg3) * 1280);
      STAMP(6 + 8 * L);
      const unsigned go = (((g3 & 1) * 32 + 4 * hg3 + wv3) * 2 + (g3 >> 1)) * 256 + lr3 * 16;
#pragma unroll
      for (int i = 0; i < 3; ++i) {
        u2 A = xp[2 * i], B = xp[i < 2 ? 2 * i + 1 : 4];
        auto s0 = __builtin_amdgcn_permlane16_swap(A[0], B[0], false, false); auto s1 = __builtin_amdgcn_permlane16_swap(A[1], B[1], false, false);
        st16(ws, (u4){s0[0], s1[0], s0[1], s1[1]}, (i == 2 && (g3 & 1)) ? OOB : go, xb + i * 32768);
      }
    }
    WAITV(0);
    BAR();
    if (wv3 == 0) st4(ws, tag, l3 == 0 ? 0 : OOB, WS_FX + L * 32768 + (tile3 * 8 + hg3) * 128);
    const int par3 = tile3 >= 15, ti3 = tile3 - 15 * par3, p03 = 68 * ti3;
    const bool halo = L == 0 && ti3 > 0;
    wait_flags(ws, g3 == 1 ? WS_FF + lr3 * 128 : WS_FX + L * 32768 + ((tile3 - (halo ? (lr3 >> 3) : 0)) * 8 + (l3 & 7)) * 128, 0, tag);
    STAMP(7 + 8 * L);
    {
      const unsigned so = (halo && row3 < 7) ? ((tile3 - 1) * 640 + 72 + row3) * 16 : (tile3 * 640 + row3) * 16;
      u4 v[8];
#pragma unroll
      for (int i = 0; i < 8; ++i) v[i] = ld16(ws, so, WS_ST + L * STB + i * 1280);
      if (L == 0) {
        const unsigned g1_ = l3 * 16 + 16384, g0_ = g1_ - ((halo && lr3 < 7) ? 16256 : 0);
#pragma unroll
        for (int i = 0; i < 4; ++i)
#pragma unroll
          for (int t = 0; t < 5; ++t) dma<1>(ws, t ? g1_ : g0_, xb - 16384 + wv3 * 1024 + t * 16384 + i * 4096, wv3 * 1024, t * 16384 + i * 4096);
      }
      const f4 st = ((asf(v[0]) + asf(v[1])) + (asf(v[2]) + asf(v[3]))) + ((asf(v[4]) + asf(v[5])) + (asf(v[6]) + asf(v[7])));
      const float m = st[0] * (1.f / 512), var = fmx(st[2] * (1.f / 512) - m * m, 0.f), rs = rsq(var + EPS), a1 = 1.f + rs;
      const float c = a1 * rsq(var * a1 * a1 + EPS);
      lds_w<float>(rc_ + O_RC, c); lds_w<float>(rc_ + O_RM, c * m); lds_w<float>(rc_ + O_RA, a1); lds_w<float>(rc_ + O_RB, -m * rs);
    }
    FRESH(4)
    unsigned q1 = L_MISC + lr4 * 4, q4 = L_MISC + g4 * 16;
    OPQ(q1); OPQ(q4);
    if (L == 0) {
      unsigned zl = l4 * 16, zh = zl + 65536;
      OPQ(zh);
      WAITC(0, 0);
      BAR();
      STAMP(26);
      {
        u4 z[2][5];
#pragma unroll
        for (int t = 0; t < 5; ++t) z[0][t] = lds_r<u4>(t < 4 ? zl + t * 16384 : zh);
        SB();
#pragma unroll
        for (int s = 0; s < 16; ++s) {
#pragma unroll
          for (int t = 0; t < 5; ++t) {
            if (s == 0) mWA<true>(aq[t], W[s][0], z[s & 1][t]); else mWA(aq[t], W[s][0], z[s & 1][t]);
            if (s < 15) z[(s + 1) & 1][t] = lds_r<u4>((t < 4 ? zl + t * 16384 : zh) + (s + 1) * 1024); else rowc_read(R, q1, q4, 3 * t);
            SB();
            if (s == 0) mWA<true>(ak[t], W[s][1], z[s & 1][t]); else mWA(ak[t], W[s][1], z[s & 1][t]);
            if (s == 15) rowc_read(R, q1, q4, 3 * t + 1);
            SB();
            if (s == 0) mAW<true>(av[t], z[s & 1][t], W[s][2]); else mAW(av[t], z[s & 1][t], W[s][2]);
            if (s == 15) rowc_read(R, q1, q4, 3 * t + 2);
            SB();
          }
        }
      }
      STAMP(8);
      const u4 sel = wait_rec(ws, (l4 & 1) * 16, WS_SEL + (b & 15) * 128, tag);
      STAMP(9);
#pragma unroll 1
      for (int i = 0; i < 2; ++i) {
        const unsigned key = rdl(sel[0], i);
        const int tok = 2047 - (key & 2047), u = (tok >> 1) - p03;
        if ((tok & 1) != par3 || u < 0 || u >= 75) continue;
        FRESH(5)
        const float s_m = __uint_as_float(rdl(sel[1], i)), s_rs = __uint_as_float(rdl(sel[2], i));
        const float sg = sigm(key_score(key)), a1 = 1.f + s_rs, b1 = -s_m * s_rs;
        const int ts = u / 15, rs_ = u - 15 * ts;
        unsigned yb = L_RING + l5 * 32, rb = L_RING + 8192 + wv5 * 1024, zb = rb + g5 * 16;
        OPQ(yb); OPQ(zb);
        const u4 xr = lds_r<u4>((ts * 16 + (l5 >> 2)) * 1024 + (l5 & 3) * 256 + rs_ * 16);
        const f4 x0 = up4((u2){xr[0], xr[1]}) * a1 + b1, x1 = up4((u2){xr[2], xr[3]}) * a1 + b1;
        const unsigned me = lmask(lr5 == rs_);
        BAR();
        u4 y[8];
        for (int sp = 0; sp < SPIN_CAP; ++sp) {
          CFENCE();
          unsigned m = 0;
#pragma unroll
          for (int j = 0; j < 8; ++j) { y[j] = ld16(ws, l5 * 32 + (j & 1) * 16, WS_YP + (4 * wv5 + (j >> 1)) * 4096 + i * 2048); m = max(m, y[j][0]); }
          if (!__any(m == SENT)) break;
          __builtin_amdgcn_s_sleep(1);
        }
        STAMP(27);
        lds_w<f4>(yb + wv5 * 2048, (asf(y[0]) + asf(y[2])) + (asf(y[4]) + asf(y[6])));
        lds_w<f4>(yb + wv5 * 2048 + 16, (asf(y[1]) + asf(y[3])) + (asf(y[5]) + asf(y[7])));
        WAITL();
        BAR();
        const f4 y0 = (lds_r<f4>(yb) + lds_r<f4>(yb + 2048)) + (lds_r<f4>(yb + 4096) + lds_r<f4>(yb + 6144));
        const f4 y1 = (lds_r<f4>(yb + 16) + lds_r<f4>(yb + 2048 + 16)) + (lds_r<f4>(yb + 4096 + 16) + lds_r<f4>(yb + 6144 + 16));
        const u2 z0 = pk4(y0 * sg + x0), z1 = pk4(y1 * sg + x1);
        lds_w<u4>(rb + l5 * 16, (u4){z0[0], z0[1], z1[0], z1[1]});
        f4 tq, tk, tv;
        u4 z[16];
#pragma unroll
        for (int s = 0; s < 4; ++s) z[s] = lds_r<u4>(zb + s * 64);
        const u2 zx = lds_r<u2>(rb + (64 * hg5 + 16 * wv5 + 4 * g5) * 2);
        SB();
#pragma unroll
        for (int s = 0; s < 16; ++s) {
          if (s == 0) mWA<true>(tq, W[s][0], z[s]); else mWA(tq, W[s][0], z[s]);
          if (s + 4 < 16) z[s + 4] = lds_r<u4>(zb + (s + 4) * 64);
          SB();
          if (s == 0) { mWA<true>(tk, W[s][1], z[s]); mAW<true>(tv, z[s], W[s][2]); } else { mWA(tk, W[s][1], z[s]); mAW(tv, z[s], W[s][2]); }
          SB();
        }
        const f4 f0 = up4(z0), f1 = up4(z1), fs = f0 + f1, fq = f0 * f0 + f1 * f1;
        const float m2 = wave_sum((fs[0] + fs[1]) + (fs[2] + fs[3])) * (1.f / 512), q2 = wave_sum((fq[0] + fq[1]) + (fq[2] + fq[3])) * (1.f / 512);
        const float r2 = rsq(fmx(q2 - m2 * m2, 0.f) + EPS), rm2 = r2 * m2;
        SB();
        if (l5 == 0) { const unsigned ra = L_MISC + (ts * 16 + rs_) * 4; lds_w<float>(ra + O_RA, 1.f); lds_w<float>(ra + O_RB, 0.f); }
#pragma unroll
        for (int t = 0; t < 5; ++t)
          if (t == ts) {
            aq[t] = bsel4(me, tq, aq[t]); ak[t] = bsel4(me, tk, ak[t]);
            xp[t] = (u2){bsel(me, zx[0], xp[t][0]), bsel(me, zx[1], xp[t][1])};
            R.c[t] = bself(me, r2, R.c[t]); R.cm[t] = bself(me, rm2, R.cm[t]);
#pragma unroll
            for (int e = 0; e < 4; ++e) {
              const bool mine = 4 * g5 + e == rs_;
              av[t][e] = mine ? tv[e] : av[t][e]; R.c4[t][e] = mine ? r2 : R.c4[t][e]; R.cm4[t][e] = mine ? rm2 : R.cm4[t][e];
            }
          }
      }
      WAITL();
      BAR();
      STAMP(10);
    } else {
      const rsrc_t ro = mk(P.out, 2048 * 1024);
      WAITL();
      BAR();
#pragma unroll
      for (int t = 0; t < 5; ++t) {
        const float ra = lds_r<float>(q1 + O_RA + t * 64), rb = lds_r<float>(q1 + O_RB + t * 64);
        lds_w<u2>((16 * t + lr4) * 144 + wv4 * 32 + g4 * 8, pk4(up4(xp[t]) * ra + rb));
      }
      WAITL();
      BAR();
#pragma unroll
      for (int i = 0; i < 3; ++i) {
        const int row = min((i * 4 + wv4) * 8 + (l4 >> 3), 79), u = row - (row >> 4), p = p03 + u;
        const bool ok = (i * 4 + wv4) < 10 && (row & 15) != 15 && p < 1024 && (ti3 == 0 || u >= 7);
        st16(ro, lds_r<u4>(row * 144 + (l4 & 7) * 16), ok ? (2 * p + par3) * 1024 + hg4 * 128 + (l4 & 7) * 16 : OOB, 0);
      }
      STAMP(16);
      WAITV(0);
      BAR();
      if (wv4 < 2) {
        const u4 sel = wait_rec(ws, wv4 * 16, WS_SEL + 2048 + (b & 15) * 128, tag);
        STAMP(17);
        const unsigned key = rfl(sel[0]);
        const int tok = 2047 - (key & 2047), u = (tok >> 1) - p03;
        if ((tok & 1) == par3 && u >= (ti3 ? 7 : 0) && u < 75) {
          const f4 base = up4(lds_r<u2>((u + u / 15) * 144 + lr4 * 8));
          const float sg = sigm(key_score(key));
          u4 y[4];
          for (int sp = 0; sp < SPIN_CAP; ++sp) {
            CFENCE();
            unsigned m = 0;
#pragma unroll
            for (int j = 0; j < 4; ++j) { y[j] = ld16(ws, g4 * 4096 + lr4 * 16, WS_YP + 65536 + j * 16384 + wv4 * 2048 + hg4 * 256); m = max(m, y[j][0]); }
            if (!__any(m == SENT)) break;
            __builtin_amdgcn_s_sleep(1);
          }
          const f4 s = (asf(y[0]) + asf(y[1])) + (asf(y[2]) + asf(y[3]));
          const f2 x = swap16(s[0], s[1]), w = swap16(s[2], s[3]), v = swap32(x[0] + x[1], w[0] + w[1]);
          const float e = v[0] + v[1];
          const f2 v2 = swap32(e, e), e01 = swap16(v2[0], v2[0]), e23 = swap16(v2[1], v2[1]);
          __builtin_amdgcn_raw_buffer_store_b64(pk4((f4){e01[0], e01[1], e23[0], e23[1]} * sg + base), ro, l4 < 16 ? tok * 1024 + hg4 * 128 + lr4 * 8 : OOB, 0, 16);
        }
        STAMP(18);
      }
    }
  }
  STAMP_FLUSH();
}

DEV void role_f(const Params& P, const int j, const int wv, const int l, unsigned g_tent) {
  const int lr = l & 15, g = l >> 4, tid = wv * 64 + l;
  const rsrc_t ws = mk(P.ws, WS_USED);
  const u64 t0 = __builtin_amdgcn_s_memrealtime();
  STAMP_INIT();
  STAMP(0);
  const unsigned tag = tag_finish(tag_issue(P.ws));
  const u4 sent = {SENT, SENT, SENT, SENT};
  st16(ws, sent, j * 4096 + tid * 16, WS_YP); st16(ws, sent, j * 4096 + tid * 16, WS_YP + 65536);
  float wsum;
  { u4 w = *(const u4*)(P.wr + l * 8); f4 a = up4((u2){w[0], w[1]}) + up4((u2){w[2], w[3]}); wsum = wave_sum((a[0] + a[1]) + (a[2] + a[3])); }
  WAITV(0);
  BAR();
  if (wv == 0) st4(ws, tag, l == 0 ? 0 : OOB, WS_FF + j * 128);
  for (int sp = 0; sp < 4096 && __builtin_amdgcn_s_memrealtime() - t0 < FDELAY; ++sp) __builtin_amdgcn_s_sleep(4);
  const int noct = j < 12 ? 11 : 10, f0 = 8 * (j < 12 ? 11 * j : 132 + 10 * (j - 12));
  u4 G[16][3], Dn[3][8];
  {
    const rsrc_t rgu = mk(wv < 2 ? P.wg : P.wu, 512 * 2752), rd = mk(P.wd, 1376 * 1024);
    unsigned go[6];
#pragma unroll
    for (int i = 0; i < 6; ++i) { const int n = 64 * ((wv & 1) * 6 + i) + l, row = n / 12; go[i] = row * 2752 + f0 * 2 + (n - 12 * row) * 16; }
    const unsigned dof = (f0 + g) * 1024 + wv * 256 + lr * 16;
    auto issue = [&](int rd_) {
      if (rd_ < 8) {
#pragma unroll
        for (int i = 0; i < 6; ++i) dma<0>(rgu, go[i], rd_ * 64 * 2752, wv * 6144, (rd_ & 3) * 32768 + i * 1024);
      } else {
#pragma unroll
        for (int i = 0; i < 8; ++i) dma<0>(rd, dof, (32 * (rd_ - 8) + 4 * i) * 1024, wv * 8192, (rd_ & 3) * 32768 + i * 1024);
      }
    };
    unsigned tg = ((l >> 1) & 1) * 12288 + (8 * g + (lr >> 2)) * 192 + wv * 48 + (l & 1) * 8, td = wv * 8192 + (8 * g + (lr >> 2)) * 256 + (l & 3) * 8;
    issue(0); issue(1); issue(2);
#pragma unroll
    for (int rd_ = 0; rd_ < 11; ++rd_) {
      if (rd_ < 6) WAITC(12, 0); else if (rd_ == 6) WAITC(14, 0); else if (rd_ < 9) WAITC(16, 0); else if (rd_ == 9) WAITC(8, 0); else WAITC(0, 0);
      BAR();
      if (rd_ + 3 < 11) issue(rd_ + 3);
      unsigned bg = tg + (rd_ & 3) * 32768, bd = td + (rd_ & 3) * 32768;
      OPQ(bg); OPQ(bd);
      if (rd_ < 8) {
#pragma unroll
        for (int ks = 0; ks < 2; ++ks)
#pragma unroll
          for (int c = 0; c < 3; ++c) G[2 * rd_ + ks][c] = trfrag2(bg + ks * 32 * 192 + c * 16, 4 * 192);
      } else {
#pragma unroll
        for (int c = 0; c < 8; ++c) Dn[rd_ - 8][c] = trfrag2(bd + c * 32, 4 * 256);
      }
    }
    WAITL();
    BAR();
  }
  constexpr unsigned F_ACT = 0;
  const unsigned mg1 = lmask(g == 1), mg2 = lmask(g >= 2), mlo = lmask(lr < 8);
  auto gather = [&](const f4& a0, const f4& a1, const f4& a2, float& gv, float& uv) {
    KEEP(a0); KEEP(a1); KEEP(a2);
    const float d0 = bself(mg2, a2[0], bself(mg1, a1[0], a0[0])), d1 = bself(mg2, a2[1], bself(mg1, a1[1], a0[1]));
    gv = __uint_as_float(__builtin_amdgcn_update_dpp(__float_as_uint(d0), __float_as_uint(d1), 0x128, 0xF, 0xC, false));
    uv = __uint_as_float(__builtin_amdgcn_update_dpp(__float_as_uint(d1), __float_as_uint(d0), 0x128, 0xF, 0x3, false));
  };
  float csg, csu;
  {
    u4 one = {ONE2, ONE2, ONE2, ONE2};
    OPQ(one);
    NOPS(2);
    SB();
    f4 a[3];
#pragma unroll
    for (int ks = 0; ks < 16; ++ks)
#pragma unroll
      for (int c = 0; c < 3; ++c) { if (ks == 0) mAW<true>(a[c], one, G[ks][c]); else mAW(a[c], one, G[ks][c]); }
    SB();
    NOPS(8);
    SB();
    gather(a[0], a[1], a[2], csg, csu);
  }
  const bool live = g < 3 && 3 * wv + g < noct;
  const unsigned actw = F_ACT + (g < 3 ? (lr >> 3) * 192 + (24 * wv + 8 * g + (l & 7)) * 2 : 512 + l * 2);
  STAMP(1);
  const int tok = 32 * (4 * j + wv) + (l & 31);
  unsigned sto;
  { const int p = tok >> 1, ti = max(p - 7, 0) / 68, u = p - 68 * ti; sto = ((((tok & 1) * 15 + ti) * 8 + 4 * (l >> 5)) * 80 + u + u / 15) * 16; }

#pragma unroll 1
  for (int L = 0; L < 2; ++L) {
    u4 v[4];
    for (int sp = 0; sp < SPIN_CAP; ++sp) {
      CFENCE();
#pragma unroll
      for (int i = 0; i < 4; ++i) v[i] = ld16(ws, sto, WS_ST + L * STB + i * 1280);
      if (__all(v[0][3] == tag && v[1][3] == tag && v[2][3] == tag && v[3][3] == tag)) break;
      __builtin_amdgcn_s_sleep(1);
    }
    STAMP(2 + 8 * L);
    {
      const f4 s = (asf(v[0]) + asf(v[1])) + (asf(v[2]) + asf(v[3]));
      const float m = xadd32(s[0]) * (1.f / 512), dt = xadd32(s[1]), qq = xadd32(s[2]) * (1.f / 512);
      const float rs = rsq(fmx(qq - m * m, 0.f) + EPS), sc = (dt - m * wsum) * rs;
      unsigned u = __float_as_uint(sc); u ^= (unsigned)((int)u >> 31) | 0x80000000u;
      const unsigned key = (u & 0xFFFFF800u) | (2047u - tok);
      const unsigned k1 = max_x16(max_row16(key)), k2 = max_x16(max_row16(key == k1 ? 0u : key));
      st16(ws, (u4){key, __float_as_uint(m), __float_as_uint(rs), tag}, l >= 32 ? OOB : key == k1 ? 0 : key == k2 ? 16 : OOB, WS_CAND + L * 8192 + (4 * j + wv) * 128);
    }
    STAMP(3 + 8 * L);
    u4 c0, c1;
    for (int sp = 0; sp < SPIN_CAP; ++sp) {
      CFENCE();
      c0 = ld16(ws, l * 128, WS_CAND + L * 8192); c1 = ld16(ws, l * 128 + 16, WS_CAND + L * 8192);
      const unsigned f = ld4(ws, (wv * 60 + min(l, 59)) * 128, WS_FX + L * 32768);
      if (__all(c0[3] == tag && c1[3] == tag && f == tag)) break;
      __builtin_amdgcn_s_sleep(1);
    }
    BAR();
    STAMP(4 + 8 * L);
    const unsigned k1 = rfl(max_x32(max_x16(max_row16(c0[0]))));
    const bool w1 = c0[0] == k1;
    const u4 cb = w1 ? c1 : c0;
    const unsigned k2 = rfl(max_x32(max_x16(max_row16(cb[0]))));
    u4 h[16];
    {
      const int t = 2047 - (((l & 1) ? k2 : k1) & 2047), p = t >> 1, ti = max(p - 7, 0) / 68, u = p - 68 * ti, ts = u / 15;
      const unsigned vo = ((t & 1) * 15 + ti) * TILEB + ts * 16384 + g * 256 + (u - 15 * ts) * 16;
#pragma unroll
      for (int ks = 0; ks < 16; ++ks) h[ks] = ld16(ws, vo + (ks & 3) * 1024, WS_XB + L * BUFB + (ks >> 2) * 4096);
    }
    SB();
    const int s1 = __builtin_ctzll(__ballot(w1) | (1ull << 63)), s2 = __builtin_ctzll(__ballot(cb[0] == k2) | (1ull << 63));
    const unsigned ma = rdl(c0[1], s1), ra = rdl(c0[2], s1), mb = rdl(cb[1], s2), rb = rdl(cb[2], s2);
    if (wv == 0) st16(ws, (l & 1) ? (u4){k2, mb, rb, tag} : (u4){k1, ma, ra, tag}, l < 2 ? l * 16 : OOB, WS_SEL + L * 2048 + j * 128);
    const float m = __uint_as_float(bsel(mlo, ma, mb)), rs = __uint_as_float(bsel(mlo, ra, rb));
    SB();
    {
      f4 a[3];
#pragma unroll
      for (int ks = 0; ks < 16; ++ks)
#pragma unroll
        for (int c = 0; c < 3; ++c) { if (ks == 0) mAW<true>(a[c], h[ks], G[ks][c]); else mAW(a[c], h[ks], G[ks][c]); }
      SB();
      NOPS(8);
      SB();
      float gg, uu;
      gather(a[0], a[1], a[2], gg, uu);
      gg = (gg - m * csg) * rs; uu = (uu - m * csu) * rs;
      const float act = live ? gg * sigm(gg) * uu : 0.f;
      lds_w<u16>(actw, (u16)pk(act, act));
    }
    STAMP(5 + 8 * L);
    WAITL();
    BAR();
    {
      u4 b[3];
#pragma unroll
      for (int ks = 0; ks < 3; ++ks) b[ks] = lds_r<u4>(F_ACT + (l & 1) * 192 + ks * 64 + g * 16);
      const unsigned m2 = lmask(l & 2);
      const unsigned vo = lr < 4 ? j * 4096 + (l & 1) * 2048 + wv * 512 + (l & 2) * 32 + g * 16 : OOB;
      f4 y[8];
      SB();
#pragma unroll
      for (int c = 0; c < 8; ++c) { mWA<true>(y[c], Dn[0][c], b[0]); mWA(y[c], Dn[1][c], b[1]); mVV(y[c], Dn[2][c], b[2]); }
      SB();
#pragma unroll
      for (int c = 0; c < 4; ++c) {
        if (c == 0) { NOPS(1); SB(); }
        u4 w = asu(bsel4(m2, y[2 * c + 1], y[2 * c]));
        w[0] = umin(w[0], SENT - 1);
        st16(ws, w, vo, WS_YP + L * 65536 + c * 128);
        SB();
      }
    }
    STAMP(6 + 8 * L);
  }
  if (j == 0 && wv == 0) st4(ws, tag, l == 0 ? 0 : OOB, WS_EPOCH);
  STAMP_FLUSH();
}

__global__ void __launch_bounds__(256, 1) fused_kernel(Params P) {
  unsigned g_tent = 0;
  u64 tent; asm volatile("s_memrealtime %0\n\ts_waitcnt lgkmcnt(0)" : "=s"(tent) :: "memory");
  g_tent = (unsigned)tent;
  const int b = blockIdx.x, wv = __builtin_amdgcn_readfirstlane(threadIdx.x >> 6), l = threadIdx.x & 63;
  if (b < 240) role_a(P, b, wv, l, g_tent);
  else role_f(P, b - 240, wv, l, g_tent);
}

__global__ void k2(unsigned* p) { unsigned t = (unsigned)__builtin_amdgcn_s_memrealtime(); unsigned d = 5000;
  for (int i = 0; i < 256; ++i) d = min(d, t - p[i]);
  d *= K2MUL; for (int sp = 0; sp < (1 << 20) && (unsigned)__builtin_amdgcn_s_memrealtime() - t < d; ++sp) __builtin_amdgcn_s_sleep(2); }
extern "C" __device__ __attribute__((used, noinline)) void code_end_marker() {}

extern "C" void kernel_launch(void* const* d_in, const int* in_sizes, int n_in, void* d_out, int out_size, void* d_ws, size_t ws_size, hipStream_t stream) {
  Params P;
  P.x = (const u16*)d_in[0]; P.wqkv = (const u16*)d_in[1]; P.wo = (const u16*)d_in[2];
  P.wr = (const u16*)d_in[4]; P.wg = (const u16*)d_in[5]; P.wu = (const u16*)d_in[6]; P.wd = (const u16*)d_in[7];
  P.out = (u16*)d_out; P.ws = (char*)d_ws;
  (void)hipFuncSetAttribute((const void*)fused_kernel, hipFuncAttributeMaxDynamicSharedMemorySize, LDS_BYTES);
  fused_kernel<<<256, 256, LDS_BYTES, stream>>>(P);
  k2<<<1, 64, 0, stream>>>((unsigned*)((char*)d_ws + 2048));
}
```

```cpp
#include <hip/hip_runtime.h>
#include <hip/hip_bf16.h>
#include <stdint.h>

typedef unsigned short u16;
typedef unsigned long long u64;
typedef __bf16 bf2 __attribute__((ext_vector_type(2)));
typedef short s4 __attribute__((ext_vector_type(4)));
typedef float f4 __attribute__((ext_vector_type(4)));
typedef float f2 __attribute__((ext_vector_type(2)));
typedef unsigned u4 __attribute__((ext_vector_type(4)));
typedef unsigned u2 __attribute__((ext_vector_type(2)));
typedef __amdgpu_buffer_rsrc_t rsrc_t;
#define LAS __attribute__((address_space(3)))
#define DEV __device__ __forceinline__

constexpr unsigned TILEB = 81920;
constexpr unsigned BUFB = 30 * TILEB;
constexpr unsigned WS_EPOCH = 0;
constexpr unsigned WS_FF = 4096;
constexpr unsigned WS_SEL = 8192;
constexpr unsigned WS_CAND = 98304;
constexpr unsigned WS_FA = 32768;
constexpr unsigned WS_HU = 917504;
constexpr unsigned WS_XG = 983040;
constexpr unsigned WS_YP = 131072;
constexpr unsigned WS_ST = 262144;
constexpr unsigned STB = 30 * 8 * 80 * 16;
constexpr unsigned WS_OB = 1048576;
constexpr unsigned WS_XB = WS_OB + 2 * BUFB;
constexpr unsigned WS_USED = WS_XB + 2 * BUFB;
constexpr unsigned L_RING = 81920;
constexpr unsigned L_RED2 = L_RING + 16384;
constexpr unsigned L_MISC = 155648;
constexpr unsigned O_RED = 0;
constexpr unsigned O_RC = 4096, O_RM = O_RC + 320, O_RA = O_RM + 320, O_RB = O_RA + 320;
constexpr unsigned O_SEL = O_RB + 320;
constexpr unsigned LDS_BYTES = 163840;
constexpr unsigned SENT = 0xFFFFFFFFu, OOB = 0x80000000u, ONE2 = 0x3F803F80u;
#define T_O0 50
#define T_O1 75
#define T_ST 78
#define T_Y 105
#define LAG_O 15
#define LAG_ST 15
#define LAG_C 22
#define LAG_X 25
#define LAG_Y 15
#define HU_STEP 9
#define TOUCH 0
#define C1_BLOCK 13
#define CL_STEP 13
constexpr int SPIN_CAP = 1 << 15;
constexpr float EPS = 1e-5f;

#define CFENCE() asm volatile("" ::: "memory")
#define WAITC(vm, lg) do { CFENCE(); __builtin_amdgcn_s_waitcnt(((vm) & 15) | 0x70 | ((lg) << 8) | (((vm) >> 4) << 14)); CFENCE(); } while (0)
#define WAITV(n) asm volatile("s_waitcnt vmcnt(%0)" ::"i"(n) : "memory")
#define WAITL() WAITC(63, 0)
#define BAR() do { CFENCE(); __builtin_amdgcn_s_barrier(); CFENCE(); } while (0)
#define SB() __builtin_amdgcn_sched_barrier(0)
#define OPQ(x) asm volatile("" : "+v"(x))
#define KEEP(x) asm volatile("" ::"v"(x))

template <int V> struct IC { static constexpr int v = V; };
template <int I, int N, class F> DEV void sfor(F&& f) { if constexpr (I < N) { f(IC<I>{}); sfor<I + 1, N>(f); } }

typedef unsigned u16v __attribute__((ext_vector_type(16)));
struct Params {
  const u16* x; const u16* wqkv; const u16* wo; const u16* wr; const u16* wg; const u16* wu; const u16* wd;
  u16* out; char* ws;
};

struct KArg { u16v a; u2 b; };
DEV KArg args_go() {
  KArg K;
  asm volatile("s_load_dwordx16 %0, %2, 0x0\n\ts_load_dwordx2 %1, %2, 0x40" : "=&s"(K.a), "=&s"(K.b) : "s"(__builtin_amdgcn_kernarg_segment_ptr()));
  return K;
}
DEV Params args_wait(KArg K) {
  asm volatile("s_waitcnt lgkmcnt(0)" : "+s"(K.a), "+s"(K.b) :: "memory");
  auto p = [](unsigned lo, unsigned hi) { return (char*)(((u64)hi << 32) | lo); };
  Params P;
  P.x = (const u16*)p(K.a[0], K.a[1]); P.wqkv = (const u16*)p(K.a[2], K.a[3]); P.wo = (const u16*)p(K.a[4], K.a[5]); P.wr = (const u16*)p(K.a[6], K.a[7]);
  P.wg = (const u16*)p(K.a[8], K.a[9]); P.wu = (const u16*)p(K.a[10], K.a[11]); P.wd = (const u16*)p(K.a[12], K.a[13]); P.out = (u16*)p(K.a[14], K.a[15]);
  P.ws = p(K.b[0], K.b[1]);
  return P;
}
DEV rsrc_t mk(const void* p, unsigned bytes) { return __builtin_amdgcn_make_buffer_rsrc((void*)p, 0, bytes, 0x00020000); }
DEV void setm0(unsigned base, int imm) { asm volatile("s_add_u32 m0, %0, %1" ::"s"(base), "i"(imm) : "scc", "memory"); }
template <int SC> DEV void dma_go(rsrc_t rs, unsigned voff, unsigned soff) {
  if (SC) asm volatile("buffer_load_dwordx4 %0, %1, %2 offen sc1 lds" ::"v"(voff), "s"(rs), "s"(soff) : "memory");
  else asm volatile("buffer_load_dwordx4 %0, %1, %2 offen lds" ::"v"(voff), "s"(rs), "s"(soff) : "memory");
}
DEV void dma_go_m(rsrc_t rs, unsigned voff, unsigned soff, u64 mask) {
  asm volatile("s_mov_b64 exec, %3\n\tbuffer_load_dwordx4 %0, %1, %2 offen lds\n\ts_mov_b64 exec, -1" ::"v"(voff), "s"(rs), "s"(soff), "s"(mask) : "memory");
}
template <int SC> DEV void dma(rsrc_t rs, unsigned voff, unsigned soff, unsigned base, int imm) {
  if (SC) asm volatile("s_add_u32 m0, %3, %4\n\ts_nop 0\n\tbuffer_load_dwordx4 %0, %1, %2 offen sc1 lds" ::"v"(voff), "s"(rs), "s"(soff), "s"(base), "i"(imm) : "scc", "memory");
  else asm volatile("s_add_u32 m0, %3, %4\n\ts_nop 0\n\tbuffer_load_dwordx4 %0, %1, %2 offen lds" ::"v"(voff), "s"(rs), "s"(soff), "s"(base), "i"(imm) : "scc", "memory");
}
DEV void st16(rsrc_t rs, u4 v, unsigned voff, unsigned soff) {
  asm volatile("buffer_store_dwordx4 %0, %1, %2, %3 offen sc1\n\ts_nop 1" ::"v"(v), "v"(voff), "s"(rs), "s"(soff) : "memory");
}
DEV void touch_go(unsigned& d, rsrc_t rs, unsigned voff, unsigned soff) { asm volatile("buffer_load_dword %0, %1, %2, %3 offen" : "=v"(d) : "v"(voff), "s"(rs), "s"(soff) : "memory"); }
DEV void wrap_add(unsigned& o) { asm volatile("s_add_u32 %0, %0, 0x18000\n\ts_and_b32 %0, %0, 0x7ffff" : "+s"(o) :: "scc"); }
DEV void st16c(rsrc_t rs, u4 v, unsigned voff, unsigned soff) {
  asm volatile("buffer_store_dwordx4 %0, %1, %2, %3 offen sc1" ::"v"(v), "v"(voff), "s"(rs), "s"(soff) : "memory");
}
DEV void st4(rsrc_t rs, unsigned v, unsigned voff, unsigned soff) {
  asm volatile("buffer_store_dword %0, %1, %2, %3 offen sc1" ::"v"(v), "v"(voff), "s"(rs), "s"(soff) : "memory");
}
DEV u4 ld16(rsrc_t rs, unsigned voff, unsigned soff) { return __builtin_amdgcn_raw_buffer_load_b128(rs, voff, soff, 16); }
DEV unsigned ld4(rsrc_t rs, unsigned voff, unsigned soff) { return __builtin_amdgcn_raw_buffer_load_b32(rs, voff, soff, 16); }

template <bool Z = false> DEV void mWA(f4& c, u4 w, u4 a) {
  if (Z) asm volatile("v_mfma_f32_16x16x32_bf16 %0, %1, %2, 0" : "=v"(c) : "a"(w), "v"(a));
  else asm volatile("v_mfma_f32_16x16x32_bf16 %0, %1, %2, %0" : "+v"(c) : "a"(w), "v"(a));
}
template <bool Z = false> DEV void mAW(f4& c, u4 a, u4 w) {
  if (Z) asm volatile("v_mfma_f32_16x16x32_bf16 %0, %1, %2, 0" : "=v"(c) : "v"(a), "a"(w));
  else asm volatile("v_mfma_f32_16x16x32_bf16 %0, %1, %2, %0" : "+v"(c) : "v"(a), "a"(w));
}
template <bool Z = false> DEV void mVV(f4& c, u4 a, u4 b) {
  if (Z) asm volatile("v_mfma_f32_16x16x32_bf16 %0, %1, %2, 0" : "=v"(c) : "v"(a), "v"(b));
  else asm volatile("v_mfma_f32_16x16x32_bf16 %0, %1, %2, %0" : "+v"(c) : "v"(a), "v"(b));
}
template <bool Z = false> DEV void m16(f4& c, u2 a, u2 b) {
  if (Z) asm volatile("v_mfma_f32_16x16x16_bf16 %0, %1, %2, 0" : "=v"(c) : "v"(a), "v"(b));
  else asm volatile("v_mfma_f32_16x16x16_bf16 %0, %1, %2, %0" : "+v"(c) : "v"(a), "v"(b));
}
DEV f4 m16c(u2 a, u2 b, f4 c) { f4 d; asm volatile("v_mfma_f32_16x16x16_bf16 %0, %1, %2, %3" : "=&v"(d) : "v"(a), "v"(b), "v"(c)); return d; }
template <bool Z = false> DEV void mf32(f4& c, float a, float b) {
  if (Z) asm volatile("v_mfma_f32_16x16x4_f32 %0, %1, %2, 0" : "=&v"(c) : "v"(a), "v"(b));
  else asm volatile("v_mfma_f32_16x16x4_f32 %0, %1, %2, %0" : "+v"(c) : "v"(a), "v"(b));
}
#define NOPS(n) asm volatile("s_nop %0" ::"i"((n) - 1))
DEV unsigned pk(float a, float b) { f2 v = {a, b}; return __builtin_bit_cast(unsigned, __builtin_convertvector(v, bf2)); }
DEV u2 pk4(f4 v) { return (u2){pk(v[0], v[1]), pk(v[2], v[3])}; }
DEV float lo(unsigned u) { return __uint_as_float(u << 16); }
DEV float hi(unsigned u) { return __uint_as_float(u & 0xFFFF0000u); }
DEV f4 up4(u2 u) { return (f4){lo(u[0]), hi(u[0]), lo(u[1]), hi(u[1])}; }
DEV f4 asf(u4 v) { return __builtin_bit_cast(f4, v); }
DEV u4 asu(f4 v) { return __builtin_bit_cast(u4, v); }
DEV unsigned bsel(unsigned m, unsigned a, unsigned b) { return __builtin_amdgcn_bitop3_b32(m, a, b, 0xCA); }
DEV float bself(unsigned m, float a, float b) { return __uint_as_float(bsel(m, __float_as_uint(a), __float_as_uint(b))); }
DEV f4 bsel4(unsigned m, f4 a, f4 b) { return (f4){bself(m, a[0], b[0]), bself(m, a[1], b[1]), bself(m, a[2], b[2]), bself(m, a[3], b[3])}; }
DEV unsigned lmask(bool c) { unsigned m = c ? ~0u : 0u; OPQ(m); return m; }
DEV float fmx(float a, float b) { return __builtin_elementwise_maximum(a, b); }
template <class T> DEV T lds_r(unsigned a) { return *(const LAS T*)(size_t)a; }
template <class T> DEV void lds_w(unsigned a, T v) { *(LAS T*)(size_t)a = v; }
DEV u4 trfrag2(unsigned a, unsigned step) {
  u2 x = __builtin_bit_cast(u2, __builtin_amdgcn_ds_read_tr16_b64_v4i16((LAS s4*)(size_t)a));
  u2 y = __builtin_bit_cast(u2, __builtin_amdgcn_ds_read_tr16_b64_v4i16((LAS s4*)(size_t)(a + step)));
  return (u4){x[0], x[1], y[0], y[1]};
}
DEV f2 swap16(float a, float b) { auto r = __builtin_amdgcn_permlane16_swap(__float_as_uint(a), __float_as_uint(b), false, false); return (f2){__uint_as_float(r[0]), __uint_as_float(r[1])}; }
DEV f2 swap32(float a, float b) { auto r = __builtin_amdgcn_permlane32_swap(__float_as_uint(a), __float_as_uint(b), false, false); return (f2){__uint_as_float(r[0]), __uint_as_float(r[1])}; }
DEV float xadd16(float v) { f2 r = swap16(v, v); return r[0] + r[1]; }
DEV float xadd32(float v) { f2 r = swap32(v, v); return r[0] + r[1]; }
template <int C> DEV unsigned dpp(unsigned v) { return __builtin_amdgcn_update_dpp(0u, v, C, 0xF, 0xF, true); }
template <int C> DEV float dppf(float v) { return __uint_as_float(dpp<C>(__float_as_uint(v))); }
DEV float wave_sum(float v) {
  v += dppf<0xB1>(v); v += dppf<0x4E>(v); v += dppf<0x141>(v); v += dppf<0x140>(v);
  return xadd32(xadd16(v));
}
DEV unsigned max_row16(unsigned v) { v = max(v, dpp<0xB1>(v)); v = max(v, dpp<0x4E>(v)); v = max(v, dpp<0x141>(v)); return max(v, dpp<0x140>(v)); }
DEV unsigned max_x16(unsigned v) { auto r = __builtin_amdgcn_permlane16_swap(v, v, false, false); return max(r[0], r[1]); }
DEV unsigned max_x32(unsigned v) { auto r = __builtin_amdgcn_permlane32_swap(v, v, false, false); return max(r[0], r[1]); }
DEV unsigned umin(unsigned a, unsigned b) { return a < b ? a : b; }
DEV float dot2(unsigned a, unsigned b, float c) { return __builtin_amdgcn_fdot2_f32_bf16(__builtin_bit_cast(bf2, a), __builtin_bit_cast(bf2, b), c, false); }
DEV unsigned rfl(unsigned v) { return __builtin_amdgcn_readfirstlane(v); }
DEV unsigned rdl(unsigned v, int l) { return __builtin_amdgcn_readlane(v, l); }
DEV float rsq(float v) { return __builtin_amdgcn_rsqf(v); }
DEV float sigm(float v) { return __builtin_amdgcn_rcpf(1.f + __builtin_amdgcn_exp2f(-1.44269504f * v)); }
DEV float key_score(unsigned k) { unsigned u = k & 0xFFFFF000u; return __uint_as_float((u & 0x80000000u) ? (u ^ 0x80000000u) : ~u); }

#define TC(set, i)
#define TCFLUSH(set, n)
#define CNT(i)
#define STAMP(i)
#define STAMP_INIT()
#define STAMP_FLUSH()
DEV unsigned tag_issue(const char* ws) { unsigned e; asm volatile("s_load_dword %0, %1, 0x0" : "=s"(e) : "s"(ws) : "memory"); return e; }
DEV unsigned tag_finish(unsigned e) { unsigned t; asm volatile("s_waitcnt lgkmcnt(0)\n\ts_add_u32 %0, %1, 1" : "=s"(t) : "s"(e) : "memory", "scc"); return t; }

DEV unsigned peek4(rsrc_t ws, unsigned voff, unsigned soff) {
  unsigned z = 0;
  asm volatile("buffer_atomic_or %0, %1, %2, %3 offen sc0 sc1\n\ts_waitcnt vmcnt(0)" : "+v"(z) : "v"(voff), "s"(ws), "s"(soff) : "memory");
  return z;
}
DEV u2 peek8(rsrc_t ws, unsigned voff, unsigned soff) {
  u2 z = {0, 0};
  asm volatile("buffer_atomic_or_x2 %0, %1, %2, %3 offen sc0 sc1\n\ts_waitcnt vmcnt(0)" : "+v"(z) : "v"(voff), "s"(ws), "s"(soff) : "memory");
  return z;
}
DEV void peek8_go(u2& z, rsrc_t ws, unsigned voff, unsigned soff) {
  asm volatile("buffer_atomic_or_x2 %0, %1, %2, %3 offen sc0 sc1" : "+v"(z) : "v"(voff), "s"(ws), "s"(soff) : "memory");
}
template <int OFF = 0> DEV void ld16_go(u4& v, rsrc_t ws, unsigned voff, unsigned soff) {
  asm volatile("buffer_load_dwordx4 %0, %1, %2, %3 offen offset:%4 sc1" : "=v"(v) : "v"(voff), "s"(ws), "s"(soff), "i"(OFF) : "memory");
}
DEV u64 time_go() { u64 t; asm volatile("s_memrealtime %0" : "=s"(t) :: "memory"); return t; }
DEV unsigned time_get(u64 t) { asm volatile("s_waitcnt lgkmcnt(0)" : "+s"(t) :: "memory"); return (unsigned)t; }
DEV unsigned now() { return (unsigned)__builtin_amdgcn_s_memrealtime(); }
DEV void wait_gate(rsrc_t ws, u2 z, unsigned voff, unsigned soff, unsigned tag, int lag) {
  unsigned t = z[0];
  bool need = voff != OOB && z[1] != tag;
#pragma unroll 1
  for (int sp = 0; sp < SPIN_CAP && __any(need); ++sp) {
    z = peek8(ws, need ? voff : OOB, soff);
    t = need ? z[0] : t;
    need = need && z[1] != tag;
  }
  t += lag;
#pragma unroll 1
  for (int sp = 0; sp < 64 && __any(voff != OOB && (int)(t - now()) > 0); ++sp) {}
}
DEV void wait_flags_ld(rsrc_t ws, unsigned voff, unsigned soff, unsigned tag) {
#pragma unroll 1
  for (int sp = 0; sp < SPIN_CAP; ++sp) {
    CFENCE();
    if (__all(ld4(ws, voff, soff) == tag)) break;
    __builtin_amdgcn_s_sleep(1);
  }
}
DEV unsigned wait_word(rsrc_t ws, unsigned voff, unsigned soff, unsigned tag) {
  u2 z;
#pragma unroll 1
  for (int sp = 0; sp < SPIN_CAP; ++sp) { z = peek8(ws, voff, soff); if (__all(z[1] == tag || voff == OOB)) break; __builtin_amdgcn_s_sleep(2); }
  return z[0];
}
DEV void st8(rsrc_t rs, u2 v, unsigned voff, unsigned soff) {
  asm volatile("buffer_store_dwordx2 %0, %1, %2, %3 offen sc1" ::"v"(v), "v"(voff), "s"(rs), "s"(soff) : "memory");
}
DEV void pause(int ticks) {
  const unsigned t = (unsigned)__builtin_amdgcn_s_memrealtime();
#pragma unroll 1
  for (int sp = 0; sp < 256 && (unsigned)__builtin_amdgcn_s_memrealtime() - t < (unsigned)ticks; ++sp) {}
}

struct P1 {
  static constexpr int op(int s, int k) {
    constexpr int T[16][5] = {{72, 73, 3, -1, -1}, {74, 75, 4, -1, -1}, {76, 5, -1, -1, -1}, {80, 6, -1, -1, -1}, {81, 82, 7, -1, -1}, {83, 84, 8, -1, -1},
                              {9, -1, -1, -1, -1}, {88, 10, -1, -1, -1}, {89, 90, 11, -1, -1}, {91, 92, 12, -1, -1}, {13, -1, -1, -1, -1},
                              {14, -1, -1, -1, -1}, {15, -1, -1, -1, -1}, {-1, -1, -1, -1, -1}, {128, -1, -1, -1, -1}, {129, 130, -1, -1, -1}};
    return T[s][k];
  }
  static constexpr int size(int o) { return o < 0 ? 0 : (o >> 6) == 1 ? 1 : 3; }
  static constexpr int ndma(int s) { int n = 0; for (int k = 0; k < 5; ++k) n += size(op(s, k)); return n; }
  static constexpr int ntouch(int s) { return (TOUCH & 2) && s == 0 ? 1 : (TOUCH & 4) && s == 1 ? 1 : 0; }
  static constexpr int nops(int s) { return ndma(s) + ntouch(s) + (s == C1_BLOCK ? 3 : 0); }
  static constexpr int sub(int s, int d) { for (int k = 0; k < 5; ++k) { if (d < size(op(s, k))) return op(s, k) * 4 + d; d -= size(op(s, k)); } return -1; }
  static constexpr int pos(int o) {
    if (o < 3) return 8 + 3 * o;
    int n = 15;
    for (int s = 0; s < 16; ++s) { int m = n; for (int k = 0; k < 5; ++k) { m += size(op(s, k)); if (op(s, k) == o) return m; } n += nops(s); }
    return 0;
  }
  static constexpr int nv(int s) {
    int n = 15; for (int t = 0; t < s; ++t) n += nops(t);
    int need = pos(s);
    if (s == 4 || s == 8 || s == 12) { const int x = pos(64 + (s >> 2) * 8 + 4); need = x > need ? x : need; }
    return n - need;
  }
};
struct RowC { float c[5], cm[5]; f4 c4[5], cm4[5]; };
DEV void rowc_read(RowC& R, unsigned r1, unsigned r4, int i) {
  const int t = i / 3;
  if (i % 3 == 0) { R.c[t] = lds_r<float>(r1 + t * 64); R.cm[t] = lds_r<float>(r1 + 320 + t * 64); }
  else if (i % 3 == 1) R.c4[t] = lds_r<f4>(r4 + t * 64);
  else R.cm4[t] = lds_r<f4>(r4 + 320 + t * 64);
}

DEV void role_a(const int b, unsigned g_tent) {
  const KArg K = args_go();
  const int wv = __builtin_amdgcn_readfirstlane(threadIdx.x >> 6), l = threadIdx.x & 63;
  unsigned pfc;
  {
    u64 pc; unsigned len;
    asm volatile("s_getpc_b64 %0\n.Lpf_here:\n\ts_mov_b32 %1, code_end_marker-.Lpf_here" : "=s"(pc), "=s"(len));
    const rsrc_t rc = mk((const void*)pc, len);
    asm volatile("buffer_load_dword %0, %1, %2, %3 offen" : "=v"(pfc) : "v"(min(l, 2) * 128), "s"(rc), "s"(((b >> 3) * 4 + wv) * 384) : "memory");
  }
  const int xcd = b & 7, r = b >> 3;
  const int q = (xcd >> 1) * 30 + r, tile = q >> 2, hg = (xcd & 1) * 4 + (q & 3);
  const int lr = l & 15, g = l >> 4;
  const int par = tile >= 15, p0 = 68 * (tile - 15 * par), rr = 4 * wv + g, k8 = l >> 3;
  unsigned xoff = (2 * (p0 + rr) + par) * 1024 + ((lr ^ rr) << 4);
  const unsigned swA = (((l & 7) ^ (((k8 >> 1) & 1) << 1)) << 4) ^ ((wv & 1) << 6);
  unsigned woffA = k8 * 3072 + hg * 128 + swA, woffB = woffA ^ 64, wro = (16 * wv + 4 * g + 64 * hg) * 2;
  unsigned lx = wv * 1024, lw = L_RING + wv * 3072, wso[3];
  u64 xmask = wv == 3 ? 0x0000FFFFFFFFFFFFull : ~0ull;
#pragma unroll
  for (int j = 0; j < 3; ++j) { const int i = wv * 3 + j; wso[j] = (i & 3) * 8 * 3072 + (i >> 2) * 1024; }
  asm volatile("" : "+v"(xoff), "+v"(woffA), "+v"(woffB), "+v"(wro));
  asm volatile("" : "+s"(lx), "+s"(lw), "+s"(wso[0]), "+s"(wso[1]), "+s"(wso[2]), "+s"(xmask));
  const Params P = args_wait(K);
  const rsrc_t ws = mk(P.ws, WS_USED);
  const unsigned epoch = tag_issue(P.ws);
  STAMP_INIT();
  STAMP(0);
  u4 W[16][3];
  f4 aq[5], ak[5], av[5];
  u2 xp[5];
  RowC R;
  unsigned tag, pm;
  f4 mask;
  u2 wr2, spa[4];
  unsigned trb, ooff, ooff4, lo2, l16, fao, l0, lw0, row3, sv_, hg0, so_[4], t4[3];
  {
    const rsrc_t rx = mk(P.x, 2048 * 1024), rw = mk(P.wqkv, 512 * 3072), rwo = mk(P.wo, 512 * 1024);
    auto ldx = [&](int pr) {
      asm volatile("s_mov_b64 exec, %0" ::"s"(xmask) : "memory");
#pragma unroll
      for (int j = 0; j < 5; ++j) dma<0>(rx, xoff, pr * 256 + j * 30720, lx, pr * 20480 + j * 4096);
      asm volatile("s_mov_b64 exec, -1" ::: "memory");
    };
    auto ldw = [&](int s) {
#pragma unroll
      for (int j = 0; j < 3; ++j) dma<0>(rw, (j & 1) ? woffB : woffA, wso[j] + s * 98304, lw, (s % 6) * 12288 + j * 1024);
    };
    asm volatile("global_load_dwordx2 %0, %1, %2" : "=v"(wr2) : "v"(wro), "s"(P.wr) : "memory");
    SB();
    ldx(0);
    SB();
    ldw(0); ldw(1); ldw(2);
    unsigned touch, touch1 = 0, touch2 = 0;
    auto wline = [&](int first, int per) {
      const int idx = first * 384 + (r * 4 + wv) * per + min(l, per - 1);
      return (idx >> 2) * 1024 + (idx & 3) * 128;
    };
    if (TOUCH & 1) {
      asm volatile("buffer_load_dword %0, %1, %2, %3 offen" : "=v"(touch) : "v"(wline(4, 16)), "s"(rw), "s"((xcd & 1) * 512) : "memory");
    } else {
      const int rh = (3 - (int)(((size_t)P.wqkv) >> 7)) & 3;
      asm volatile("buffer_load_dword %0, %1, %2, %3 offen" : "=v"(touch) : "v"(min(l, 12) * 1024), "s"(rw), "s"((r * 52 + wv * 13) * 1024 + ((xcd & 1) * 4 + rh) * 128) : "memory");
    }
    unsigned to1 = (2 * (p0 + (q & 3) * 19 + wv * 5 + min(l, 29) / 6) + par) * 1024 + 256 + (min(l, 29) % 6) * 128, to2 = wline(9, 23);
    OPQ(to1); OPQ(to2);
    woffA += 3 * 98304; woffB += 3 * 98304;
    SB();
    {
      const u4 one = {ONE2, ONE2, ONE2, ONE2};
      lds_w<u4>(rr * 4096 + 15 * 256 + lr * 16, one);
      lds_w<u4>(g * 4096 + 16 * 4096 + 15 * 256 + lr * 16, one);
    }
    ooff = (((g & 1) * 32 + wv) * 2 + (g >> 1)) * 256 + lr * 16;
    ooff4 = ooff | ((g & 1) << 31);
    lo2 = ooff + 65536 - (g & 1) * (65536 - 2048);
    l16 = l * 16;
    fao = wv == 0 && l < 32 ? tile * 1024 + l * 32 : OOB;
    lw0 = l == 0 ? 0 : OOB;
    OPQ(lw0);
    row3 = wv < 2 ? wv * 24 + min(l, 23) : 16 + wv * 16 + min(l, 15);
    sv_ = l < (wv < 2 ? 24 : 16) ? row3 * 16 : OOB;
    hg0 = wv == 0 && l < 2 ? l * 65536 : OOB;
#pragma unroll
    for (int i = 0; i < 3; ++i) { so_[i] = WS_OB + tile * TILEB + ((2 * hg + 2 + 4 * i + wv) & 15) * 1024; t4[i] = min((wv >> 1) * 3 + i, 4) * 16384; }
    so_[3] = WS_OB + tile * TILEB + ((2 * hg + 14 + (wv & 1)) & 15) * 1024;
    asm volatile("" : "+s"(so_[0]), "+s"(so_[1]), "+s"(so_[2]), "+s"(so_[3]), "+s"(t4[0]), "+s"(t4[1]), "+s"(t4[2]));
    OPQ(row3); OPQ(sv_); OPQ(hg0);
    OPQ(lo2); OPQ(l16); OPQ(fao);
    unsigned obase = WS_OB + tile * TILEB + hg * 2048, wo5 = ((15 + 2 * hg) & 15) * 32768 + wv * 8192;
    u4 sent = {SENT, SENT, SENT, SENT};
    unsigned wooffA = k8 * 1024 + hg * 128 + swA, wooffB = wooffA ^ 64;
    unsigned xa[4], xh[4], oso[3];
#pragma unroll
    for (int i = 0; i < 4; ++i) { xa[i] = lr * 256 + (((4 * i + g) ^ lr) << 4); xh[i] = xa[i] + 61440; OPQ(xa[i]); OPQ(xh[i]); }
#pragma unroll
    for (int j = 0; j < 3; ++j) { const int i = wv * 3 + j; oso[j] = (((i >> 2) + 2 * hg) & 15) * 32768 + (i & 3) * 8192; }
    unsigned xg = lr * 256 + (((4 * wv + g) ^ lr) << 4), xgh = xg + 61440;
    unsigned xr = (hg >> 1) * 20480 + lr * 256 + ((((hg & 1) * 8 + 2 * wv + (g >> 1)) ^ lr) << 4) + 8 * (g & 1);
    const unsigned dum = L_MISC + O_RED + 2560 + l * 4, red = L_MISC + O_RED + (wv * 80 + lr) * 8;
    unsigned mq = bsel(lmask(g == (lr >> 2)), red + 4, dum), ms = bsel(lmask(g == 3), red, dum);
    unsigned rb_ = L_MISC + O_RED + (wv * 20 + min(l, 19)) * 8, rc_ = L_MISC + O_RC + (wv * 20 + min(l, 19)) * 4;
    unsigned r1 = L_MISC + O_RC + lr * 4, r4 = L_MISC + O_RC + g * 16;
    l0 = (wv * 64 + l) == 0 ? 0 : OOB;
    const unsigned m1 = lmask(l & 1), m2 = lmask(l & 2);
    unsigned mt[4], sA = ONE2 & ~(m1 | m2), sB = m1 & ~m2;
#pragma unroll
    for (int t = 0; t < 4; ++t) mt[t] = lmask((lr >> 2) == t);
    pm = lmask(lr < 8 && g >= 2);
#pragma unroll
    for (int i = 0; i < 4; ++i) { const int rk = 4 * g + i, d = lr + (pm & 15) - rk; mask[i] = ((unsigned)d <= 7 && rk != 15) ? 0.f : -1e30f; }
    trb = L_RING + (8 * g + (lr >> 2)) * 128 + (((2 * wv + ((l & 3) >> 1)) ^ ((((lr >> 3) & 1) | ((g & 1) << 1)) << 1)) << 4) + 8 * (l & 1);
    OPQ(ooff); OPQ(ooff4); OPQ(sent); OPQ(wooffA); OPQ(wooffB); OPQ(xg); OPQ(xgh); OPQ(xr); OPQ(mq); OPQ(ms); OPQ(rb_); OPQ(rc_); OPQ(r1); OPQ(r4); OPQ(l0);
    OPQ(sA); OPQ(sB); OPQ(mask); OPQ(trb);
    asm volatile("" : "+s"(obase), "+s"(wo5), "+s"(oso[0]), "+s"(oso[1]), "+s"(oso[2]));
    OPQ(woffA); OPQ(woffB);
    tag = tag_finish(epoch);
    f4 gr[5];
    u4 a[2][5], z[2][5];
    f2 rv[4];
    float dg, sa, sb;
    sfor<0, 17>([&](auto S_) __attribute__((always_inline)) {
      constexpr int s = decltype(S_)::v;
      if constexpr (s < 16) WAITC(P1::nv(s), 0); else WAITL();
      BAR();
      TC(1, s);
      if (s == 0) STAMP(1);
      if (s == 4) STAMP(19);
      if (s == 8) STAMP(20);
      if (s == 12) STAMP(21);
      if (s == 16) STAMP(22);
      SB();
      constexpr auto gtile = [](int s_, int i) constexpr {
        if (s_ < 0) return -1;
        if (s_ < 5) return i == 0 ? s_ : -1;
        if (s_ < 7) return i < 2 ? 8 + 2 * (s_ - 5) + i : -1;
        if (s_ == 7) return i == 0 ? 12 : -1;
        if (s_ == 8) return i < 2 ? 16 + i : -1;
        if (s_ < 12) return i == 0 ? 16 + s_ - 7 : -1;
        if (s_ == 12) return 24 + i;
        return -1;
      };
      constexpr int nd = s < 16 ? P1::ndma(s) : 0, ng0 = s == 13 ? 5 : 0;
      sfor<0, 20>([&](auto I_) __attribute__((always_inline)) {
        constexpr int i = decltype(I_)::v;
        if constexpr (s > 0) {
          constexpr int pb = (s - 1) & 1, im = i - ng0, t = im / 3, ig = ng0 ? i : i - 15;
          if constexpr (im >= 0 && im < 15) {
            if constexpr (im % 3 == 0) mWA<s == 1>(aq[t], W[s - 1][0], a[pb][t]); else if constexpr (im % 3 == 1) mWA<s == 1>(ak[t], W[s - 1][1], a[pb][t]); else mAW<s == 1>(av[t], a[pb][t], W[s - 1][2]);
          } else if constexpr (gtile(s - 1, ig) >= 0) mVV<(gtile(s - 1, ig) < 8)>(gr[gtile(s - 1, ig) & 7], z[pb][ig], z[pb][ig]);
        }
        if constexpr (s < 16) {
          constexpr bool dense = nd > 5;
          constexpr int d0 = dense ? i - 3 : ((i - 3) & 1) == 0 ? (i - 3) / 2 : -1, d1 = dense ? i - 3 : ((i - 3) & 1) == 1 ? (i - 3) / 2 : -1;
          if constexpr (i >= 3 && d0 >= 0 && d0 < nd) {
            constexpr int o = P1::sub(s, d0) >> 2, j = P1::sub(s, d0) & 3;
            if constexpr ((o >> 6) == 0) setm0(lw, (o % 6) * 12288 + j * 1024);
            else if constexpr ((o >> 6) == 1) setm0(lx, ((o >> 3) & 7) * 20480 + (o & 7) * 4096);
            else setm0(lw, ((4 + (o & 7)) % 6) * 12288 + j * 1024);
          }
          if constexpr (i < 3) W[s][i] = trfrag2(trb + (s % 6) * 12288 + i * 4096, 512);
          else if constexpr (i < 8) a[s & 1][i - 3] = lds_r<u4>((s < 12 ? xa[s & 3] + (s >> 2) * 20480 : xh[s & 3]) + (i - 3) * 4096);
          else if constexpr (i < 13 && gtile(s, i - 8) >= 0) z[s & 1][i - 8] = lds_r<u4>((s < 12 ? xg + (gtile(s, i - 8) >> 3) * 20480 : xgh) + (gtile(s, i - 8) & 7) * 4096);
          if constexpr (i >= 3 && d1 >= 0 && d1 < nd) {
            constexpr int o = P1::sub(s, d1) >> 2, j = P1::sub(s, d1) & 3;
            if constexpr (dense || s == 0) { SB(); if constexpr ((o >> 6) != 1) NOPS(1); }
            if constexpr ((o >> 6) == 0) {
              dma_go<0>(rw, (j & 1) ? woffB : woffA, wso[j]);
              if constexpr (o < 15 && j == 1) { woffB += 98304; OPQ(woffB); }
              if constexpr (o < 15 && j == 2) { woffA += 98304; OPQ(woffA); }
            } else if constexpr ((o >> 6) == 1) dma_go_m(rx, xoff, ((o >> 3) & 7) * 256 + (o & 7) * 30720, xmask);
            else { dma_go<0>(rwo, (j & 1) ? wooffB : wooffA, oso[j]); wrap_add(oso[j]); }
          }
          if constexpr (P1::ntouch(s) && i == 14) { if constexpr (s == 0) touch_go(touch1, rx, to1, 0); else touch_go(touch2, rw, to2, (xcd & 1) * 512); }
          if constexpr (s == C1_BLOCK && (i == 5 || i == 8 || i == 11)) st16c(ws, sent, i < 11 ? ooff : ooff4, obase + (i - 5) / 3 * 32768);
          if constexpr (s == C1_BLOCK && i == 14) KEEP(sent);
          if constexpr (s == 2 && i == 13) { OPQ(wr2); wr2 = (wr2 & sB) | sA; OPQ(wr2); }
          if constexpr ((s == 5 || s == 7) && (i == 13 || i == 14)) { constexpr int t = (s - 5) + i - 13; spa[t] = wr2 & mt[t]; OPQ(spa[t]); }
          constexpr int rt = s == 13 && i >= 13 && i < 19 ? i - 13 : s == 14 && i >= 11 && i < 15 ? i - 5 : -1;
          if constexpr (rt >= 0 && (rt & 1) == 0) { const f4 v = gr[rt / 2]; dg = bself(m2, bself(m1, v[3], v[2]), bself(m1, v[1], v[0])); OPQ(dg); }
          if constexpr (rt >= 0 && (rt & 1) == 1) { lds_w<float>(mq + (rt / 2) * 128, dg); lds_w<float>(ms + (rt / 2) * 128, gr[rt / 2][3]); }
          if constexpr (s == 14 && i >= 8 && i < 11) xp[i - 8] = lds_r<u2>(xr + (i - 8) * 4096);
          if constexpr (s == 15 && (i == 13 || i == 14)) xp[i - 10] = lds_r<u2>(xr + (i - 10) * 4096);
          if constexpr (s == 15 && i == 0) { rv[0] = lds_r<f2>(rb_); rv[1] = lds_r<f2>(rb_ + 640); rv[2] = lds_r<f2>(rb_ + 1280); rv[3] = lds_r<f2>(rb_ + 1920); }
          if constexpr (s == 15 && i == 8) { sa = rv[0][0] + rv[1][0]; OPQ(sa); sb = rv[0][1] + rv[1][1]; OPQ(sb); }
          if constexpr (s == 15 && i == 9) { sa += rv[2][0]; OPQ(sa); }
          if constexpr (s == 15 && i == 10) { sb += rv[2][1]; OPQ(sb); }
          if constexpr (s == 15 && i == 11) { sa += rv[3][0]; OPQ(sa); }
          if constexpr (s == 15 && i == 12) { sb += rv[3][1]; OPQ(sb); sa *= 1.f / 512; OPQ(sa); }
          if constexpr (s == 15 && i == 13) { sb = sb * (1.f / 512) - sa * sa; OPQ(sb); }
          if constexpr (s == 15 && i == 14) { sb = rsq(fmx(sb, 0.f) + EPS); lds_w<float>(rc_, sb); lds_w<float>(rc_ + 320, sb * sa); lds_w<float>(rc_ + 640, 1.f); lds_w<float>(rc_ + 960, 0.f); }
        } else {
          if (i < 15) rowc_read(R, r1, r4, i);
          if constexpr (i < 12 && (i & 1) == 0) setm0(lw, (1 + i / 6) * 12288 + (i % 6) / 2 * 1024);
          if constexpr (i < 12 && (i & 1) == 1) { constexpr int j = (i % 6) / 2; dma_go<0>(rwo, (j & 1) ? wooffB : wooffA, oso[j]); if constexpr (i < 6) wrap_add(oso[j]); }
          if constexpr (i == 12) setm0(lx, L_RING + 3 * 12288);
          if constexpr (i == 13) dma_go<0>(rwo, wooffA, wo5);
        }
        SB();
      });
    });
    asm volatile("" ::"v"(touch), "v"(touch1), "v"(touch2), "v"(pfc));
  }
  NOPS(2);
  SB();
  f4 csq, csk;
#pragma unroll
  for (int i = 0; i < 4; ++i) { csq[i] = dppf<0x15F>(aq[0][i]); csk[i] = dppf<0x15F>(ak[0][i]); }
  float csv;
  { f2 x = swap32(av[0][3], av[0][3]); f2 y = swap16(x[1], x[1]); csv = y[1]; }
  TC(1, 17);
  TCFLUSH(1, 18);
  STAMP(2);
  u4 WO[16];

#pragma unroll 1
  for (int L = 0;; ++L) {
#define FRESH(n) int l##n = l, wv##n = wv, hg##n = hg, tile##n = tile; asm volatile("" : "+v"(l##n)); asm volatile("" : "+s"(wv##n), "+s"(hg##n), "+s"(tile##n)); \
    const int lr##n = l##n & 15, g##n = l##n >> 4; (void)lr##n; (void)g##n;
    FRESH(1)
    const unsigned ob = WS_OB + L * BUFB + tile1 * TILEB;
    u2 kb[5], vb[5], qb[5], o[5];
    u2 one2 = {ONE2, ONE2};
    float kap = 0.36067376f;
    OPQ(one2); OPQ(kap);
    const unsigned gate = WS_FA + L * 32768 + (tile1 * 8 + hg1) * 128 + wv1 * 32;
    u2 fa = {0, 0}, zs = {0, 0};
    unsigned ff;
    {
      const bool w = L == 1 && wv1 == 0;
      asm volatile("buffer_load_dword %0, %1, %2, %3 offen sc1" : "=v"(ff) : "v"(w ? lr1 * 128 : OOB), "s"(ws), "s"(WS_FF) : "memory");
      peek8_go(zs, ws, w && l1 < 3 ? l1 * 8 : OOB, WS_SEL + (b & 15) * 128);
    }
    const f4 neg = {-1e30f, -1e30f, -1e30f, -1e30f};
    auto ex = [](float x) { return __builtin_amdgcn_exp2f(x); };
    {
      f4 ss[5], sp[5], sc[5], sum[5], oa[5];
      u2 pb[5], ps[5], pp[5];
      float mx[5];
      sfor<0, 5>([&](auto T_) __attribute__((always_inline)) {
        constexpr int t = decltype(T_)::v;
        f4 fq = aq[t] * R.c[t] - csq * R.cm[t], fk = ak[t] * R.c[t] - csk * R.cm[t], fv = av[t] * R.c4[t] - R.cm4[t] * csv;
        OPQ(fq); OPQ(fk); OPQ(fv);
        SB();
        if constexpr (t > 0) ss[t - 1] = m16c(kb[t - 1], qb[t - 1], mask);
        qb[t] = pk4(fq); kb[t][0] = pk(fk[0], fk[1]);
        SB();
        if constexpr (t > 1) sp[t - 1] = m16c(kb[t - 2], qb[t - 1], mask);
        kb[t][1] = pk(fk[2], fk[3]);
        OPQ(kb[t]);
        SB();
        vb[t] = pk4(fv);
        OPQ(vb[t]);
        SB();
      });
      auto pick = [&](int t) {
        sc[t] = bsel4(pm, t ? sp[t] : neg, ss[t]);
        mx[t] = fmx(fmx(fmx(sc[t][0], sc[t][1]), sc[t][2]), sc[t][3]);
      };
      ss[4] = m16c(kb[4], qb[4], mask); SB();
      pick(0); SB();
      sp[4] = m16c(kb[3], qb[4], mask); SB();
      pick(1); pick(2); SB();
      pick(3); pick(4);
      {
        f2 x = swap16(mx[0], mx[1]), y = swap16(mx[2], mx[3]), w = swap16(mx[4], mx[4]);
        f2 u = swap32(fmx(x[0], x[1]), fmx(y[0], y[1]));
        const float w1 = fmx(w[0], w[1]), c = fmx(u[0], u[1]) * kap;
        f2 w2 = swap32(w1, w1), v = swap32(c, c);
        f2 v0 = swap16(v[0], v[0]), v1 = swap16(v[1], v[1]);
        mx[0] = v0[0]; mx[1] = v0[1]; mx[2] = v1[0]; mx[3] = v1[1]; mx[4] = fmx(w2[0], w2[1]) * kap;
      }
      if (L == 0) {
        WAITV(18);
        st8(ws, (u2){now() + T_O0, tag}, lw0, gate);
        u4 sent = {SENT, SENT, SENT, SENT};
        OPQ(sent);
#pragma unroll
        for (int i = 0; i < 3; ++i) st16(ws, sent, i < 2 ? ooff : ooff4, WS_XB + tile1 * TILEB + hg1 * 2048 + i * 32768);
      }
      SB();
      auto norm = [&](int t) { KEEP(sum[t]); o[t] = pk4(oa[t] * __builtin_amdgcn_rcpf(sum[t][0])); };
      sfor<0, 5>([&](auto T_) __attribute__((always_inline)) {
        constexpr int t = decltype(T_)::v;
        f4 d = sc[t] * kap - mx[t];
        OPQ(d);
        SB();
        if constexpr (t > 0) m16<true>(oa[t - 1], vb[t - 1], ps[t - 1]);
        float e0 = ex(d[0]), e1 = ex(d[1]);
        OPQ(e0); OPQ(e1);
        SB();
        if constexpr (t > 1) m16(oa[t - 1], vb[t - 2], pp[t - 1]);
        float e2 = ex(d[2]), e3 = ex(d[3]);
        OPQ(e2); OPQ(e3);
        SB();
        if constexpr (t > 0) m16<true>(sum[t - 1], one2, pb[t - 1]);
        pb[t] = (u2){pk(e0, e1), pk(e2, e3)};
        if constexpr (t > 0) { pp[t] = pb[t] & pm; ps[t] = pb[t] ^ pp[t]; } else ps[t] = pb[t];
        SB();
        if constexpr (t > 1) { norm(t - 2); SB(); }
        if constexpr (t == 3) peek8_go(fa, ws, L ? OOB : fao, WS_FA);
      });
      m16<true>(oa[4], vb[4], ps[4]); m16(oa[4], vb[3], pp[4]); m16<true>(sum[4], one2, pb[4]);
      SB();
      NOPS(2);
      norm(3);
      SB();
      NOPS(2);
      norm(4);
    }
    if (L == 1) {
      STAMP(10);
      if (wv1 == 0) {
        WAITV(0);
        OPQ(zs); OPQ(ff);
        if (__builtin_expect(!__all(ff == tag), 0)) wait_flags_ld(ws, lr1 * 128, WS_FF, tag);
        if (!__all(zs[1] == tag || l1 >= 3)) zs[0] = wait_word(ws, l1 < 3 ? l1 * 8 : OOB, WS_SEL + (b & 15) * 128, tag);
        lds_w<unsigned>(L_MISC + O_SEL + l1 * 4, zs[0]);
      }
      WAITL();
      BAR();
      const u4 sel = lds_r<u4>(L_MISC + O_SEL);
      const int halo1 = (tile1 != 0 && tile1 != 15);
      auto local = [&](unsigned key) { const int tl = (key >> 7) & 31, rho = key & 127; return tl == tile1 ? rho : (halo1 && tl == tile1 - 1 && rho >= 72) ? rho - 72 : -1; };
      const int row0 = local(rfl(sel[0])), row1 = local(rfl(sel[1]));
      STAMP(9);
      if (row0 < 0 && row1 < 0) st8(ws, (u2){now() + 5, tag}, lw0, gate);
#pragma unroll 1
      for (int i = 0; i < 2; ++i) {
        const int rho = i ? row1 : row0;
        if (rho < 0) continue;
        const unsigned key = rfl(i ? sel[1] : sel[0]);
        FRESH(5)
        const int ts = rho >> 4, rs_ = rho & 15;
        const unsigned ra = L_MISC + O_RA + rho * 4;
        float sg = sigm(key_score(key));
        const float a1 = lds_r<float>(ra), b1 = lds_r<float>(ra + 320);
        const int col = 128 * wv5 + 4 * (l5 & 31);
        unsigned rb = L_RING + i * 1024, zb = rb + g5 * 16, zr = rb + l16, zw = rb + col * 2, zxa = rb + (64 * hg5 + 16 * wv5 + 4 * g5) * 2, yo = (l5 >> 5) * 32768 + col * 4;
        OPQ(zb); OPQ(zr); OPQ(zw); OPQ(zxa); OPQ(yo); OPQ(sg);
        const u2 xr = __builtin_amdgcn_raw_buffer_load_b64(ws, (col >> 5) * 1024 + ((col >> 3) & 3) * 256 + (col & 4) * 2, WS_XB + ((key >> 7) & 31) * TILEB + ((key >> 4) & 7) * 16384 + (key & 15) * 16, 16);
        f4 x0 = up4(xr) * a1 + b1;
        const unsigned me = lmask(lr5 == rs_), half = (rs_ & 1) ? 0xFFFF0000u : 0xFFFFu;
        const unsigned mv0 = lmask(2 * g5 == (rs_ >> 1)) & half, mv1 = lmask(2 * g5 + 1 == (rs_ >> 1)) & half;
        u2 Ka, Kb, Kc, Qb, Qc, Va, Vb, Vc, Xb;
        f4 mk0 = mask;
        sfor<0, 5>([&](auto T_) __attribute__((always_inline)) {
          constexpr int t = decltype(T_)::v, tp = t > 0 ? t - 1 : 0, tn = t < 4 ? t + 1 : 4;
          if (ts == t) { CFENCE(); Ka = kb[tp]; Kb = kb[t]; Kc = kb[tn]; Qb = qb[t]; Qc = qb[tn]; Va = vb[tp]; Vb = vb[t]; Vc = vb[tn]; Xb = xp[t]; if (t == 0) mk0 = neg; }
        });
        OPQ(x0); OPQ(Ka); OPQ(Kb); OPQ(Kc); OPQ(Qb); OPQ(Qc); OPQ(Va); OPQ(Vb); OPQ(Vc); OPQ(Xb); OPQ(mk0);
#pragma unroll 1
        for (int sp = 0; sp < 256 && (int)(rfl(sel[2]) + LAG_Y - now()) > 0; ++sp) {}
        u4 y[8];
#pragma unroll 1
        for (int sp = 0; sp < SPIN_CAP; ++sp) {
          CFENCE();
          unsigned m = 0;
          CNT(10);
#pragma unroll
          for (int j = 0; j < 8; ++j) { y[j] = ld16(ws, yo, WS_YP + j * 4096 + i * 2048); m = max(m, y[j][0]); }
          if (!__any(m == SENT)) break;
          __builtin_amdgcn_s_sleep(1);
        }
        STAMP(27);
        if (i == 1 || row1 < 0) st8(ws, (u2){now() + T_O1, tag}, lw0, gate);
        {
          const f4 t = ((asf(y[0]) + asf(y[1])) + (asf(y[2]) + asf(y[3]))) + ((asf(y[4]) + asf(y[5])) + (asf(y[6]) + asf(y[7])));
          const f2 p = swap32(t[0], t[1]), q = swap32(t[2], t[3]);
          const float e = p[0] + p[1], f = q[0] + q[1];
          const f2 ee = swap32(e, e), ff_ = swap32(f, f);
          lds_w<u2>(zw, pk4((f4){ee[0], ee[1], ff_[0], ff_[1]} * sg + x0));
        }
        WAITL();
        BAR();
        const u4 zz = lds_r<u4>(zr);
        const u2 z0 = {zz[0], zz[1]}, z1 = {zz[2], zz[3]};
        f4 tq, tk, tv;
        u4 z[16];
#pragma unroll
        for (int s = 0; s < 4; ++s) z[s] = lds_r<u4>(zb + s * 64);
        const u2 zx = lds_r<u2>(zxa);
        float sm, sq;
        SB();
        sfor<0, 16>([&](auto S_) __attribute__((always_inline)) {
          constexpr int s = decltype(S_)::v;
          mWA<s == 0>(tq, W[s][0], z[s]);
          if constexpr (s + 4 < 16) z[s + 4] = lds_r<u4>(zb + (s + 4) * 64);
          SB();
          mWA<s == 0>(tk, W[s][1], z[s]);
          if (s == 0) { sq = dot2(z0[1], z0[1], dot2(z0[0], z0[0], 0.f)); OPQ(sq); }
          if (s == 1) { sm = dot2(z0[1], one2[0], dot2(z0[0], one2[0], 0.f)); OPQ(sm); }
          if (s == 2) sq = dot2(z1[1], z1[1], dot2(z1[0], z1[0], sq));
          if (s == 3) sm = dot2(z1[1], one2[0], dot2(z1[0], one2[0], sm));
          if (s == 4) { sm += dppf<0xB1>(sm); sq += dppf<0xB1>(sq); } if (s == 5) { sm += dppf<0x4E>(sm); sq += dppf<0x4E>(sq); }
          if (s == 6) { sm += dppf<0x141>(sm); sq += dppf<0x141>(sq); } if (s == 7) { sm += dppf<0x140>(sm); sq += dppf<0x140>(sq); }
          if (s == 8) sm = xadd16(sm); if (s == 9) sq = xadd16(sq); if (s == 10) sm = xadd32(sm) * (1.f / 512); if (s == 11) sq = xadd32(sq) * (1.f / 512);
          if (s == 12) sq = fmx(sq - sm * sm, 0.f) + EPS; if (s == 13) sq = rsq(sq); if (s == 14) sm *= sq;
          if (s == 15) { lds_w<float>(ra, 1.f); lds_w<float>(ra + 320, 0.f); }
          if (s >= 2 && s < 15) { OPQ(sm); OPQ(sq); }
          SB();
          mAW<s == 0>(tv, z[s], W[s][2]);
          SB();
        });
        Xb = (u2){bsel(me, zx[0], Xb[0]), bsel(me, zx[1], Xb[1])};
        f4 cq = csq * -sm, ck = csk * -sm;
        float cv = csv * -sm;
        OPQ(Xb); OPQ(cq); OPQ(ck); OPQ(cv);
        SB();
        if (i == 1 || row1 < 0) peek8_go(fa, ws, fao, WS_FA + 32768);
        const u2 nq = pk4(tq * sq + cq), nk = pk4(tk * sq + ck);
        KEEP(tv);
        const float nv = tv[0] * sq + cv;
        const unsigned nvp = pk(nv, nv);
        Qb = (u2){bsel(me, nq[0], Qb[0]), bsel(me, nq[1], Qb[1])}; Kb = (u2){bsel(me, nk[0], Kb[0]), bsel(me, nk[1], Kb[1])};
        OPQ(Qb); OPQ(Kb);
        SB();
        Vb = (u2){bsel(mv0, nvp, Vb[0]), bsel(mv1, nvp, Vb[1])};
        OPQ(Vb);
        SB();
        f4 sb = m16c(Kb, Qb, mask), pb_ = m16c(Ka, Qb, mk0), sc_ = m16c(Kc, Qc, mask), pc_ = m16c(Kb, Qc, mask);
        SB();
        NOPS(6);
        SB();
        sb = bsel4(pm, pb_, sb);
        float mb = fmx(fmx(fmx(sb[0], sb[1]), sb[2]), sb[3]);
        OPQ(mb);
        SB();
        sc_ = bsel4(pm, pc_, sc_);
        float mc = fmx(fmx(fmx(sc_[0], sc_[1]), sc_[2]), sc_[3]);
        { f2 x = swap16(mb, mc); const float y_ = fmx(x[0], x[1]); f2 w = swap32(y_, y_); const float c = fmx(w[0], w[1]) * kap; f2 v = swap16(c, c); mb = v[0]; mc = v[1]; }
        const f4 db = sb * kap - mb, dc = sc_ * kap - mc;
        u2 Pb = {pk(ex(db[0]), ex(db[1])), pk(ex(db[2]), ex(db[3]))}, Pc = {pk(ex(dc[0]), ex(dc[1])), pk(ex(dc[2]), ex(dc[3]))};
        u2 Pbp = Pb & pm, Pbs = Pb ^ Pbp, Pcp = Pc & pm, Pcs = Pc ^ Pcp;
        OPQ(Pbp); OPQ(Pbs); OPQ(Pcp); OPQ(Pcs);
        f4 ob_, oc_, lb, lc;
        SB();
        m16<true>(lb, one2, Pb); m16<true>(lc, one2, Pc); m16<true>(ob_, Vb, Pbs); m16(ob_, Va, Pbp); m16<true>(oc_, Vc, Pcs); m16(oc_, Vb, Pcp);
        SB();
        NOPS(4);
        SB();
        KEEP(lb); KEEP(lc);
        float ib = __builtin_amdgcn_rcpf(lb[0]), ic = __builtin_amdgcn_rcpf(lc[0]);
        OPQ(ib); OPQ(ic);
        SB();
        NOPS(1);
        u2 Ob = pk4(ob_ * ib);
        OPQ(Ob);
        SB();
        const u2 Oc = pk4(oc_ * ic);
        sfor<0, 5>([&](auto T_) __attribute__((always_inline)) {
          constexpr int t = decltype(T_)::v;
          if (ts == t) { CFENCE(); kb[t] = Kb; qb[t] = Qb; vb[t] = Vb; xp[t] = Xb; o[t] = Ob; if constexpr (t < 4) o[t + 1] = Oc; }
        });
      }
      STAMP(25);
    }
#pragma unroll
    for (int i = 0; i < 3; ++i) {
      u2 A = o[2 * i], B = o[i < 2 ? 2 * i + 1 : 4];
      auto s0 = __builtin_amdgcn_permlane16_swap(A[0], B[0], false, false); auto s1 = __builtin_amdgcn_permlane16_swap(A[1], B[1], false, false);
      const u4 e = {umin(s0[0], SENT - 1), s1[0], s0[1], s1[1]};
      st16(ws, e, i < 2 ? ooff : ooff4, ob + hg1 * 2048 + i * 32768);
      lds_w<u4>(i < 2 ? ooff + i * 32768 : lo2, e);
    }
    STAMP(3 + 8 * L);
    FRESH(2)
    unsigned l16h = l16 + 65536, r1 = L_MISC + O_RA + lr2 * 4;
    OPQ(l16h); OPQ(r1);
    f4 acc[5];
    {
      float ra[5], rb[5];
#pragma unroll
      for (int t = 0; t < 5; ++t) { ra[t] = lds_r<float>(r1 + t * 64); rb[t] = lds_r<float>(r1 + 320 + t * 64); }
      SB();
#pragma unroll
      for (int t = 0; t < 5; ++t) acc[t] = up4(xp[t]);
      SB();
#pragma unroll
      for (int t = 0; t < 5; ++t) { acc[t] = acc[t] * ra[t] + rb[t]; OPQ(acc[t]); }
    }
    unsigned rw_ = L_RED2 + wv2 * 2048 + l16, rr_ = L_RED2 + row3 * 16;
    unsigned lw2 = wv2 * 1024, lw4 = (wv2 & 1) * 1024, hub = WS_HU + L * 32768 + (tile2 * 8 + hg2) * 128, stb = WS_ST + L * STB + (tile2 * 8 + hg2) * 1280;
    OPQ(rw_); OPQ(rr_);
    asm volatile("" : "+s"(lw2), "+s"(lw4), "+s"(hub), "+s"(stb));
    WAITC(3, 0);
    OPQ(fa);
    asm volatile("" ::"v"(zs), "v"(ff));
    if (wv2 == 0) wait_gate(ws, fa, fao, WS_FA + L * 32768, tag, LAG_O);
    BAR();
    STAMP(23);
    u4 a[3][5];
    auto rda = [&](int j, int t) { a[j % 3][t] = lds_r<u4>((t < 4 ? l16 + t * 16384 : l16h) + j * 1024); };
    auto rdwo = [&](int j) { WO[j] = trfrag2(trb + ((4 + j / 3) % 6) * 12288 + (j % 3) * 4096, 512); asm volatile("" : "+a"(WO[j])); };
    if (L == 0) rdwo(0);
#pragma unroll
    for (int t = 0; t < 5; ++t) rda(0, t);
#pragma unroll
    for (int t = 0; t < 5; ++t) rda(1, t);
    STAMP(24);
    SB();
#pragma unroll
    for (int t = 0; t < 5; ++t) { setm0(lw2, 2048 + t * 16384); SB(); mWA(acc[t], WO[0], a[0][t]); SB(); dma_go<1>(ws, l16, so_[0] + t * 16384); }
#pragma unroll
    for (int t = 0; t < 5; ++t) dma<1>(ws, l16, so_[1] + t * 16384, lw2, 6 * 1024 + t * 16384);
    if (L == 0) {
#pragma unroll
      for (int j = 1; j < 16; ++j) rdwo(j);
    }
    STAMP(4 + 8 * L);
    auto refetch = [&](u4& c0, u4& c1, u4& c2, u4& c3, u4& c4, unsigned v0, unsigned v1, unsigned so, int ci) {
      CNT(ci);
#pragma unroll 1
      for (int sp = 0; sp < SPIN_CAP; ++sp) {
        CNT(ci + 1);
        CFENCE();
        c0 = ld16(ws, v0, so); c1 = ld16(ws, v1, so + 16384); c2 = ld16(ws, v1, so + 32768); c3 = ld16(ws, v1, so + 49152); c4 = ld16(ws, v1, so + 65536);
        if (!__any(max(max(max(c0[0], c1[0]), c2[0]), max(c3[0], c4[0])) == SENT)) break;
        __builtin_amdgcn_s_sleep(6);
      }
      NOPS(2);
    };
    WAITV(5);
    BAR();
#pragma unroll
    for (int t = 0; t < 5; ++t) rda(2, t);
    SB();
    sfor<1, 16>([&](auto J_) __attribute__((always_inline)) {
      constexpr int j = decltype(J_)::v, n = (j + 1) % 3;
      u64 th;
      if (j == 8) th = time_go();
      if (j == 4) WAITV(5); else if (j == 8) WAITV(3); else if (j == 12) WAITV(1);
      if (j == 4 || j == 8 || j == 12) BAR();
      if (j == 8) st8(ws, (u2){time_get(th) + T_ST, tag}, hg0, hub);
      SB();
      unsigned m;
      sfor<0, 5>([&](auto T_) __attribute__((always_inline)) {
        constexpr int t = decltype(T_)::v, k = j < 4 ? 2 * (j - 1) + (t >> 1) : 5 + (j - 4);
        constexpr bool dm = j < 4 ? (t < 4 && k < 5) : (j < 7 && t < 2);
        mWA(acc[t], WO[j], a[j % 3][t]);
        if constexpr (j + 2 < 16) rda(j + 2, t);
        if constexpr (dm && (t & 1) == 0) { if constexpr (k < 5) setm0(lw2, 10 * 1024 + k * 16384); else setm0(lw4 + t4[k - 5], 14 * 1024); }
        if constexpr (dm && (t & 1) == 1) { if constexpr (k < 5) dma_go<1>(ws, l16, so_[2] + k * 16384); else dma_go<1>(ws, l16, so_[3] + t4[k - 5]); }
        if constexpr (j + 1 < 16) {
          if constexpr (t == 1) WAITC(63, j + 2 < 16 ? 2 : 0);
          if constexpr (t == 2) { m = max(max(a[n][0][0], a[n][1][0]), a[n][2][0]); OPQ(m); }
          if constexpr (t == 3) { m = max(max(m, a[n][3][0]), a[n][4][0]); OPQ(m); }
        }
        SB();
      });
      if constexpr (j + 1 < 16)
        if (__builtin_expect(__any(m == SENT), 0)) refetch(a[n][0], a[n][1], a[n][2], a[n][3], a[n][4], l16, l16, ob + ((2 * hg2 + j + 1) & 15) * 1024, 2 * L);
      SB();
    });
    STAMP(5 + 8 * L);
    {
      float q2[5];
      f4 da, ea;
      NOPS(4);
      SB();
      sfor<0, 5>([&](auto T_) __attribute__((always_inline)) {
        constexpr int t = decltype(T_)::v;
        xp[t] = pk4(acc[t]);
        if constexpr (t > 0) { q2[t - 1] = dot2(xp[t - 1][1], xp[t - 1][1], dot2(xp[t - 1][0], xp[t - 1][0], 0.f)); OPQ(q2[t - 1]); }
        OPQ(xp[t]);
        SB();
        if constexpr (t > 0) m16<t == 1>(da, spa[t - 1], xp[t - 1]);
        SB();
      });
      q2[4] = dot2(xp[4][1], xp[4][1], dot2(xp[4][0], xp[4][0], 0.f));
      OPQ(q2[4]);
      SB();
      m16<true>(ea, spa[0], xp[4]);
      SB();
      const f2 x = swap16(q2[0], q2[1]), y = swap16(q2[2], q2[3]), u = swap32(x[0] + x[1], y[0] + y[1]);
      float qa = u[0] + u[1], qe = xadd32(xadd16(q2[4]));
      OPQ(qa); OPQ(qe);
      SB();
      KEEP(da); KEEP(ea);
      lds_w<f4>(rw_, (f4){da[0], da[1], qa, da[3]});
      lds_w<f4>(rw_ + 1024, (f4){ea[0], ea[1], qe, ea[3]});
    }
    WAITL();
    BAR();
    {
      u4 w = asu((lds_r<f4>(rr_) + lds_r<f4>(rr_ + 2048)) + (lds_r<f4>(rr_ + 4096) + lds_r<f4>(rr_ + 6144)));
      w[3] = tag;
      st16(ws, w, sv_, stb);
    }
    STAMP(6 + 8 * L);
    FRESH(3)
    const unsigned xb = WS_XB + L * BUFB + tile3 * TILEB;
    const int par3 = tile3 >= 15, ti3 = tile3 - 15 * par3, p03 = 68 * ti3;
    const bool halo = L == 0 && ti3 > 0;
    const unsigned fo = wv3 == 0 && l3 < (halo ? 16 : 8) ? ((tile3 - (l3 >> 3)) * 8 + (l3 & 7)) * 128 : OOB;
    u2 hu = {0, 0};
    peek8_go(hu, ws, fo, WS_XG + L * 32768);
#pragma unroll
    for (int i = 0; i < 3; ++i) {
      u2 A = xp[2 * i], B = xp[i < 2 ? 2 * i + 1 : 4];
      auto s0 = __builtin_amdgcn_permlane16_swap(A[0], B[0], false, false); auto s1 = __builtin_amdgcn_permlane16_swap(A[1], B[1], false, false);
      st16(ws, (u4){umin(s0[0], SENT - 1), s1[0], s0[1], s1[1]}, i < 2 ? ooff : ooff4, xb + hg3 * 2048 + i * 32768);
    }
    unsigned so = (halo && row3 < 7) ? ((tile3 - 1) * 640 + 72 + row3) * 16 : (tile3 * 640 + row3) * 16;
    unsigned zg1 = l16 + 16384, zg0 = zg1 - ((halo && lr3 < 7) ? 16256 : 0);
    unsigned rc_ = L_MISC + O_RC + row3 * 4, q1 = L_MISC + O_RC + lr3 * 4, q4 = L_MISC + O_RC + g3 * 16;
    OPQ(so); OPQ(zg0); OPQ(zg1); OPQ(rc_); OPQ(q1); OPQ(q4);
    u4 v[8];
    auto stats_ok = [&]() { return __all(((v[0][3] & v[1][3] & v[2][3] & v[3][3] & v[4][3] & v[5][3] & v[6][3] & v[7][3]) == tag) && ((v[0][3] | v[1][3] | v[2][3] | v[3][3] | v[4][3] | v[5][3] | v[6][3] | v[7][3]) == tag)); };
    auto stats_wait = [&]() {
#pragma unroll 1
      for (int sp = 0; sp < SPIN_CAP; ++sp) {
        CFENCE();
#pragma unroll
        for (int i = 0; i < 8; ++i) v[i] = ld16(ws, so, WS_ST + L * STB + i * 1280);
        if (stats_ok()) break;
        __builtin_amdgcn_s_sleep(1);
      }
    };
    auto rowstats = [&]() {
      const f4 st = ((asf(v[0]) + asf(v[1])) + (asf(v[2]) + asf(v[3]))) + ((asf(v[4]) + asf(v[5])) + (asf(v[6]) + asf(v[7])));
      const float m = st[0] * (1.f / 512), var = fmx(st[2] * (1.f / 512) - m * m, 0.f), rs = rsq(var + EPS), a1 = 1.f + rs;
      const float c = a1 * rsq(var * a1 * a1 + EPS);
      lds_w<float>(rc_, c); lds_w<float>(rc_ + 320, c * m); lds_w<float>(rc_ + 640, a1); lds_w<float>(rc_ + 960, -m * rs);
    };
    WAITV(3);
    OPQ(hu);
    if (wv3 == 0) wait_gate(ws, hu, fo, WS_XG + L * 32768, tag, LAG_X);
    BAR();
    if (L == 1) {
      const rsrc_t ro = mk(P.out, 2048 * 1024);
      stats_wait();
      STAMP(15);
      rowstats();
      FRESH(4)
      WAITL();
      BAR();
#pragma unroll
      for (int t = 0; t < 5; ++t) {
        const float ra = lds_r<float>(q1 + 640 + t * 64), rb = lds_r<float>(q1 + 960 + t * 64);
        lds_w<u2>((16 * t + lr4) * 144 + wv4 * 32 + g4 * 8, pk4(up4(xp[t]) * ra + rb));
      }
      WAITL();
      BAR();
#pragma unroll
      for (int i = 0; i < 3; ++i) {
        const int row = min((i * 4 + wv4) * 8 + (l4 >> 3), 79), u = row - (row >> 4), p = p03 + u;
        const bool ok = (i * 4 + wv4) < 10 && (row & 15) != 15 && (ti3 == 0 || u >= 7);
        st16(ro, lds_r<u4>(row * 144 + (l4 & 7) * 16), ok ? (2 * p + par3) * 1024 + hg4 * 128 + (l4 & 7) * 16 : OOB, 0);
      }
      STAMP(16);
      WAITV(0);
      BAR();
      if (wv4 < 2) {
        const unsigned zz = wait_word(ws, l4 < 2 ? (l4 ? 16 : wv4 * 8) : OOB, WS_SEL + 2048 + (b & 15) * 128, tag);
        const unsigned key = rdl(zz, 0), ty = rdl(zz, 1);
        STAMP(17);
        if (((key >> 7) & 31) == tile4) {
          const int rho = key & 127;
          f4 base = up4(lds_r<u2>(rho * 144 + lr4 * 8));
          float sg = sigm(key_score(key));
          unsigned oo = l4 < 16 ? (2 * (p03 + rho - (rho >> 4)) + par3) * 1024 + hg4 * 128 + lr4 * 8 : OOB;
          OPQ(base); OPQ(sg); OPQ(oo);
#pragma unroll 1
          for (int sp = 0; sp < 256 && (int)(ty + LAG_Y - now()) > 0; ++sp) {}
          u4 y[4];
#pragma unroll 1
          for (int sp = 0; sp < SPIN_CAP; ++sp) {
            CFENCE();
            unsigned m = 0;
            CNT(11);
#pragma unroll
            for (int j = 0; j < 4; ++j) { y[j] = ld16(ws, g4 * 4096 + lr4 * 16, WS_YP + 65536 + j * 16384 + wv4 * 2048 + hg4 * 256); m = max(m, y[j][0]); }
            if (!__any(m == SENT)) break;
            __builtin_amdgcn_s_sleep(1);
          }
          const f4 s = (asf(y[0]) + asf(y[1])) + (asf(y[2]) + asf(y[3]));
          const f2 x = swap16(s[0], s[1]), w = swap16(s[2], s[3]), vv = swap32(x[0] + x[1], w[0] + w[1]);
          const float e = vv[0] + vv[1];
          const f2 v2 = swap32(e, e), e01 = swap16(v2[0], v2[0]), e23 = swap16(v2[1], v2[1]);
          st8(ro, pk4((f4){e01[0], e01[1], e23[0], e23[1]} * sg + base), oo, 0);
        }
        STAMP(18);
      }
#pragma unroll
      for (int t = 0; t < 5; ++t) asm volatile("" : "=v"(aq[t]), "=v"(ak[t]), "=v"(av[t]), "=v"(R.c[t]), "=v"(R.cm[t]), "=v"(R.c4[t]), "=v"(R.cm4[t]));
      break;
    }
    {
      const unsigned xs = xb - 16384 + wv3 * 1024, lw3 = wv3 * 1024;
      u4 sent = {SENT, SENT, SENT, SENT};
      OPQ(sent);
      STAMP(7);
#pragma unroll
      for (int i = 0; i < 2; ++i)
#pragma unroll
        for (int t = 0; t < 5; ++t) dma<1>(ws, t ? zg1 : zg0, xs + t * 16384 + i * 4096, lw3, t * 16384 + i * 4096);
#pragma unroll
      for (int k = 0; k < 8; ++k) ld16_go(v[k], ws, so, WS_ST + k * 1280);
      WAITV(13);
      BAR();
      STAMP(26);
      u4 z[2][5];
      unsigned tg;
      float s1, s2, s3, s4;
#pragma unroll
      for (int t = 0; t < 5; ++t) z[0][t] = lds_r<u4>(t < 4 ? l16 + t * 16384 : l16h);
      if (__builtin_expect(__any(max(max(max(z[0][0][0], z[0][1][0]), z[0][2][0]), max(z[0][3][0], z[0][4][0])) == SENT), 0)) refetch(z[0][0], z[0][1], z[0][2], z[0][3], z[0][4], zg0, zg1, xb - 16384, 4);
      SB();
      sfor<0, 16>([&](auto S_) __attribute__((always_inline)) {
        constexpr int s = decltype(S_)::v, c = s & 1, n = c ^ 1;
        if (s == 3) { WAITV(14); BAR(); } else if (s == 7) { WAITV(5); BAR(); } else if (s == 11) { WAITC(0, 0); BAR(); }
        if (s == 8) {
          asm volatile("" : "+v"(v[0]), "+v"(v[1]), "+v"(v[2]), "+v"(v[3]), "+v"(v[4]), "+v"(v[5]), "+v"(v[6]), "+v"(v[7]));
          if (__builtin_expect(__any(tg != 0), 0)) { CNT(9); stats_wait(); }
        }
        SB();
        unsigned m;
        bool bad;
        sfor<0, 15>([&](auto I_) __attribute__((always_inline)) {
          constexpr int i = decltype(I_)::v, t = i / 3, k = 2 * s + (i > 6);
          if constexpr (i % 3 == 0) mWA<s == 0>(aq[t], W[s][0], z[c][t]); else if constexpr (i % 3 == 1) mWA<s == 0>(ak[t], W[s][1], z[c][t]); else mAW<s == 0>(av[t], z[c][t], W[s][2]);
          if constexpr (s == 15) rowc_read(R, q1, q4, i);
          else if constexpr (i < 5) z[n][i] = lds_r<u4>((i < 4 ? l16 + i * 16384 : l16h) + (s + 1) * 1024);
          if constexpr (s < 5 && (i == 5 || i == 9)) setm0(lw3, (k % 5) * 16384 + (2 + k / 5) * 4096);
          if constexpr (s < 5 && (i == 6 || i == 10)) dma_go<1>(ws, (k % 5) ? zg1 : zg0, xs + (k % 5) * 16384 + (2 + k / 5) * 4096);
          if constexpr (s < 15 && i == 11) { WAITC(63, 0); m = max(max(z[n][0][0], z[n][1][0]), z[n][2][0]); OPQ(m); }
          if constexpr (s < 15 && i == 12) { m = max(max(m, z[n][3][0]), z[n][4][0]); OPQ(m); }
          if constexpr (s < 15 && i == 13) bad = __any(m == SENT);
          if constexpr (s == 7 && i >= 5 && i < 9) { constexpr int e = 2 * (i - 5); OPQ(v[e]); OPQ(v[e + 1]); const unsigned w = (v[e][3] ^ tag) | (v[e + 1][3] ^ tag); tg = e ? tg | w : w; OPQ(tg); }
          if constexpr (s == 8 && i == 4) { s1 = asf(v[0])[0]; s2 = asf(v[0])[2]; }
          if constexpr (s == 8 && i >= 5 && i < 14 && i != 11 && i != 12) { constexpr int e = i < 11 ? i - 4 : 7; s1 += asf(v[e])[0]; OPQ(s1); s2 += asf(v[e])[2]; OPQ(s2); }
          if constexpr (s == 9 && i == 5) { s1 *= 1.f / 512; OPQ(s1); s3 = s1 * s1; OPQ(s3); }
          if constexpr (s == 9 && i == 6) { s2 = fmx(s2 * (1.f / 512) - s3, 0.f); OPQ(s2); }
          if constexpr (s == 9 && i == 7) { s3 = rsq(s2 + EPS); OPQ(s3); }
          if constexpr (s == 9 && i == 8) { s4 = 1.f + s3; OPQ(s4); s2 *= s4; OPQ(s2); }
          if constexpr (s == 9 && i == 9) { s2 = rsq(s2 * s4 + EPS); OPQ(s2); }
          if constexpr (s == 9 && i == 10) { s2 *= s4; OPQ(s2); s3 *= -s1; OPQ(s3); }
          if constexpr (s == 10 && i == 5) { lds_w<float>(rc_, s2); lds_w<float>(rc_ + 320, s2 * s1); }
          if constexpr (s == 10 && i == 6) { lds_w<float>(rc_ + 640, s4); lds_w<float>(rc_ + 960, s3); }
          if constexpr ((s == CL_STEP || s == CL_STEP + 1) && (i == 5 || i == 7 || i == 9))
            st16c(ws, sent, i < 9 ? ooff : ooff4, (s == CL_STEP ? WS_OB : WS_XB) + BUFB + tile3 * TILEB + hg3 * 2048 + (i - 5) / 2 * 32768);
          SB();
        });
        if constexpr (s < 15)
          if (__builtin_expect(bad, 0)) refetch(z[n][0], z[n][1], z[n][2], z[n][3], z[n][4], zg0, zg1, xb - 16384 + (s + 1) * 1024, 4);
        SB();
      });
      STAMP(8);
#pragma unroll
      for (int i = 0; i < 4; ++i) so_[i] += BUFB;
      WAITV(0);
      BAR();
    }
  }
  STAMP_FLUSH();
}

DEV void role_f(const int j, unsigned g_tent) {
  const Params P = args_wait(args_go());
  const int wv = __builtin_amdgcn_readfirstlane(threadIdx.x >> 6), l = threadIdx.x & 63;
  const int lr = l & 15, g = l >> 4, tid = wv * 64 + l;
  const rsrc_t ws = mk(P.ws, WS_USED);
  const unsigned epoch = tag_issue(P.ws);
  STAMP_INIT();
  STAMP(0);
  const int noct = j < 11 ? 12 : 8, f0 = 32 * (j < 11 ? 3 * j : 33 + 2 * (j - 11));
  u4 G[16][3], Dn[3][8];
  const unsigned mg1 = lmask(g == 1), mg2 = lmask(g >= 2);
  auto gather = [&](const f4& a0, const f4& a1, const f4& a2, float& gv, float& uv) {
    KEEP(a0); KEEP(a1); KEEP(a2);
    const float d0 = bself(mg2, a2[0], bself(mg1, a1[0], a0[0])), d1 = bself(mg2, a2[1], bself(mg1, a1[1], a0[1]));
    gv = __uint_as_float(__builtin_amdgcn_update_dpp(__float_as_uint(d0), __float_as_uint(d1), 0x128, 0xF, 0xC, false));
    uv = __uint_as_float(__builtin_amdgcn_update_dpp(__float_as_uint(d1), __float_as_uint(d0), 0x128, 0xF, 0x3, false));
  };
  float csg, csu, wsum;
  unsigned tag;
  {
    const rsrc_t rgu = mk(wv < 2 ? P.wg : P.wu, 512 * 2752), rd = mk(P.wd, 1376 * 1024);
    unsigned go[6];
#pragma unroll
    for (int i = 0; i < 6; ++i) { const int n = 64 * ((wv & 1) * 6 + i) + l, row = n / 12; go[i] = row * 2752 + f0 * 2 + (n - 12 * row) * 16; }
    const unsigned dof = (f0 + g) * 1024 + wv * 256 + lr * 16;
    auto issue = [&](int rd_) {
      if (rd_ < 8) {
#pragma unroll
        for (int i = 0; i < 6; ++i) dma<0>(rgu, go[i], rd_ * 64 * 2752, wv * 6144, (rd_ & 3) * 32768 + i * 1024);
      } else {
#pragma unroll
        for (int i = 0; i < 8; ++i) dma<0>(rd, dof, (32 * (rd_ - 8) + 4 * i) * 1024, wv * 8192, (rd_ & 3) * 32768 + i * 1024);
      }
    };
    unsigned tg = ((l >> 1) & 1) * 12288 + (8 * g + (lr >> 2)) * 192 + wv * 48 + (l & 1) * 8, td = wv * 8192 + (8 * g + (lr >> 2)) * 256 + (l & 3) * 8;
    u4 w8;
    const u4 sent = {SENT, SENT, SENT, SENT};
    asm volatile("global_load_dwordx4 %0, %1, %2" : "=v"(w8) : "v"(l * 16), "s"(P.wr) : "memory");
    st16(ws, sent, j * 4096 + tid * 16, WS_YP); st16(ws, sent, j * 4096 + tid * 16, WS_YP + 65536);
    STAMP(17);
    issue(0); issue(1); issue(2);
    tag = tag_finish(epoch);
    u4 one = {ONE2, ONE2, ONE2, ONE2};
    OPQ(one);
    f4 a[3];
#pragma unroll
    for (int rd_ = 0; rd_ < 11; ++rd_) {
      if (rd_ == 1 || rd_ == 2) WAITC(13, 0); else if (rd_ < 6) WAITC(12, 0); else if (rd_ == 6) WAITC(14, 0); else if (rd_ < 9) WAITC(16, 0); else if (rd_ == 9) WAITC(8, 0); else WAITC(0, 0);
      BAR();
      if (rd_ == 0) STAMP(19); if (rd_ == 4) STAMP(21); if (rd_ == 8) STAMP(23); if (rd_ == 10) STAMP(24);
      if (rd_ == 0) {
        st4(ws, tag, tid == 0 ? 0 : OOB, WS_FF + j * 128);
        OPQ(w8);
        const f4 t = up4((u2){w8[0], w8[1]}) + up4((u2){w8[2], w8[3]});
        wsum = wave_sum((t[0] + t[1]) + (t[2] + t[3]));
      }
      if (rd_ + 3 < 11) issue(rd_ + 3);
      unsigned bg = tg + (rd_ & 3) * 32768, bd = td + (rd_ & 3) * 32768;
      OPQ(bg); OPQ(bd);
      if (rd_ < 8) {
#pragma unroll
        for (int ks = 0; ks < 2; ++ks)
#pragma unroll
          for (int c = 0; c < 3; ++c) G[2 * rd_ + ks][c] = trfrag2(bg + ks * 32 * 192 + c * 16, 4 * 192);
#pragma unroll
        for (int ks = 0; ks < 2; ++ks)
#pragma unroll
          for (int c = 0; c < 3; ++c) { if (rd_ + ks == 0) mAW<true>(a[c], one, G[0][c]); else mAW(a[c], one, G[2 * rd_ + ks][c]); }
      } else {
#pragma unroll
        for (int c = 0; c < 8; ++c) Dn[rd_ - 8][c] = trfrag2(bd + c * 32, 4 * 256);
        SB();
        if (rd_ == 8) gather(a[0], a[1], a[2], csg, csu);
      }
    }
    WAITL();
    BAR();
  }
  constexpr unsigned F_ACT = 0, F_KEY = 1024;
  const unsigned mo1 = lmask(l & 1), mo8 = lmask(l & 8), t2o = tid < 3 ? l * 8 : OOB;
  const bool live = g < 3 && 3 * wv + g < noct;
  const unsigned actw = F_ACT + (g < 3 ? (lr >> 3) * 192 + (24 * wv + 8 * g + (l & 7)) * 2 : 512 + l * 2);
  const int tok = 32 * (4 * j + wv) + (l & 31);
  unsigned sto, huo, loc;
  { const int p = tok >> 1, ti = max(p - 7, 0) / 68, u = p - 68 * ti, tl = (tok & 1) * 15 + ti, rho = u + u / 15; loc = tl * 128 + rho; sto = ((tl * 8 + 4 * (l >> 5)) * 80 + rho) * 16; }
  huo = l < 60 ? (wv * 60 + l) * 128 : OOB;
  unsigned cw = l < 32 ? (l >> 1) * 1024 + (4 * j + wv) * 16 + (l & 1) * 8 : OOB, co = j * 1024 + l * 16, keyw = F_KEY + (l == 0 ? wv * 4 : 64 + l * 4);
  unsigned m2 = lmask(l & 2), yo = lr < 4 ? j * 4096 + (l & 1) * 2048 + wv * 512 + (l & 2) * 32 + g * 16 : OOB;
  OPQ(sto); OPQ(huo); OPQ(loc); OPQ(cw); OPQ(co); OPQ(keyw); OPQ(yo);
  STAMP(1);

#pragma unroll 1
  for (int L = 0; L < 2; ++L) {
    {
      const unsigned t0 = now();
      unsigned t = t0;
      bool need = l < 60;
#pragma unroll 1
      for (int sp = 0; sp < SPIN_CAP && __any(need); ++sp) {
        const u2 z = peek8(ws, need ? huo : OOB, WS_HU + L * 32768);
        t = need ? z[0] : t;
        need = need && z[1] != tag;
      }
      lds_w<unsigned>(keyw, max_x32(max_x16(max_row16(t - t0 + 0x40000000u))) + t0);
      WAITL();
      BAR();
      const u4 q = lds_r<u4>(F_KEY) - t0;
      const unsigned tt = rfl(max(max(q[0], q[1]), max(q[2], q[3]))) + t0 - 0x40000000u + LAG_ST;
#pragma unroll 1
      for (int sp = 0; sp < 128 && (int)(tt - now()) > 0; ++sp) {}
    }
    STAMP(7 + 8 * L);
    u4 v[4];
#pragma unroll 1
    for (int sp = 0; sp < SPIN_CAP; ++sp) {
      CFENCE();
      CNT(12 + L);
#pragma unroll
      for (int i = 0; i < 4; ++i) v[i] = ld16(ws, sto, WS_ST + L * STB + i * 1280);
      if (__all(v[0][3] == tag && v[1][3] == tag && v[2][3] == tag && v[3][3] == tag)) break;
      __builtin_amdgcn_s_sleep(1);
    }
    STAMP(2 + 8 * L);
    {
      const f4 s = (asf(v[0]) + asf(v[1])) + (asf(v[2]) + asf(v[3]));
      const float m = xadd32(s[0]) * (1.f / 512), dt = xadd32(s[1]), qq = xadd32(s[2]) * (1.f / 512);
      const float sc = (dt - m * wsum) * rsq(fmx(qq - m * m, 0.f) + EPS);
      unsigned u = __float_as_uint(sc); u ^= (unsigned)((int)u >> 31) | 0x80000000u;
      const unsigned key = (u & 0xFFFFF000u) | loc;
      const unsigned k1 = max_x16(max_row16(key)), k2 = max_x16(max_row16(key == k1 ? 0u : key));
      st8(ws, (u2){bsel(mo1, k2, k1), tag}, cw, WS_CAND + L * 16384);
    }
    STAMP(3 + 8 * L);
    pause(LAG_C);
    u4 c;
#pragma unroll 1
    for (int sp = 0; sp < SPIN_CAP; ++sp) {
      CNT(14 + L);
      ld16_go(c, ws, co, WS_CAND + L * 16384);
      WAITV(0);
      OPQ(c);
      if (__all(c[1] == tag && c[3] == tag)) break;
    }
    STAMP(4 + 8 * L);
    const unsigned k1 = max_x32(max_x16(max_row16(c[0]))), k2 = max_x32(max_x16(max_row16(c[0] == k1 ? c[2] : c[0])));
    const unsigned ka = bsel(mo1, k2, k1), kb_ = bsel(mo8, k2, k1);
    const unsigned vo_ = ((ka >> 7) & 31) * TILEB + ((ka >> 4) & 7) * 16384 + (ka & 15) * 16 + g * 256;
    const unsigned so_ = (((kb_ >> 7) & 31) * 640 + (kb_ & 127)) * 16 + (l & 7) * 1280;
    {
      f4 a[3];
      float sm, sq;
#pragma unroll 1
      for (int sp = 0; sp < SPIN_CAP; ++sp) {
        CNT(16 + L);
        u4 h[16], sr;
        sfor<0, 16>([&](auto K_) __attribute__((always_inline)) {
          constexpr int ks = decltype(K_)::v;
          ld16_go<(ks & 3) * 1024>(h[ks], ws, vo_, WS_XB + L * BUFB + (ks >> 2) * 4096);
        });
        ld16_go(sr, ws, so_, WS_ST + L * STB);
        st8(ws, (u2){l == 2 ? now() + T_Y : ka, tag}, t2o, WS_SEL + L * 2048 + j * 128);
        unsigned m = 0;
        SB();
        sfor<0, 16>([&](auto K_) __attribute__((always_inline)) {
          constexpr int ks = decltype(K_)::v;
          WAITV(17 - ks);
          OPQ(h[ks]);
          mAW<ks == 0>(a[0], h[ks], G[ks][0]);
          if constexpr (ks & 1) { m = max(max(m, h[ks - 1][0]), h[ks][0]); OPQ(m); }
          SB();
          mAW<ks == 0>(a[1], h[ks], G[ks][1]); mAW<ks == 0>(a[2], h[ks], G[ks][2]);
          SB();
        });
        WAITV(1);
        OPQ(sr);
        sm = asf(sr)[0]; sq = asf(sr)[2]; sm += dppf<0xB1>(sm); sq += dppf<0xB1>(sq); sm += dppf<0x4E>(sm); sq += dppf<0x4E>(sq); sm += dppf<0x141>(sm); sq += dppf<0x141>(sq);
        if (__builtin_expect(!__any(m == SENT), 1)) break;
      }
      const float m = sm * (1.f / 512), rs = rsq(fmx(sq * (1.f / 512) - m * m, 0.f) + EPS);
      float gg, uu;
      SB();
      gather(a[0], a[1], a[2], gg, uu);
      gg = (gg - m * csg) * rs; uu = (uu - m * csu) * rs;
      const float act = live ? gg * sigm(gg) * uu : 0.f;
      lds_w<u16>(actw, (u16)pk(act, act));
    }
    STAMP(5 + 8 * L);
    WAITL();
    BAR();
    {
      u4 b[3];
#pragma unroll
      for (int ks = 0; ks < 3; ++ks) b[ks] = lds_r<u4>(F_ACT + (l & 1) * 192 + ks * 64 + g * 16);
      f4 y[8];
      auto put = [&](int c) { u4 w = asu(bsel4(m2, y[2 * c + 1], y[2 * c])); w[0] = umin(w[0], SENT - 1); st16(ws, w, yo, WS_YP + L * 65536 + c * 128); };
      SB();
#pragma unroll
      for (int c = 0; c < 4; ++c) {
        mWA<true>(y[2 * c], Dn[0][2 * c], b[0]); mWA<true>(y[2 * c + 1], Dn[0][2 * c + 1], b[0]);
        mWA(y[2 * c], Dn[1][2 * c], b[1]); mWA(y[2 * c + 1], Dn[1][2 * c + 1], b[1]);
        mVV(y[2 * c], Dn[2][2 * c], b[2]); mVV(y[2 * c + 1], Dn[2][2 * c + 1], b[2]);
        SB();
        if (c >= 2) { put(c - 2); SB(); }
      }
      put(2);
      SB();
      NOPS(1);
      put(3);
    }
    STAMP(6 + 8 * L);
  }
  if (j == 0) st4(ws, tag, tid == 0 ? 0 : OOB, WS_EPOCH);
  STAMP_FLUSH();
}

__global__ void __launch_bounds__(256, 1) fused_kernel(Params) {
  unsigned g_tent = 0;
  const int b = blockIdx.x;
  if (__builtin_expect(b < 240, 1)) role_a(b, g_tent);
  else role_f(b - 240, g_tent);
}

extern "C" __device__ __attribute__((used, noinline)) void code_end_marker() {}

extern "C" void kernel_launch(void* const* d_in, const int* in_sizes, int n_in, void* d_out, int out_size, void* d_ws, size_t ws_size, hipStream_t stream) {
  Params P;
  P.x = (const u16*)d_in[0]; P.wqkv = (const u16*)d_in[1]; P.wo = (const u16*)d_in[2];
  P.wr = (const u16*)d_in[4]; P.wg = (const u16*)d_in[5]; P.wu = (const u16*)d_in[6]; P.wd = (const u16*)d_in[7];
  P.out = (u16*)d_out; P.ws = (char*)d_ws;
  (void)hipFuncSetAttribute((const void*)fused_kernel, hipFuncAttributeMaxDynamicSharedMemorySize, LDS_BYTES);
  fused_kernel<<<256, 256, LDS_BYTES, stream>>>(P);
}
```

```cpp
#define STAMPS
#define STAMPMASK 0x2000000cu
#define PROBE_A 2
#define PROBE_B 3
#define PROBE_BLK 0
#define PROBE_MUL 10
#define PROBE_DIV 1
#include <hip/hip_runtime.h>
#include <hip/hip_bf16.h>
#include <stdint.h>

typedef unsigned short u16;
typedef unsigned long long u64;
typedef __bf16 bf2 __attribute__((ext_vector_type(2)));
typedef short s4 __attribute__((ext_vector_type(4)));
typedef float f4 __attribute__((ext_vector_type(4)));
typedef float f2 __attribute__((ext_vector_type(2)));
typedef unsigned u4 __attribute__((ext_vector_type(4)));
typedef unsigned u2 __attribute__((ext_vector_type(2)));
typedef __amdgpu_buffer_rsrc_t rsrc_t;
#define LAS __attribute__((address_space(3)))
#define DEV __device__ __forceinline__

constexpr unsigned TILEB = 81920;
constexpr unsigned BUFB = 30 * TILEB;
constexpr unsigned WS_EPOCH = 0;
constexpr unsigned WS_FF = 4096;
constexpr unsigned WS_SEL = 8192;
constexpr unsigned WS_CAND = 98304;
constexpr unsigned WS_FA = 32768;
constexpr unsigned WS_HU = 917504;
constexpr unsigned WS_XG = 983040;
constexpr unsigned WS_YP = 131072;
constexpr unsigned WS_ST = 262144;
constexpr unsigned STB = 30 * 8 * 80 * 16;
constexpr unsigned WS_OB = 1048576;
constexpr unsigned WS_XB = WS_OB + 2 * BUFB;
constexpr unsigned WS_USED = WS_XB + 2 * BUFB;
constexpr unsigned WS_STAMP = 16u << 20;
constexpr unsigned L_RING = 81920;
constexpr unsigned L_RED2 = L_RING + 16384;
constexpr unsigned L_MISC = 155648;
constexpr unsigned O_RED = 0;
constexpr unsigned O_RC = 4096, O_RM = O_RC + 320, O_RA = O_RM + 320, O_RB = O_RA + 320;
constexpr unsigned O_SEL = O_RB + 320;
constexpr unsigned LDS_BYTES = 163840;
constexpr unsigned SENT = 0xFFFFFFFFu, OOB = 0x80000000u, ONE2 = 0x3F803F80u;
#define T_O0 50
#define T_O1 75
#define T_ST 78
#define T_Y 105
#define LAG_O 15
#define LAG_ST 15
#define LAG_C 22
#define LAG_X 25
#define LAG_Y 15
#define HU_STEP 9
#define TOUCH 0
#define C1_BLOCK 13
#define CL_STEP 13
constexpr int SPIN_CAP = 1 << 15;
constexpr float EPS = 1e-5f;

#define CFENCE() asm volatile("" ::: "memory")
#define WAITC(vm, lg) do { CFENCE(); __builtin_amdgcn_s_waitcnt(((vm) & 15) | 0x70 | ((lg) << 8) | (((vm) >> 4) << 14)); CFENCE(); } while (0)
#define WAITV(n) asm volatile("s_waitcnt vmcnt(%0)" ::"i"(n) : "memory")
#define WAITL() WAITC(63, 0)
#define BAR() do { CFENCE(); __builtin_amdgcn_s_barrier(); CFENCE(); } while (0)
#define SB() __builtin_amdgcn_sched_barrier(0)
#define OPQ(x) asm volatile("" : "+v"(x))
#define KEEP(x) asm volatile("" ::"v"(x))

template <int V> struct IC { static constexpr int v = V; };
template <int I, int N, class F> DEV void sfor(F&& f) { if constexpr (I < N) { f(IC<I>{}); sfor<I + 1, N>(f); } }

typedef unsigned u16v __attribute__((ext_vector_type(16)));
struct Params {
  const u16* x; const u16* wqkv; const u16* wo; const u16* wr; const u16* wg; const u16* wu; const u16* wd;
  u16* out; char* ws;
};

struct KArg { u16v a; u2 b; };
DEV KArg args_go() {
  KArg K;
  asm volatile("s_load_dwordx16 %0, %2, 0x0\n\ts_load_dwordx2 %1, %2, 0x40" : "=&s"(K.a), "=&s"(K.b) : "s"(__builtin_amdgcn_kernarg_segment_ptr()));
  return K;
}
DEV Params args_wait(KArg K) {
  asm volatile("s_waitcnt lgkmcnt(0)" : "+s"(K.a), "+s"(K.b) :: "memory");
  auto p = [](unsigned lo, unsigned hi) { return (char*)(((u64)hi << 32) | lo); };
  Params P;
  P.x = (const u16*)p(K.a[0], K.a[1]); P.wqkv = (const u16*)p(K.a[2], K.a[3]); P.wo = (const u16*)p(K.a[4], K.a[5]); P.wr = (const u16*)p(K.a[6], K.a[7]);
  P.wg = (const u16*)p(K.a[8], K.a[9]); P.wu = (const u16*)p(K.a[10], K.a[11]); P.wd = (const u16*)p(K.a[12], K.a[13]); P.out = (u16*)p(K.a[14], K.a[15]);
  P.ws = p(K.b[0], K.b[1]);
  return P;
}
DEV rsrc_t mk(const void* p, unsigned bytes) { return __builtin_amdgcn_make_buffer_rsrc((void*)p, 0, bytes, 0x00020000); }
DEV void setm0(unsigned base, int imm) { asm volatile("s_add_u32 m0, %0, %1" ::"s"(base), "i"(imm) : "scc", "memory"); }
template <int SC> DEV void dma_go(rsrc_t rs, unsigned voff, unsigned soff) {
  if (SC) asm volatile("buffer_load_dwordx4 %0, %1, %2 offen sc1 lds" ::"v"(voff), "s"(rs), "s"(soff) : "memory");
  else asm volatile("buffer_load_dwordx4 %0, %1, %2 offen lds" ::"v"(voff), "s"(rs), "s"(soff) : "memory");
}
DEV void dma_go_m(rsrc_t rs, unsigned voff, unsigned soff, u64 mask) {
  asm volatile("s_mov_b64 exec, %3\n\tbuffer_load_dwordx4 %0, %1, %2 offen lds\n\ts_mov_b64 exec, -1" ::"v"(voff), "s"(rs), "s"(soff), "s"(mask) : "memory");
}
template <int SC> DEV void dma(rsrc_t rs, unsigned voff, unsigned soff, unsigned base, int imm) {
  if (SC) asm volatile("s_add_u32 m0, %3, %4\n\ts_nop 0\n\tbuffer_load_dwordx4 %0, %1, %2 offen sc1 lds" ::"v"(voff), "s"(rs), "s"(soff), "s"(base), "i"(imm) : "scc", "memory");
  else asm volatile("s_add_u32 m0, %3, %4\n\ts_nop 0\n\tbuffer_load_dwordx4 %0, %1, %2 offen lds" ::"v"(voff), "s"(rs), "s"(soff), "s"(base), "i"(imm) : "scc", "memory");
}
DEV void st16(rsrc_t rs, u4 v, unsigned voff, unsigned soff) {
  asm volatile("buffer_store_dwordx4 %0, %1, %2, %3 offen sc1\n\ts_nop 1" ::"v"(v), "v"(voff), "s"(rs), "s"(soff) : "memory");
}
DEV void touch_go(unsigned& d, rsrc_t rs, unsigned voff, unsigned soff) { asm volatile("buffer_load_dword %0, %1, %2, %3 offen" : "=v"(d) : "v"(voff), "s"(rs), "s"(soff) : "memory"); }
DEV void wrap_add(unsigned& o) { asm volatile("s_add_u32 %0, %0, 0x18000\n\ts_and_b32 %0, %0, 0x7ffff" : "+s"(o) :: "scc"); }
DEV void st16c(rsrc_t rs, u4 v, unsigned voff, unsigned soff) {
  asm volatile("buffer_store_dwordx4 %0, %1, %2, %3 offen sc1" ::"v"(v), "v"(voff), "s"(rs), "s"(soff) : "memory");
}
DEV void st4(rsrc_t rs, unsigned v, unsigned voff, unsigned soff) {
  asm volatile("buffer_store_dword %0, %1, %2, %3 offen sc1" ::"v"(v), "v"(voff), "s"(rs), "s"(soff) : "memory");
}
DEV u4 ld16(rsrc_t rs, unsigned voff, unsigned soff) { return __builtin_amdgcn_raw_buffer_load_b128(rs, voff, soff, 16); }
DEV unsigned ld4(rsrc_t rs, unsigned voff, unsigned soff) { return __builtin_amdgcn_raw_buffer_load_b32(rs, voff, soff, 16); }

template <bool Z = false> DEV void mWA(f4& c, u4 w, u4 a) {
  if (Z) asm volatile("v_mfma_f32_16x16x32_bf16 %0, %1, %2, 0" : "=v"(c) : "a"(w), "v"(a));
  else asm volatile("v_mfma_f32_16x16x32_bf16 %0, %1, %2, %0" : "+v"(c) : "a"(w), "v"(a));
}
template <bool Z = false> DEV void mAW(f4& c, u4 a, u4 w) {
  if (Z) asm volatile("v_mfma_f32_16x16x32_bf16 %0, %1, %2, 0" : "=v"(c) : "v"(a), "a"(w));
  else asm volatile("v_mfma_f32_16x16x32_bf16 %0, %1, %2, %0" : "+v"(c) : "v"(a), "a"(w));
}
template <bool Z = false> DEV void mVV(f4& c, u4 a, u4 b) {
  if (Z) asm volatile("v_mfma_f32_16x16x32_bf16 %0, %1, %2, 0" : "=v"(c) : "v"(a), "v"(b));
  else asm volatile("v_mfma_f32_16x16x32_bf16 %0, %1, %2, %0" : "+v"(c) : "v"(a), "v"(b));
}
template <bool Z = false> DEV void m16(f4& c, u2 a, u2 b) {
  if (Z) asm volatile("v_mfma_f32_16x16x16_bf16 %0, %1, %2, 0" : "=v"(c) : "v"(a), "v"(b));
  else asm volatile("v_mfma_f32_16x16x16_bf16 %0, %1, %2, %0" : "+v"(c) : "v"(a), "v"(b));
}
DEV f4 m16c(u2 a, u2 b, f4 c) { f4 d; asm volatile("v_mfma_f32_16x16x16_bf16 %0, %1, %2, %3" : "=&v"(d) : "v"(a), "v"(b), "v"(c)); return d; }
template <bool Z = false> DEV void mf32(f4& c, float a, float b) {
  if (Z) asm volatile("v_mfma_f32_16x16x4_f32 %0, %1, %2, 0" : "=&v"(c) : "v"(a), "v"(b));
  else asm volatile("v_mfma_f32_16x16x4_f32 %0, %1, %2, %0" : "+v"(c) : "v"(a), "v"(b));
}
#define NOPS(n) asm volatile("s_nop %0" ::"i"((n) - 1))
DEV unsigned pk(float a, float b) { f2 v = {a, b}; return __builtin_bit_cast(unsigned, __builtin_convertvector(v, bf2)); }
DEV u2 pk4(f4 v) { return (u2){pk(v[0], v[1]), pk(v[2], v[3])}; }
DEV float lo(unsigned u) { return __uint_as_float(u << 16); }
DEV float hi(unsigned u) { return __uint_as_float(u & 0xFFFF0000u); }
DEV f4 up4(u2 u) { return (f4){lo(u[0]), hi(u[0]), lo(u[1]), hi(u[1])}; }
DEV f4 asf(u4 v) { return __builtin_bit_cast(f4, v); }
DEV u4 asu(f4 v) { return __builtin_bit_cast(u4, v); }
DEV unsigned bsel(unsigned m, unsigned a, unsigned b) { return __builtin_amdgcn_bitop3_b32(m, a, b, 0xCA); }
DEV float bself(unsigned m, float a, float b) { return __uint_as_float(bsel(m, __float_as_uint(a), __float_as_uint(b))); }
DEV f4 bsel4(unsigned m, f4 a, f4 b) { return (f4){bself(m, a[0], b[0]), bself(m, a[1], b[1]), bself(m, a[2], b[2]), bself(m, a[3], b[3])}; }
DEV unsigned lmask(bool c) { unsigned m = c ? ~0u : 0u; OPQ(m); return m; }
DEV float fmx(float a, float b) { return __builtin_elementwise_maximum(a, b); }
template <class T> DEV T lds_r(unsigned a) { return *(const LAS T*)(size_t)a; }
template <class T> DEV void lds_w(unsigned a, T v) { *(LAS T*)(size_t)a = v; }
DEV u4 trfrag2(unsigned a, unsigned step) {
  u2 x = __builtin_bit_cast(u2, __builtin_amdgcn_ds_read_tr16_b64_v4i16((LAS s4*)(size_t)a));
  u2 y = __builtin_bit_cast(u2, __builtin_amdgcn_ds_read_tr16_b64_v4i16((LAS s4*)(size_t)(a + step)));
  return (u4){x[0], x[1], y[0], y[1]};
}
DEV f2 swap16(float a, float b) { auto r = __builtin_amdgcn_permlane16_swap(__float_as_uint(a), __float_as_uint(b), false, false); return (f2){__uint_as_float(r[0]), __uint_as_float(r[1])}; }
DEV f2 swap32(float a, float b) { auto r = __builtin_amdgcn_permlane32_swap(__float_as_uint(a), __float_as_uint(b), false, false); return (f2){__uint_as_float(r[0]), __uint_as_float(r[1])}; }
DEV float xadd16(float v) { f2 r = swap16(v, v); return r[0] + r[1]; }
DEV float xadd32(float v) { f2 r = swap32(v, v); return r[0] + r[1]; }
template <int C> DEV unsigned dpp(unsigned v) { return __builtin_amdgcn_update_dpp(0u, v, C, 0xF, 0xF, true); }
template <int C> DEV float dppf(float v) { return __uint_as_float(dpp<C>(__float_as_uint(v))); }
DEV float wave_sum(float v) {
  v += dppf<0xB1>(v); v += dppf<0x4E>(v); v += dppf<0x141>(v); v += dppf<0x140>(v);
  return xadd32(xadd16(v));
}
DEV unsigned max_row16(unsigned v) { v = max(v, dpp<0xB1>(v)); v = max(v, dpp<0x4E>(v)); v = max(v, dpp<0x141>(v)); return max(v, dpp<0x140>(v)); }
DEV unsigned max_x16(unsigned v) { auto r = __builtin_amdgcn_permlane16_swap(v, v, false, false); return max(r[0], r[1]); }
DEV unsigned max_x32(unsigned v) { auto r = __builtin_amdgcn_permlane32_swap(v, v, false, false); return max(r[0], r[1]); }
DEV unsigned umin(unsigned a, unsigned b) { return a < b ? a : b; }
DEV float dot2(unsigned a, unsigned b, float c) { return __builtin_amdgcn_fdot2_f32_bf16(__builtin_bit_cast(bf2, a), __builtin_bit_cast(bf2, b), c, false); }
DEV unsigned rfl(unsigned v) { return __builtin_amdgcn_readfirstlane(v); }
DEV unsigned rdl(unsigned v, int l) { return __builtin_amdgcn_readlane(v, l); }
DEV float rsq(float v) { return __builtin_amdgcn_rsqf(v); }
DEV float sigm(float v) { return __builtin_amdgcn_rcpf(1.f + __builtin_amdgcn_exp2f(-1.44269504f * v)); }
DEV float key_score(unsigned k) { unsigned u = k & 0xFFFFF000u; return __uint_as_float((u & 0x80000000u) ? (u ^ 0x80000000u) : ~u); }

#define DYNOK 1
#define STAMP(i) do { if (__builtin_constant_p(i) ? !((STAMPMASK >> (i)) & 1) : (!DYNOK || !((STAMPMASK >> (i)) & 1))) break; u64 a_, b_; asm volatile("s_memrealtime %0\n\ts_memtime %1\n\ts_waitcnt lgkmcnt(0)" : "=&s"(a_), "=&s"(b_) :: "memory"); \
    asm volatile("s_mov_b32 m0, %3\n\ts_nop 0\n\tv_writelane_b32 %0, %1, m0\n\ts_add_u32 m0, m0, 32\n\ts_nop 0\n\tv_writelane_b32 %0, %2, m0" : "+v"(stv) : "s"((unsigned)a_), "s"((unsigned)b_), "s"(i) : "scc"); } while (0)
#define CNT(i) do { unsigned c_; asm volatile("s_mov_b32 m0, %2\n\ts_nop 1\n\tv_readlane_b32 %1, %0, m0\n\ts_nop 3\n\ts_add_u32 %1, %1, 1\n\ts_nop 1\n\tv_writelane_b32 %0, %1, m0" : "+v"(cntv), "=&s"(c_) : "s"(i) : "scc"); } while (0)
#define TC(set, i)
#define TCFLUSH(set, n)
DEV void tc_put(u64& t) { asm volatile("s_memtime %0" : "=s"(t) :: "memory"); }
DEV void wl_put(unsigned& v, unsigned d, int q) { asm volatile("s_mov_b32 m0, %2\n\ts_nop 1\n\tv_writelane_b32 %0, %1, m0" : "+v"(v) : "s"(d), "s"(q)); }
#define STAMP_INIT() u64 tcv[20]; (void)tcv; unsigned stv = 0, cntv = 0; asm volatile("v_writelane_b32 %0, %1, 28" : "+v"(stv) : "s"(g_tent))
#define PROBE_X 0
#define STAMP_FLUSH() do { STAMP(29); if (PROBE_X && blockIdx.x == PROBE_BLK) { unsigned o_ = rfl(ld4(ws, 0, 2048)); asm volatile("v_writelane_b32 %0, %1, 27" : "+v"(stv) : "s"(o_)); } if (blockIdx.x == PROBE_BLK && threadIdx.x < 64) { unsigned d_ = min(max((int)(rdl(stv, PROBE_B) - rdl(stv, PROBE_A)) / PROBE_DIV + (PROBE_X ? 200 : 0), 0), 5000) * PROBE_MUL, t_ = rdl(stv, 29); \
    for (int sp_ = 0; sp_ < (1 << 20) && (unsigned)__builtin_amdgcn_s_memrealtime() - t_ < d_; ++sp_) __builtin_amdgcn_s_sleep(2); } } while (0)
DEV unsigned tag_issue(const char* ws) { unsigned e; asm volatile("s_load_dword %0, %1, 0x0" : "=s"(e) : "s"(ws) : "memory"); return e; }
DEV unsigned tag_finish(unsigned e) { unsigned t; asm volatile("s_waitcnt lgkmcnt(0)\n\ts_add_u32 %0, %1, 1" : "=s"(t) : "s"(e) : "memory", "scc"); return t; }

DEV unsigned peek4(rsrc_t ws, unsigned voff, unsigned soff) {
  unsigned z = 0;
  asm volatile("buffer_atomic_or %0, %1, %2, %3 offen sc0 sc1\n\ts_waitcnt vmcnt(0)" : "+v"(z) : "v"(voff), "s"(ws), "s"(soff) : "memory");
  return z;
}
DEV u2 peek8(rsrc_t ws, unsigned voff, unsigned soff) {
  u2 z = {0, 0};
  asm volatile("buffer_atomic_or_x2 %0, %1, %2, %3 offen sc0 sc1\n\ts_waitcnt vmcnt(0)" : "+v"(z) : "v"(voff), "s"(ws), "s"(soff) : "memory");
  return z;
}
DEV void peek8_go(u2& z, rsrc_t ws, unsigned voff, unsigned soff) {
  asm volatile("buffer_atomic_or_x2 %0, %1, %2, %3 offen sc0 sc1" : "+v"(z) : "v"(voff), "s"(ws), "s"(soff) : "memory");
}
template <int OFF = 0> DEV void ld16_go(u4& v, rsrc_t ws, unsigned voff, unsigned soff) {
  asm volatile("buffer_load_dwordx4 %0, %1, %2, %3 offen offset:%4 sc1" : "=v"(v) : "v"(voff), "s"(ws), "s"(soff), "i"(OFF) : "memory");
}
DEV u64 time_go() { u64 t; asm volatile("s_memrealtime %0" : "=s"(t) :: "memory"); return t; }
DEV unsigned time_get(u64 t) { asm volatile("s_waitcnt lgkmcnt(0)" : "+s"(t) :: "memory"); return (unsigned)t; }
DEV unsigned now() { return (unsigned)__builtin_amdgcn_s_memrealtime(); }
DEV void wait_gate(rsrc_t ws, u2 z, unsigned voff, unsigned soff, unsigned tag, int lag) {
  unsigned t = z[0];
  bool need = voff != OOB && z[1] != tag;
#pragma unroll 1
  for (int sp = 0; sp < SPIN_CAP && __any(need); ++sp) {
    z = peek8(ws, need ? voff : OOB, soff);
    t = need ? z[0] : t;
    need = need && z[1] != tag;
  }
  t += lag;
#pragma unroll 1
  for (int sp = 0; sp < 64 && __any(voff != OOB && (int)(t - now()) > 0); ++sp) {}
}
DEV void wait_flags_ld(rsrc_t ws, unsigned voff, unsigned soff, unsigned tag) {
#pragma unroll 1
  for (int sp = 0; sp < SPIN_CAP; ++sp) {
    CFENCE();
    if (__all(ld4(ws, voff, soff) == tag)) break;
    __builtin_amdgcn_s_sleep(1);
  }
}
DEV unsigned wait_word(rsrc_t ws, unsigned voff, unsigned soff, unsigned tag) {
  u2 z;
#pragma unroll 1
  for (int sp = 0; sp < SPIN_CAP; ++sp) { z = peek8(ws, voff, soff); if (__all(z[1] == tag || voff == OOB)) break; __builtin_amdgcn_s_sleep(2); }
  return z[0];
}
DEV void st8(rsrc_t rs, u2 v, unsigned voff, unsigned soff) {
  asm volatile("buffer_store_dwordx2 %0, %1, %2, %3 offen sc1" ::"v"(v), "v"(voff), "s"(rs), "s"(soff) : "memory");
}
DEV void pause(int ticks) {
  const unsigned t = (unsigned)__builtin_amdgcn_s_memrealtime();
#pragma unroll 1
  for (int sp = 0; sp < 256 && (unsigned)__builtin_amdgcn_s_memrealtime() - t < (unsigned)ticks; ++sp) {}
}

struct P1 {
  static constexpr int op(int s, int k) {
    constexpr int T[16][5] = {{72, 73, 3, -1, -1}, {74, 75, 4, -1, -1}, {76, 5, -1, -1, -1}, {80, 6, -1, -1, -1}, {81, 82, 7, -1, -1}, {83, 84, 8, -1, -1},
                              {9, -1, -1, -1, -1}, {88, 10, -1, -1, -1}, {89, 90, 11, -1, -1}, {91, 92, 12, -1, -1}, {13, -1, -1, -1, -1},
                              {14, -1, -1, -1, -1}, {15, -1, -1, -1, -1}, {-1, -1, -1, -1, -1}, {128, -1, -1, -1, -1}, {129, 130, -1, -1, -1}};
    return T[s][k];
  }
  static constexpr int size(int o) { return o < 0 ? 0 : (o >> 6) == 1 ? 1 : 3; }
  static constexpr int ndma(int s) { int n = 0; for (int k = 0; k < 5; ++k) n += size(op(s, k)); return n; }
  static constexpr int ntouch(int s) { return (TOUCH & 2) && s == 0 ? 1 : (TOUCH & 4) && s == 1 ? 1 : 0; }
  static constexpr int nops(int s) { return ndma(s) + ntouch(s) + (s == C1_BLOCK ? 3 : 0); }
  static constexpr int sub(int s, int d) { for (int k = 0; k < 5; ++k) { if (d < size(op(s, k))) return op(s, k) * 4 + d; d -= size(op(s, k)); } return -1; }
  static constexpr int pos(int o) {
    if (o < 3) return 8 + 3 * o;
    int n = 15;
    for (int s = 0; s < 16; ++s) { int m = n; for (int k = 0; k < 5; ++k) { m += size(op(s, k)); if (op(s, k) == o) return m; } n += nops(s); }
    return 0;
  }
  static constexpr int nv(int s) {
    int n = 15; for (int t = 0; t < s; ++t) n += nops(t);
    int need = pos(s);
    if (s == 4 || s == 8 || s == 12) { const int x = pos(64 + (s >> 2) * 8 + 4); need = x > need ? x : need; }
    return n - need;
  }
};
struct RowC { float c[5], cm[5]; f4 c4[5], cm4[5]; };
DEV void rowc_read(RowC& R, unsigned r1, unsigned r4, int i) {
  const int t = i / 3;
  if (i % 3 == 0) { R.c[t] = lds_r<float>(r1 + t * 64); R.cm[t] = lds_r<float>(r1 + 320 + t * 64); }
  else if (i % 3 == 1) R.c4[t] = lds_r<f4>(r4 + t * 64);
  else R.cm4[t] = lds_r<f4>(r4 + 320 + t * 64);
}

DEV void role_a(const int b, unsigned g_tent) {
  const KArg K = args_go();
  const int wv = __builtin_amdgcn_readfirstlane(threadIdx.x >> 6), l = threadIdx.x & 63;
  unsigned pfc;
  {
    u64 pc; unsigned len;
    asm volatile("s_getpc_b64 %0\n.Lpf_here:\n\ts_mov_b32 %1, code_end_marker-.Lpf_here" : "=s"(pc), "=s"(len));
    const rsrc_t rc = mk((const void*)pc, len);
    asm volatile("buffer_load_dword %0, %1, %2, %3 offen" : "=v"(pfc) : "v"(min(l, 2) * 128), "s"(rc), "s"(((b >> 3) * 4 + wv) * 384) : "memory");
  }
  const int xcd = b & 7, r = b >> 3;
  const int q = (xcd >> 1) * 30 + r, tile = q >> 2, hg = (xcd & 1) * 4 + (q & 3);
  const int lr = l & 15, g = l >> 4;
  const int par = tile >= 15, p0 = 68 * (tile - 15 * par), rr = 4 * wv + g, k8 = l >> 3;
  unsigned xoff = (2 * (p0 + rr) + par) * 1024 + ((lr ^ rr) << 4);
  const unsigned swA = (((l & 7) ^ (((k8 >> 1) & 1) << 1)) << 4) ^ ((wv & 1) << 6);
  unsigned woffA = k8 * 3072 + hg * 128 + swA, woffB = woffA ^ 64, wro = (16 * wv + 4 * g + 64 * hg) * 2;
  unsigned lx = wv * 1024, lw = L_RING + wv * 3072, wso[3];
  u64 xmask = wv == 3 ? 0x0000FFFFFFFFFFFFull : ~0ull;
#pragma unroll
  for (int j = 0; j < 3; ++j) { const int i = wv * 3 + j; wso[j] = (i & 3) * 8 * 3072 + (i >> 2) * 1024; }
  asm volatile("" : "+v"(xoff), "+v"(woffA), "+v"(woffB), "+v"(wro));
  asm volatile("" : "+s"(lx), "+s"(lw), "+s"(wso[0]), "+s"(wso[1]), "+s"(wso[2]), "+s"(xmask));
  const Params P = args_wait(K);
  const rsrc_t ws = mk(P.ws, WS_USED);
  const unsigned epoch = tag_issue(P.ws);
  STAMP_INIT();
  STAMP(0);
  if (PROBE_X && b == 0 && wv == 0) st4(ws, g_tent, l == 0 ? 0 : OOB, 2048);
  u4 W[16][3];
  f4 aq[5], ak[5], av[5];
  u2 xp[5];
  RowC R;
  unsigned tag, pm;
  f4 mask;
  u2 wr2, spa[4];
  unsigned trb, ooff, ooff4, lo2, l16, fao, l0, lw0, row3, sv_, hg0, so_[4], t4[3];
  {
    const rsrc_t rx = mk(P.x, 2048 * 1024), rw = mk(P.wqkv, 512 * 3072), rwo = mk(P.wo, 512 * 1024);
    auto ldx = [&](int pr) {
      asm volatile("s_mov_b64 exec, %0" ::"s"(xmask) : "memory");
#pragma unroll
      for (int j = 0; j < 5; ++j) dma<0>(rx, xoff, pr * 256 + j * 30720, lx, pr * 20480 + j * 4096);
      asm volatile("s_mov_b64 exec, -1" ::: "memory");
    };
    auto ldw = [&](int s) {
#pragma unroll
      for (int j = 0; j < 3; ++j) dma<0>(rw, (j & 1) ? woffB : woffA, wso[j] + s * 98304, lw, (s % 6) * 12288 + j * 1024);
    };
    asm volatile("global_load_dwordx2 %0, %1, %2" : "=v"(wr2) : "v"(wro), "s"(P.wr) : "memory");
    SB();
    ldx(0);
    SB();
    ldw(0); ldw(1); ldw(2);
    unsigned touch, touch1 = 0, touch2 = 0;
    auto wline = [&](int first, int per) {
      const int idx = first * 384 + (r * 4 + wv) * per + min(l, per - 1);
      return (idx >> 2) * 1024 + (idx & 3) * 128;
    };
    if (TOUCH & 1) {
      asm volatile("buffer_load_dword %0, %1, %2, %3 offen" : "=v"(touch) : "v"(wline(4, 16)), "s"(rw), "s"((xcd & 1) * 512) : "memory");
    } else {
      const int rh = (3 - (int)(((size_t)P.wqkv) >> 7)) & 3;
      asm volatile("buffer_load_dword %0, %1, %2, %3 offen" : "=v"(touch) : "v"(min(l, 12) * 1024), "s"(rw), "s"((r * 52 + wv * 13) * 1024 + ((xcd & 1) * 4 + rh) * 128) : "memory");
    }
    unsigned to1 = (2 * (p0 + (q & 3) * 19 + wv * 5 + min(l, 29) / 6) + par) * 1024 + 256 + (min(l, 29) % 6) * 128, to2 = wline(9, 23);
    OPQ(to1); OPQ(to2);
    woffA += 3 * 98304; woffB += 3 * 98304;
    SB();
    {
      const u4 one = {ONE2, ONE2, ONE2, ONE2};
      lds_w<u4>(rr * 4096 + 15 * 256 + lr * 16, one);
      lds_w<u4>(g * 4096 + 16 * 4096 + 15 * 256 + lr * 16, one);
    }
    ooff = (((g & 1) * 32 + wv) * 2 + (g >> 1)) * 256 + lr * 16;
    ooff4 = ooff | ((g & 1) << 31);
    lo2 = ooff + 65536 - (g & 1) * (65536 - 2048);
    l16 = l * 16;
    fao = wv == 0 && l < 32 ? tile * 1024 + l * 32 : OOB;
    lw0 = l == 0 ? 0 : OOB;
    OPQ(lw0);
    row3 = wv < 2 ? wv * 24 + min(l, 23) : 16 + wv * 16 + min(l, 15);
    sv_ = l < (wv < 2 ? 24 : 16) ? row3 * 16 : OOB;
    hg0 = wv == 0 && l < 2 ? l * 65536 : OOB;
#pragma unroll
    for (int i = 0; i < 3; ++i) { so_[i] = WS_OB + tile * TILEB + ((2 * hg + 2 + 4 * i + wv) & 15) * 1024; t4[i] = min((wv >> 1) * 3 + i, 4) * 16384; }
    so_[3] = WS_OB + tile * TILEB + ((2 * hg + 14 + (wv & 1)) & 15) * 1024;
    asm volatile("" : "+s"(so_[0]), "+s"(so_[1]), "+s"(so_[2]), "+s"(so_[3]), "+s"(t4[0]), "+s"(t4[1]), "+s"(t4[2]));
    OPQ(row3); OPQ(sv_); OPQ(hg0);
    OPQ(lo2); OPQ(l16); OPQ(fao);
    unsigned obase = WS_OB + tile * TILEB + hg * 2048, wo5 = ((15 + 2 * hg) & 15) * 32768 + wv * 8192;
    u4 sent = {SENT, SENT, SENT, SENT};
    unsigned wooffA = k8 * 1024 + hg * 128 + swA, wooffB = wooffA ^ 64;
    unsigned xa[4], xh[4], oso[3];
#pragma unroll
    for (int i = 0; i < 4; ++i) { xa[i] = lr * 256 + (((4 * i + g) ^ lr) << 4); xh[i] = xa[i] + 61440; OPQ(xa[i]); OPQ(xh[i]); }
#pragma unroll
    for (int j = 0; j < 3; ++j) { const int i = wv * 3 + j; oso[j] = (((i >> 2) + 2 * hg) & 15) * 32768 + (i & 3) * 8192; }
    unsigned xg = lr * 256 + (((4 * wv + g) ^ lr) << 4), xgh = xg + 61440;
    unsigned xr = (hg >> 1) * 20480 + lr * 256 + ((((hg & 1) * 8 + 2 * wv + (g >> 1)) ^ lr) << 4) + 8 * (g & 1);
    const unsigned dum = L_MISC + O_RED + 2560 + l * 4, red = L_MISC + O_RED + (wv * 80 + lr) * 8;
    unsigned mq = bsel(lmask(g == (lr >> 2)), red + 4, dum), ms = bsel(lmask(g == 3), red, dum);
    unsigned rb_ = L_MISC + O_RED + (wv * 20 + min(l, 19)) * 8, rc_ = L_MISC + O_RC + (wv * 20 + min(l, 19)) * 4;
    unsigned r1 = L_MISC + O_RC + lr * 4, r4 = L_MISC + O_RC + g * 16;
    l0 = (wv * 64 + l) == 0 ? 0 : OOB;
    const unsigned m1 = lmask(l & 1), m2 = lmask(l & 2);
    unsigned mt[4], sA = ONE2 & ~(m1 | m2), sB = m1 & ~m2;
#pragma unroll
    for (int t = 0; t < 4; ++t) mt[t] = lmask((lr >> 2) == t);
    pm = lmask(lr < 8 && g >= 2);
#pragma unroll
    for (int i = 0; i < 4; ++i) { const int rk = 4 * g + i, d = lr + (pm & 15) - rk; mask[i] = ((unsigned)d <= 7 && rk != 15) ? 0.f : -1e30f; }
    trb = L_RING + (8 * g + (lr >> 2)) * 128 + (((2 * wv + ((l & 3) >> 1)) ^ ((((lr >> 3) & 1) | ((g & 1) << 1)) << 1)) << 4) + 8 * (l & 1);
    OPQ(ooff); OPQ(ooff4); OPQ(sent); OPQ(wooffA); OPQ(wooffB); OPQ(xg); OPQ(xgh); OPQ(xr); OPQ(mq); OPQ(ms); OPQ(rb_); OPQ(rc_); OPQ(r1); OPQ(r4); OPQ(l0);
    OPQ(sA); OPQ(sB); OPQ(mask); OPQ(trb);
    asm volatile("" : "+s"(obase), "+s"(wo5), "+s"(oso[0]), "+s"(oso[1]), "+s"(oso[2]));
    OPQ(woffA); OPQ(woffB);
    tag = tag_finish(epoch);
    f4 gr[5];
    u4 a[2][5], z[2][5];
    f2 rv[4];
    float dg, sa, sb;
    sfor<0, 17>([&](auto S_) __attribute__((always_inline)) {
      constexpr int s = decltype(S_)::v;
      if constexpr (s < 16) WAITC(P1::nv(s), 0); else WAITL();
      BAR();
      TC(1, s);
      if (s == 0) STAMP(1);
      if (s == 4) STAMP(19);
      if (s == 8) STAMP(20);
      if (s == 12) STAMP(21);
      if (s == 16) STAMP(22);
      SB();
      constexpr auto gtile = [](int s_, int i) constexpr {
        if (s_ < 0) return -1;
        if (s_ < 5) return i == 0 ? s_ : -1;
        if (s_ < 7) return i < 2 ? 8 + 2 * (s_ - 5) + i : -1;
        if (s_ == 7) return i == 0 ? 12 : -1;
        if (s_ == 8) return i < 2 ? 16 + i : -1;
        if (s_ < 12) return i == 0 ? 16 + s_ - 7 : -1;
        if (s_ == 12) return 24 + i;
        return -1;
      };
      constexpr int nd = s < 16 ? P1::ndma(s) : 0, ng0 = s == 13 ? 5 : 0;
      sfor<0, 20>([&](auto I_) __attribute__((always_inline)) {
        constexpr int i = decltype(I_)::v;
        if constexpr (s > 0) {
          constexpr int pb = (s - 1) & 1, im = i - ng0, t = im / 3, ig = ng0 ? i : i - 15;
          if constexpr (im >= 0 && im < 15) {
            if constexpr (im % 3 == 0) mWA<s == 1>(aq[t], W[s - 1][0], a[pb][t]); else if constexpr (im % 3 == 1) mWA<s == 1>(ak[t], W[s - 1][1], a[pb][t]); else mAW<s == 1>(av[t], a[pb][t], W[s - 1][2]);
          } else if constexpr (gtile(s - 1, ig) >= 0) mVV<(gtile(s - 1, ig) < 8)>(gr[gtile(s - 1, ig) & 7], z[pb][ig], z[pb][ig]);
        }
        if constexpr (s < 16) {
          constexpr bool dense = nd > 5;
          constexpr int d0 = dense ? i - 3 : ((i - 3) & 1) == 0 ? (i - 3) / 2 : -1, d1 = dense ? i - 3 : ((i - 3) & 1) == 1 ? (i - 3) / 2 : -1;
          if constexpr (i >= 3 && d0 >= 0 && d0 < nd) {
            constexpr int o = P1::sub(s, d0) >> 2, j = P1::sub(s, d0) & 3;
            if constexpr ((o >> 6) == 0) setm0(lw, (o % 6) * 12288 + j * 1024);
            else if constexpr ((o >> 6) == 1) setm0(lx, ((o >> 3) & 7) * 20480 + (o & 7) * 4096);
            else setm0(lw, ((4 + (o & 7)) % 6) * 12288 + j * 1024);
          }
          if constexpr (i < 3) W[s][i] = trfrag2(trb + (s % 6) * 12288 + i * 4096, 512);
          else if constexpr (i < 8) a[s & 1][i - 3] = lds_r<u4>((s < 12 ? xa[s & 3] + (s >> 2) * 20480 : xh[s & 3]) + (i - 3) * 4096);
          else if constexpr (i < 13 && gtile(s, i - 8) >= 0) z[s & 1][i - 8] = lds_r<u4>((s < 12 ? xg + (gtile(s, i - 8) >> 3) * 20480 : xgh) + (gtile(s, i - 8) & 7) * 4096);
          if constexpr (i >= 3 && d1 >= 0 && d1 < nd) {
            constexpr int o = P1::sub(s, d1) >> 2, j = P1::sub(s, d1) & 3;
            if constexpr (dense || s == 0) { SB(); if constexpr ((o >> 6) != 1) NOPS(1); }
            if constexpr ((o >> 6) == 0) {
              dma_go<0>(rw, (j & 1) ? woffB : woffA, wso[j]);
              if constexpr (o < 15 && j == 1) { woffB += 98304; OPQ(woffB); }
              if constexpr (o < 15 && j == 2) { woffA += 98304; OPQ(woffA); }
            } else if constexpr ((o >> 6) == 1) dma_go_m(rx, xoff, ((o >> 3) & 7) * 256 + (o & 7) * 30720, xmask);
            else { dma_go<0>(rwo, (j & 1) ? wooffB : wooffA, oso[j]); wrap_add(oso[j]); }
          }
          if constexpr (P1::ntouch(s) && i == 14) { if constexpr (s == 0) touch_go(touch1, rx, to1, 0); else touch_go(touch2, rw, to2, (xcd & 1) * 512); }
          if constexpr (s == C1_BLOCK && (i == 5 || i == 8 || i == 11)) st16c(ws, sent, i < 11 ? ooff : ooff4, obase + (i - 5) / 3 * 32768);
          if constexpr (s == C1_BLOCK && i == 14) KEEP(sent);
          if constexpr (s == 2 && i == 13) { OPQ(wr2); wr2 = (wr2 & sB) | sA; OPQ(wr2); }
          if constexpr ((s == 5 || s == 7) && (i == 13 || i == 14)) { constexpr int t = (s - 5) + i - 13; spa[t] = wr2 & mt[t]; OPQ(spa[t]); }
          constexpr int rt = s == 13 && i >= 13 && i < 19 ? i - 13 : s == 14 && i >= 11 && i < 15 ? i - 5 : -1;
          if constexpr (rt >= 0 && (rt & 1) == 0) { const f4 v = gr[rt / 2]; dg = bself(m2, bself(m1, v[3], v[2]), bself(m1, v[1], v[0])); OPQ(dg); }
          if constexpr (rt >= 0 && (rt & 1) == 1) { lds_w<float>(mq + (rt / 2) * 128, dg); lds_w<float>(ms + (rt / 2) * 128, gr[rt / 2][3]); }
          if constexpr (s == 14 && i >= 8 && i < 11) xp[i - 8] = lds_r<u2>(xr + (i - 8) * 4096);
          if constexpr (s == 15 && (i == 13 || i == 14)) xp[i - 10] = lds_r<u2>(xr + (i - 10) * 4096);
          if constexpr (s == 15 && i == 0) { rv[0] = lds_r<f2>(rb_); rv[1] = lds_r<f2>(rb_ + 640); rv[2] = lds_r<f2>(rb_ + 1280); rv[3] = lds_r<f2>(rb_ + 1920); }
          if constexpr (s == 15 && i == 8) { sa = rv[0][0] + rv[1][0]; OPQ(sa); sb = rv[0][1] + rv[1][1]; OPQ(sb); }
          if constexpr (s == 15 && i == 9) { sa += rv[2][0]; OPQ(sa); }
          if constexpr (s == 15 && i == 10) { sb += rv[2][1]; OPQ(sb); }
          if constexpr (s == 15 && i == 11) { sa += rv[3][0]; OPQ(sa); }
          if constexpr (s == 15 && i == 12) { sb += rv[3][1]; OPQ(sb); sa *= 1.f / 512; OPQ(sa); }
          if constexpr (s == 15 && i == 13) { sb = sb * (1.f / 512) - sa * sa; OPQ(sb); }
          if constexpr (s == 15 && i == 14) { sb = rsq(fmx(sb, 0.f) + EPS); lds_w<float>(rc_, sb); lds_w<float>(rc_ + 320, sb * sa); lds_w<float>(rc_ + 640, 1.f); lds_w<float>(rc_ + 960, 0.f); }
        } else {
          if (i < 15) rowc_read(R, r1, r4, i);
          if constexpr (i < 12 && (i & 1) == 0) setm0(lw, (1 + i / 6) * 12288 + (i % 6) / 2 * 1024);
          if constexpr (i < 12 && (i & 1) == 1) { constexpr int j = (i % 6) / 2; dma_go<0>(rwo, (j & 1) ? wooffB : wooffA, oso[j]); if constexpr (i < 6) wrap_add(oso[j]); }
          if constexpr (i == 12) setm0(lx, L_RING + 3 * 12288);
          if constexpr (i == 13) dma_go<0>(rwo, wooffA, wo5);
        }
        SB();
      });
    });
    asm volatile("" ::"v"(touch), "v"(touch1), "v"(touch2), "v"(pfc));
  }
  NOPS(2);
  SB();
  f4 csq, csk;
#pragma unroll
  for (int i = 0; i < 4; ++i) { csq[i] = dppf<0x15F>(aq[0][i]); csk[i] = dppf<0x15F>(ak[0][i]); }
  float csv;
  { f2 x = swap32(av[0][3], av[0][3]); f2 y = swap16(x[1], x[1]); csv = y[1]; }
  TC(1, 17);
  TCFLUSH(1, 18);
  STAMP(2);
  u4 WO[16];

#pragma unroll 1
  for (int L = 0;; ++L) {
#define FRESH(n) int l##n = l, wv##n = wv, hg##n = hg, tile##n = tile; asm volatile("" : "+v"(l##n)); asm volatile("" : "+s"(wv##n), "+s"(hg##n), "+s"(tile##n)); \
    const int lr##n = l##n & 15, g##n = l##n >> 4; (void)lr##n; (void)g##n;
    FRESH(1)
    const unsigned ob = WS_OB + L * BUFB + tile1 * TILEB;
    u2 kb[5], vb[5], qb[5], o[5];
    u2 one2 = {ONE2, ONE2};
    float kap = 0.36067376f;
    OPQ(one2); OPQ(kap);
    const unsigned gate = WS_FA + L * 32768 + (tile1 * 8 + hg1) * 128 + wv1 * 32;
    u2 fa = {0, 0}, zs = {0, 0};
    unsigned ff;
    {
      const bool w = L == 1 && wv1 == 0;
      asm volatile("buffer_load_dword %0, %1, %2, %3 offen sc1" : "=v"(ff) : "v"(w ? lr1 * 128 : OOB), "s"(ws), "s"(WS_FF) : "memory");
      peek8_go(zs, ws, w && l1 < 3 ? l1 * 8 : OOB, WS_SEL + (b & 15) * 128);
    }
    const f4 neg = {-1e30f, -1e30f, -1e30f, -1e30f};
    auto ex = [](float x) { return __builtin_amdgcn_exp2f(x); };
    {
      f4 ss[5], sp[5], sc[5], sum[5], oa[5];
      u2 pb[5], ps[5], pp[5];
      float mx[5];
      sfor<0, 5>([&](auto T_) __attribute__((always_inline)) {
        constexpr int t = decltype(T_)::v;
        f4 fq = aq[t] * R.c[t] - csq * R.cm[t], fk = ak[t] * R.c[t] - csk * R.cm[t], fv = av[t] * R.c4[t] - R.cm4[t] * csv;
        OPQ(fq); OPQ(fk); OPQ(fv);
        SB();
        if constexpr (t > 0) ss[t - 1] = m16c(kb[t - 1], qb[t - 1], mask);
        qb[t] = pk4(fq); kb[t][0] = pk(fk[0], fk[1]);
        SB();
        if constexpr (t > 1) sp[t - 1] = m16c(kb[t - 2], qb[t - 1], mask);
        kb[t][1] = pk(fk[2], fk[3]);
        OPQ(kb[t]);
        SB();
        vb[t] = pk4(fv);
        OPQ(vb[t]);
        SB();
      });
      auto pick = [&](int t) {
        sc[t] = bsel4(pm, t ? sp[t] : neg, ss[t]);
        mx[t] = fmx(fmx(fmx(sc[t][0], sc[t][1]), sc[t][2]), sc[t][3]);
      };
      ss[4] = m16c(kb[4], qb[4], mask); SB();
      pick(0); SB();
      sp[4] = m16c(kb[3], qb[4], mask); SB();
      pick(1); pick(2); SB();
      pick(3); pick(4);
      {
        f2 x = swap16(mx[0], mx[1]), y = swap16(mx[2], mx[3]), w = swap16(mx[4], mx[4]);
        f2 u = swap32(fmx(x[0], x[1]), fmx(y[0], y[1]));
        const float w1 = fmx(w[0], w[1]), c = fmx(u[0], u[1]) * kap;
        f2 w2 = swap32(w1, w1), v = swap32(c, c);
        f2 v0 = swap16(v[0], v[0]), v1 = swap16(v[1], v[1]);
        mx[0] = v0[0]; mx[1] = v0[1]; mx[2] = v1[0]; mx[3] = v1[1]; mx[4] = fmx(w2[0], w2[1]) * kap;
      }
      if (L == 0) {
        WAITV(18);
        st8(ws, (u2){now() + T_O0, tag}, lw0, gate);
        u4 sent = {SENT, SENT, SENT, SENT};
        OPQ(sent);
#pragma unroll
        for (int i = 0; i < 3; ++i) st16(ws, sent, i < 2 ? ooff : ooff4, WS_XB + tile1 * TILEB + hg1 * 2048 + i * 32768);
      }
      SB();
      auto norm = [&](int t) { KEEP(sum[t]); o[t] = pk4(oa[t] * __builtin_amdgcn_rcpf(sum[t][0])); };
      sfor<0, 5>([&](auto T_) __attribute__((always_inline)) {
        constexpr int t = decltype(T_)::v;
        f4 d = sc[t] * kap - mx[t];
        OPQ(d);
        SB();
        if constexpr (t > 0) m16<true>(oa[t - 1], vb[t - 1], ps[t - 1]);
        float e0 = ex(d[0]), e1 = ex(d[1]);
        OPQ(e0); OPQ(e1);
        SB();
        if constexpr (t > 1) m16(oa[t - 1], vb[t - 2], pp[t - 1]);
        float e2 = ex(d[2]), e3 = ex(d[3]);
        OPQ(e2); OPQ(e3);
        SB();
        if constexpr (t > 0) m16<true>(sum[t - 1], one2, pb[t - 1]);
        pb[t] = (u2){pk(e0, e1), pk(e2, e3)};
        if constexpr (t > 0) { pp[t] = pb[t] & pm; ps[t] = pb[t] ^ pp[t]; } else ps[t] = pb[t];
        SB();
        if constexpr (t > 1) { norm(t - 2); SB(); }
        if constexpr (t == 3) peek8_go(fa, ws, L ? OOB : fao, WS_FA);
      });
      m16<true>(oa[4], vb[4], ps[4]); m16(oa[4], vb[3], pp[4]); m16<true>(sum[4], one2, pb[4]);
      SB();
      NOPS(2);
      norm(3);
      SB();
      NOPS(2);
      norm(4);
    }
    if (L == 1) {
      STAMP(10);
      if (wv1 == 0) {
        WAITV(0);
        OPQ(zs); OPQ(ff);
        if (__builtin_expect(!__all(ff == tag), 0)) wait_flags_ld(ws, lr1 * 128, WS_FF, tag);
        if (!__all(zs[1] == tag || l1 >= 3)) zs[0] = wait_word(ws, l1 < 3 ? l1 * 8 : OOB, WS_SEL + (b & 15) * 128, tag);
        lds_w<unsigned>(L_MISC + O_SEL + l1 * 4, zs[0]);
      }
      WAITL();
      BAR();
      const u4 sel = lds_r<u4>(L_MISC + O_SEL);
      const int halo1 = (tile1 != 0 && tile1 != 15);
      auto local = [&](unsigned key) { const int tl = (key >> 7) & 31, rho = key & 127; return tl == tile1 ? rho : (halo1 && tl == tile1 - 1 && rho >= 72) ? rho - 72 : -1; };
      const int row0 = local(rfl(sel[0])), row1 = local(rfl(sel[1]));
      STAMP(9);
      if (row0 < 0 && row1 < 0) st8(ws, (u2){now() + 5, tag}, lw0, gate);
#pragma unroll 1
      for (int i = 0; i < 2; ++i) {
        const int rho = i ? row1 : row0;
        if (rho < 0) continue;
        const unsigned key = rfl(i ? sel[1] : sel[0]);
        FRESH(5)
        const int ts = rho >> 4, rs_ = rho & 15;
        const unsigned ra = L_MISC + O_RA + rho * 4;
        float sg = sigm(key_score(key));
        const float a1 = lds_r<float>(ra), b1 = lds_r<float>(ra + 320);
        const int col = 128 * wv5 + 4 * (l5 & 31);
        unsigned rb = L_RING + i * 1024, zb = rb + g5 * 16, zr = rb + l16, zw = rb + col * 2, zxa = rb + (64 * hg5 + 16 * wv5 + 4 * g5) * 2, yo = (l5 >> 5) * 32768 + col * 4;
        OPQ(zb); OPQ(zr); OPQ(zw); OPQ(zxa); OPQ(yo); OPQ(sg);
        const u2 xr = __builtin_amdgcn_raw_buffer_load_b64(ws, (col >> 5) * 1024 + ((col >> 3) & 3) * 256 + (col & 4) * 2, WS_XB + ((key >> 7) & 31) * TILEB + ((key >> 4) & 7) * 16384 + (key & 15) * 16, 16);
        f4 x0 = up4(xr) * a1 + b1;
        const unsigned me = lmask(lr5 == rs_), half = (rs_ & 1) ? 0xFFFF0000u : 0xFFFFu;
        const unsigned mv0 = lmask(2 * g5 == (rs_ >> 1)) & half, mv1 = lmask(2 * g5 + 1 == (rs_ >> 1)) & half;
        u2 Ka, Kb, Kc, Qb, Qc, Va, Vb, Vc, Xb;
        f4 mk0 = mask;
        sfor<0, 5>([&](auto T_) __attribute__((always_inline)) {
          constexpr int t = decltype(T_)::v, tp = t > 0 ? t - 1 : 0, tn = t < 4 ? t + 1 : 4;
          if (ts == t) { CFENCE(); Ka = kb[tp]; Kb = kb[t]; Kc = kb[tn]; Qb = qb[t]; Qc = qb[tn]; Va = vb[tp]; Vb = vb[t]; Vc = vb[tn]; Xb = xp[t]; if (t == 0) mk0 = neg; }
        });
        OPQ(x0); OPQ(Ka); OPQ(Kb); OPQ(Kc); OPQ(Qb); OPQ(Qc); OPQ(Va); OPQ(Vb); OPQ(Vc); OPQ(Xb); OPQ(mk0);
#pragma unroll 1
        for (int sp = 0; sp < 256 && (int)(rfl(sel[2]) + LAG_Y - now()) > 0; ++sp) {}
        u4 y[8];
#pragma unroll 1
        for (int sp = 0; sp < SPIN_CAP; ++sp) {
          CFENCE();
          unsigned m = 0;
          CNT(10);
#pragma unroll
          for (int j = 0; j < 8; ++j) { y[j] = ld16(ws, yo, WS_YP + j * 4096 + i * 2048); m = max(m, y[j][0]); }
          if (!__any(m == SENT)) break;
          __builtin_amdgcn_s_sleep(1);
        }
        STAMP(27);
        if (i == 1 || row1 < 0) st8(ws, (u2){now() + T_O1, tag}, lw0, gate);
        {
          const f4 t = ((asf(y[0]) + asf(y[1])) + (asf(y[2]) + asf(y[3]))) + ((asf(y[4]) + asf(y[5])) + (asf(y[6]) + asf(y[7])));
          const f2 p = swap32(t[0], t[1]), q = swap32(t[2], t[3]);
          const float e = p[0] + p[1], f = q[0] + q[1];
          const f2 ee = swap32(e, e), ff_ = swap32(f, f);
          lds_w<u2>(zw, pk4((f4){ee[0], ee[1], ff_[0], ff_[1]} * sg + x0));
        }
        WAITL();
        BAR();
        const u4 zz = lds_r<u4>(zr);
        const u2 z0 = {zz[0], zz[1]}, z1 = {zz[2], zz[3]};
        f4 tq, tk, tv;
        u4 z[16];
#pragma unroll
        for (int s = 0; s < 4; ++s) z[s] = lds_r<u4>(zb + s * 64);
        const u2 zx = lds_r<u2>(zxa);
        float sm, sq;
        SB();
        sfor<0, 16>([&](auto S_) __attribute__((always_inline)) {
          constexpr int s = decltype(S_)::v;
          mWA<s == 0>(tq, W[s][0], z[s]);
          if constexpr (s + 4 < 16) z[s + 4] = lds_r<u4>(zb + (s + 4) * 64);
          SB();
          mWA<s == 0>(tk, W[s][1], z[s]);
          if (s == 0) { sq = dot2(z0[1], z0[1], dot2(z0[0], z0[0], 0.f)); OPQ(sq); }
          if (s == 1) { sm = dot2(z0[1], one2[0], dot2(z0[0], one2[0], 0.f)); OPQ(sm); }
          if (s == 2) sq = dot2(z1[1], z1[1], dot2(z1[0], z1[0], sq));
          if (s == 3) sm = dot2(z1[1], one2[0], dot2(z1[0], one2[0], sm));
          if (s == 4) { sm += dppf<0xB1>(sm); sq += dppf<0xB1>(sq); } if (s == 5) { sm += dppf<0x4E>(sm); sq += dppf<0x4E>(sq); }
          if (s == 6) { sm += dppf<0x141>(sm); sq += dppf<0x141>(sq); } if (s == 7) { sm += dppf<0x140>(sm); sq += dppf<0x140>(sq); }
          if (s == 8) sm = xadd16(sm); if (s == 9) sq = xadd16(sq); if (s == 10) sm = xadd32(sm) * (1.f / 512); if (s == 11) sq = xadd32(sq) * (1.f / 512);
          if (s == 12) sq = fmx(sq - sm * sm, 0.f) + EPS; if (s == 13) sq = rsq(sq); if (s == 14) sm *= sq;
          if (s == 15) { lds_w<float>(ra, 1.f); lds_w<float>(ra + 320, 0.f); }
          if (s >= 2 && s < 15) { OPQ(sm); OPQ(sq); }
          SB();
          mAW<s == 0>(tv, z[s], W[s][2]);
          SB();
        });
        Xb = (u2){bsel(me, zx[0], Xb[0]), bsel(me, zx[1], Xb[1])};
        f4 cq = csq * -sm, ck = csk * -sm;
        float cv = csv * -sm;
        OPQ(Xb); OPQ(cq); OPQ(ck); OPQ(cv);
        SB();
        if (i == 1 || row1 < 0) peek8_go(fa, ws, fao, WS_FA + 32768);
        const u2 nq = pk4(tq * sq + cq), nk = pk4(tk * sq + ck);
        KEEP(tv);
        const float nv = tv[0] * sq + cv;
        const unsigned nvp = pk(nv, nv);
        Qb = (u2){bsel(me, nq[0], Qb[0]), bsel(me, nq[1], Qb[1])}; Kb = (u2){bsel(me, nk[0], Kb[0]), bsel(me, nk[1], Kb[1])};
        OPQ(Qb); OPQ(Kb);
        SB();
        Vb = (u2){bsel(mv0, nvp, Vb[0]), bsel(mv1, nvp, Vb[1])};
        OPQ(Vb);
        SB();
        f4 sb = m16c(Kb, Qb, mask), pb_ = m16c(Ka, Qb, mk0), sc_ = m16c(Kc, Qc, mask), pc_ = m16c(Kb, Qc, mask);
        SB();
        NOPS(6);
        SB();
        sb = bsel4(pm, pb_, sb);
        float mb = fmx(fmx(fmx(sb[0], sb[1]), sb[2]), sb[3]);
        OPQ(mb);
        SB();
        sc_ = bsel4(pm, pc_, sc_);
        float mc = fmx(fmx(fmx(sc_[0], sc_[1]), sc_[2]), sc_[3]);
        { f2 x = swap16(mb, mc); const float y_ = fmx(x[0], x[1]); f2 w = swap32(y_, y_); const float c = fmx(w[0], w[1]) * kap; f2 v = swap16(c, c); mb = v[0]; mc = v[1]; }
        const f4 db = sb * kap - mb, dc = sc_ * kap - mc;
        u2 Pb = {pk(ex(db[0]), ex(db[1])), pk(ex(db[2]), ex(db[3]))}, Pc = {pk(ex(dc[0]), ex(dc[1])), pk(ex(dc[2]), ex(dc[3]))};
        u2 Pbp = Pb & pm, Pbs = Pb ^ Pbp, Pcp = Pc & pm, Pcs = Pc ^ Pcp;
        OPQ(Pbp); OPQ(Pbs); OPQ(Pcp); OPQ(Pcs);
        f4 ob_, oc_, lb, lc;
        SB();
        m16<true>(lb, one2, Pb); m16<true>(lc, one2, Pc); m16<true>(ob_, Vb, Pbs); m16(ob_, Va, Pbp); m16<true>(oc_, Vc, Pcs); m16(oc_, Vb, Pcp);
        SB();
        NOPS(4);
        SB();
        KEEP(lb); KEEP(lc);
        float ib = __builtin_amdgcn_rcpf(lb[0]), ic = __builtin_amdgcn_rcpf(lc[0]);
        OPQ(ib); OPQ(ic);
        SB();
        NOPS(1);
        u2 Ob = pk4(ob_ * ib);
        OPQ(Ob);
        SB();
        const u2 Oc = pk4(oc_ * ic);
        sfor<0, 5>([&](auto T_) __attribute__((always_inline)) {
          constexpr int t = decltype(T_)::v;
          if (ts == t) { CFENCE(); kb[t] = Kb; qb[t] = Qb; vb[t] = Vb; xp[t] = Xb; o[t] = Ob; if constexpr (t < 4) o[t + 1] = Oc; }
        });
      }
      STAMP(25);
    }
#pragma unroll
    for (int i = 0; i < 3; ++i) {
      u2 A = o[2 * i], B = o[i < 2 ? 2 * i + 1 : 4];
      auto s0 = __builtin_amdgcn_permlane16_swap(A[0], B[0], false, false); auto s1 = __builtin_amdgcn_permlane16_swap(A[1], B[1], false, false);
      const u4 e = {umin(s0[0], SENT - 1), s1[0], s0[1], s1[1]};
      st16(ws, e, i < 2 ? ooff : ooff4, ob + hg1 * 2048 + i * 32768);
      lds_w<u4>(i < 2 ? ooff + i * 32768 : lo2, e);
    }
    STAMP(3 + 8 * L);
    FRESH(2)
    unsigned l16h = l16 + 65536, r1 = L_MISC + O_RA + lr2 * 4;
    OPQ(l16h); OPQ(r1);
    f4 acc[5];
    {
      float ra[5], rb[5];
#pragma unroll
      for (int t = 0; t < 5; ++t) { ra[t] = lds_r<float>(r1 + t * 64); rb[t] = lds_r<float>(r1 + 320 + t * 64); }
      SB();
#pragma unroll
      for (int t = 0; t < 5; ++t) acc[t] = up4(xp[t]);
      SB();
#pragma unroll
      for (int t = 0; t < 5; ++t) { acc[t] = acc[t] * ra[t] + rb[t]; OPQ(acc[t]); }
    }
    unsigned rw_ = L_RED2 + wv2 * 2048 + l16, rr_ = L_RED2 + row3 * 16;
    unsigned lw2 = wv2 * 1024, lw4 = (wv2 & 1) * 1024, hub = WS_HU + L * 32768 + (tile2 * 8 + hg2) * 128, stb = WS_ST + L * STB + (tile2 * 8 + hg2) * 1280;
    OPQ(rw_); OPQ(rr_);
    asm volatile("" : "+s"(lw2), "+s"(lw4), "+s"(hub), "+s"(stb));
    WAITC(3, 0);
    OPQ(fa);
    asm volatile("" ::"v"(zs), "v"(ff));
    if (wv2 == 0) wait_gate(ws, fa, fao, WS_FA + L * 32768, tag, LAG_O);
    BAR();
    STAMP(23);
    u4 a[3][5];
    auto rda = [&](int j, int t) { a[j % 3][t] = lds_r<u4>((t < 4 ? l16 + t * 16384 : l16h) + j * 1024); };
    auto rdwo = [&](int j) { WO[j] = trfrag2(trb + ((4 + j / 3) % 6) * 12288 + (j % 3) * 4096, 512); asm volatile("" : "+a"(WO[j])); };
    if (L == 0) rdwo(0);
#pragma unroll
    for (int t = 0; t < 5; ++t) rda(0, t);
#pragma unroll
    for (int t = 0; t < 5; ++t) rda(1, t);
    STAMP(24);
    SB();
#pragma unroll
    for (int t = 0; t < 5; ++t) { setm0(lw2, 2048 + t * 16384); SB(); mWA(acc[t], WO[0], a[0][t]); SB(); dma_go<1>(ws, l16, so_[0] + t * 16384); }
#pragma unroll
    for (int t = 0; t < 5; ++t) dma<1>(ws, l16, so_[1] + t * 16384, lw2, 6 * 1024 + t * 16384);
    if (L == 0) {
#pragma unroll
      for (int j = 1; j < 16; ++j) rdwo(j);
    }
    STAMP(4 + 8 * L);
    auto refetch = [&](u4& c0, u4& c1, u4& c2, u4& c3, u4& c4, unsigned v0, unsigned v1, unsigned so, int ci) {
      CNT(ci);
#pragma unroll 1
      for (int sp = 0; sp < SPIN_CAP; ++sp) {
        CNT(ci + 1);
        CFENCE();
        c0 = ld16(ws, v0, so); c1 = ld16(ws, v1, so + 16384); c2 = ld16(ws, v1, so + 32768); c3 = ld16(ws, v1, so + 49152); c4 = ld16(ws, v1, so + 65536);
        if (!__any(max(max(max(c0[0], c1[0]), c2[0]), max(c3[0], c4[0])) == SENT)) break;
        __builtin_amdgcn_s_sleep(6);
      }
      NOPS(2);
    };
    WAITV(5);
    BAR();
#pragma unroll
    for (int t = 0; t < 5; ++t) rda(2, t);
    SB();
    sfor<1, 16>([&](auto J_) __attribute__((always_inline)) {
      constexpr int j = decltype(J_)::v, n = (j + 1) % 3;
      u64 th;
      if (j == 8) th = time_go();
      if (j == 4) WAITV(5); else if (j == 8) WAITV(3); else if (j == 12) WAITV(1);
      if (j == 4 || j == 8 || j == 12) BAR();
      if (j == 8) st8(ws, (u2){time_get(th) + T_ST, tag}, hg0, hub);
      SB();
      unsigned m;
      sfor<0, 5>([&](auto T_) __attribute__((always_inline)) {
        constexpr int t = decltype(T_)::v, k = j < 4 ? 2 * (j - 1) + (t >> 1) : 5 + (j - 4);
        constexpr bool dm = j < 4 ? (t < 4 && k < 5) : (j < 7 && t < 2);
        mWA(acc[t], WO[j], a[j % 3][t]);
        if constexpr (j + 2 < 16) rda(j + 2, t);
        if constexpr (dm && (t & 1) == 0) { if constexpr (k < 5) setm0(lw2, 10 * 1024 + k * 16384); else setm0(lw4 + t4[k - 5], 14 * 1024); }
        if constexpr (dm && (t & 1) == 1) { if constexpr (k < 5) dma_go<1>(ws, l16, so_[2] + k * 16384); else dma_go<1>(ws, l16, so_[3] + t4[k - 5]); }
        if constexpr (j + 1 < 16) {
          if constexpr (t == 1) WAITC(63, j + 2 < 16 ? 2 : 0);
          if constexpr (t == 2) { m = max(max(a[n][0][0], a[n][1][0]), a[n][2][0]); OPQ(m); }
          if constexpr (t == 3) { m = max(max(m, a[n][3][0]), a[n][4][0]); OPQ(m); }
        }
        SB();
      });
      if constexpr (j + 1 < 16)
        if (__builtin_expect(__any(m == SENT), 0)) refetch(a[n][0], a[n][1], a[n][2], a[n][3], a[n][4], l16, l16, ob + ((2 * hg2 + j + 1) & 15) * 1024, 2 * L);
      SB();
    });
    STAMP(5 + 8 * L);
    {
      float q2[5];
      f4 da, ea;
      NOPS(4);
      SB();
      sfor<0, 5>([&](auto T_) __attribute__((always_inline)) {
        constexpr int t = decltype(T_)::v;
        xp[t] = pk4(acc[t]);
        if constexpr (t > 0) { q2[t - 1] = dot2(xp[t - 1][1], xp[t - 1][1], dot2(xp[t - 1][0], xp[t - 1][0], 0.f)); OPQ(q2[t - 1]); }
        OPQ(xp[t]);
        SB();
        if constexpr (t > 0) m16<t == 1>(da, spa[t - 1], xp[t - 1]);
        SB();
      });
      q2[4] = dot2(xp[4][1], xp[4][1], dot2(xp[4][0], xp[4][0], 0.f));
      OPQ(q2[4]);
      SB();
      m16<true>(ea, spa[0], xp[4]);
      SB();
      const f2 x = swap16(q2[0], q2[1]), y = swap16(q2[2], q2[3]), u = swap32(x[0] + x[1], y[0] + y[1]);
      float qa = u[0] + u[1], qe = xadd32(xadd16(q2[4]));
      OPQ(qa); OPQ(qe);
      SB();
      KEEP(da); KEEP(ea);
      lds_w<f4>(rw_, (f4){da[0], da[1], qa, da[3]});
      lds_w<f4>(rw_ + 1024, (f4){ea[0], ea[1], qe, ea[3]});
    }
    WAITL();
    BAR();
    {
      u4 w = asu((lds_r<f4>(rr_) + lds_r<f4>(rr_ + 2048)) + (lds_r<f4>(rr_ + 4096) + lds_r<f4>(rr_ + 6144)));
      w[3] = tag;
      st16(ws, w, sv_, stb);
    }
    STAMP(6 + 8 * L);
    FRESH(3)
    const unsigned xb = WS_XB + L * BUFB + tile3 * TILEB;
    const int par3 = tile3 >= 15, ti3 = tile3 - 15 * par3, p03 = 68 * ti3;
    const bool halo = L == 0 && ti3 > 0;
    const unsigned fo = wv3 == 0 && l3 < (halo ? 16 : 8) ? ((tile3 - (l3 >> 3)) * 8 + (l3 & 7)) * 128 : OOB;
    u2 hu = {0, 0};
    peek8_go(hu, ws, fo, WS_XG + L * 32768);
#pragma unroll
    for (int i = 0; i < 3; ++i) {
      u2 A = xp[2 * i], B = xp[i < 2 ? 2 * i + 1 : 4];
      auto s0 = __builtin_amdgcn_permlane16_swap(A[0], B[0], false, false); auto s1 = __builtin_amdgcn_permlane16_swap(A[1], B[1], false, false);
      st16(ws, (u4){umin(s0[0], SENT - 1), s1[0], s0[1], s1[1]}, i < 2 ? ooff : ooff4, xb + hg3 * 2048 + i * 32768);
    }
    unsigned so = (halo && row3 < 7) ? ((tile3 - 1) * 640 + 72 + row3) * 16 : (tile3 * 640 + row3) * 16;
    unsigned zg1 = l16 + 16384, zg0 = zg1 - ((halo && lr3 < 7) ? 16256 : 0);
    unsigned rc_ = L_MISC + O_RC + row3 * 4, q1 = L_MISC + O_RC + lr3 * 4, q4 = L_MISC + O_RC + g3 * 16;
    OPQ(so); OPQ(zg0); OPQ(zg1); OPQ(rc_); OPQ(q1); OPQ(q4);
    u4 v[8];
    auto stats_ok = [&]() { return __all(((v[0][3] & v[1][3] & v[2][3] & v[3][3] & v[4][3] & v[5][3] & v[6][3] & v[7][3]) == tag) && ((v[0][3] | v[1][3] | v[2][3] | v[3][3] | v[4][3] | v[5][3] | v[6][3] | v[7][3]) == tag)); };
    auto stats_wait = [&]() {
#pragma unroll 1
      for (int sp = 0; sp < SPIN_CAP; ++sp) {
        CFENCE();
#pragma unroll
        for (int i = 0; i < 8; ++i) v[i] = ld16(ws, so, WS_ST + L * STB + i * 1280);
        if (stats_ok()) break;
        __builtin_amdgcn_s_sleep(1);
      }
    };
    auto rowstats = [&]() {
      const f4 st = ((asf(v[0]) + asf(v[1])) + (asf(v[2]) + asf(v[3]))) + ((asf(v[4]) + asf(v[5])) + (asf(v[6]) + asf(v[7])));
      const float m = st[0] * (1.f / 512), var = fmx(st[2] * (1.f / 512) - m * m, 0.f), rs = rsq(var + EPS), a1 = 1.f + rs;
      const float c = a1 * rsq(var * a1 * a1 + EPS);
      lds_w<float>(rc_, c); lds_w<float>(rc_ + 320, c * m); lds_w<float>(rc_ + 640, a1); lds_w<float>(rc_ + 960, -m * rs);
    };
    WAITV(3);
    OPQ(hu);
    if (wv3 == 0) wait_gate(ws, hu, fo, WS_XG + L * 32768, tag, LAG_X);
    BAR();
    if (L == 1) {
      const rsrc_t ro = mk(P.out, 2048 * 1024);
      stats_wait();
      STAMP(15);
      rowstats();
      FRESH(4)
      WAITL();
      BAR();
#pragma unroll
      for (int t = 0; t < 5; ++t) {
        const float ra = lds_r<float>(q1 + 640 + t * 64), rb = lds_r<float>(q1 + 960 + t * 64);
        lds_w<u2>((16 * t + lr4) * 144 + wv4 * 32 + g4 * 8, pk4(up4(xp[t]) * ra + rb));
      }
      WAITL();
      BAR();
#pragma unroll
      for (int i = 0; i < 3; ++i) {
        const int row = min((i * 4 + wv4) * 8 + (l4 >> 3), 79), u = row - (row >> 4), p = p03 + u;
        const bool ok = (i * 4 + wv4) < 10 && (row & 15) != 15 && (ti3 == 0 || u >= 7);
        st16(ro, lds_r<u4>(row * 144 + (l4 & 7) * 16), ok ? (2 * p + par3) * 1024 + hg4 * 128 + (l4 & 7) * 16 : OOB, 0);
      }
      STAMP(16);
      WAITV(0);
      BAR();
      if (wv4 < 2) {
        const unsigned zz = wait_word(ws, l4 < 2 ? (l4 ? 16 : wv4 * 8) : OOB, WS_SEL + 2048 + (b & 15) * 128, tag);
        const unsigned key = rdl(zz, 0), ty = rdl(zz, 1);
        STAMP(17);
        if (((key >> 7) & 31) == tile4) {
          const int rho = key & 127;
          f4 base = up4(lds_r<u2>(rho * 144 + lr4 * 8));
          float sg = sigm(key_score(key));
          unsigned oo = l4 < 16 ? (2 * (p03 + rho - (rho >> 4)) + par3) * 1024 + hg4 * 128 + lr4 * 8 : OOB;
          OPQ(base); OPQ(sg); OPQ(oo);
#pragma unroll 1
          for (int sp = 0; sp < 256 && (int)(ty + LAG_Y - now()) > 0; ++sp) {}
          u4 y[4];
#pragma unroll 1
          for (int sp = 0; sp < SPIN_CAP; ++sp) {
            CFENCE();
            unsigned m = 0;
            CNT(11);
#pragma unroll
            for (int j = 0; j < 4; ++j) { y[j] = ld16(ws, g4 * 4096 + lr4 * 16, WS_YP + 65536 + j * 16384 + wv4 * 2048 + hg4 * 256); m = max(m, y[j][0]); }
            if (!__any(m == SENT)) break;
            __builtin_amdgcn_s_sleep(1);
          }
          const f4 s = (asf(y[0]) + asf(y[1])) + (asf(y[2]) + asf(y[3]));
          const f2 x = swap16(s[0], s[1]), w = swap16(s[2], s[3]), vv = swap32(x[0] + x[1], w[0] + w[1]);
          const float e = vv[0] + vv[1];
          const f2 v2 = swap32(e, e), e01 = swap16(v2[0], v2[0]), e23 = swap16(v2[1], v2[1]);
          st8(ro, pk4((f4){e01[0], e01[1], e23[0], e23[1]} * sg + base), oo, 0);
        }
        STAMP(18);
      }
#pragma unroll
      for (int t = 0; t < 5; ++t) asm volatile("" : "=v"(aq[t]), "=v"(ak[t]), "=v"(av[t]), "=v"(R.c[t]), "=v"(R.cm[t]), "=v"(R.c4[t]), "=v"(R.cm4[t]));
      break;
    }
    {
      const unsigned xs = xb - 16384 + wv3 * 1024, lw3 = wv3 * 1024;
      u4 sent = {SENT, SENT, SENT, SENT};
      OPQ(sent);
      STAMP(7);
#pragma unroll
      for (int i = 0; i < 2; ++i)
#pragma unroll
        for (int t = 0; t < 5; ++t) dma<1>(ws, t ? zg1 : zg0, xs + t * 16384 + i * 4096, lw3, t * 16384 + i * 4096);
#pragma unroll
      for (int k = 0; k < 8; ++k) ld16_go(v[k], ws, so, WS_ST + k * 1280);
      WAITV(13);
      BAR();
      STAMP(26);
      u4 z[2][5];
      unsigned tg;
      float s1, s2, s3, s4;
#pragma unroll
      for (int t = 0; t < 5; ++t) z[0][t] = lds_r<u4>(t < 4 ? l16 + t * 16384 : l16h);
      if (__builtin_expect(__any(max(max(max(z[0][0][0], z[0][1][0]), z[0][2][0]), max(z[0][3][0], z[0][4][0])) == SENT), 0)) refetch(z[0][0], z[0][1], z[0][2], z[0][3], z[0][4], zg0, zg1, xb - 16384, 4);
      SB();
      sfor<0, 16>([&](auto S_) __attribute__((always_inline)) {
        constexpr int s = decltype(S_)::v, c = s & 1, n = c ^ 1;
        if (s == 3) { WAITV(14); BAR(); } else if (s == 7) { WAITV(5); BAR(); } else if (s == 11) { WAITC(0, 0); BAR(); }
        if (s == 8) {
          asm volatile("" : "+v"(v[0]), "+v"(v[1]), "+v"(v[2]), "+v"(v[3]), "+v"(v[4]), "+v"(v[5]), "+v"(v[6]), "+v"(v[7]));
          if (__builtin_expect(__any(tg != 0), 0)) { CNT(9); stats_wait(); }
        }
        SB();
        unsigned m;
        bool bad;
        sfor<0, 15>([&](auto I_) __attribute__((always_inline)) {
          constexpr int i = decltype(I_)::v, t = i / 3, k = 2 * s + (i > 6);
          if constexpr (i % 3 == 0) mWA<s == 0>(aq[t], W[s][0], z[c][t]); else if constexpr (i % 3 == 1) mWA<s == 0>(ak[t], W[s][1], z[c][t]); else mAW<s == 0>(av[t], z[c][t], W[s][2]);
          if constexpr (s == 15) rowc_read(R, q1, q4, i);
          else if constexpr (i < 5) z[n][i] = lds_r<u4>((i < 4 ? l16 + i * 16384 : l16h) + (s + 1) * 1024);
          if constexpr (s < 5 && (i == 5 || i == 9)) setm0(lw3, (k % 5) * 16384 + (2 + k / 5) * 4096);
          if constexpr (s < 5 && (i == 6 || i == 10)) dma_go<1>(ws, (k % 5) ? zg1 : zg0, xs + (k % 5) * 16384 + (2 + k / 5) * 4096);
          if constexpr (s < 15 && i == 11) { WAITC(63, 0); m = max(max(z[n][0][0], z[n][1][0]), z[n][2][0]); OPQ(m); }
          if constexpr (s < 15 && i == 12) { m = max(max(m, z[n][3][0]), z[n][4][0]); OPQ(m); }
          if constexpr (s < 15 && i == 13) bad = __any(m == SENT);
          if constexpr (s == 7 && i >= 5 && i < 9) { constexpr int e = 2 * (i - 5); OPQ(v[e]); OPQ(v[e + 1]); const unsigned w = (v[e][3] ^ tag) | (v[e + 1][3] ^ tag); tg = e ? tg | w : w; OPQ(tg); }
          if constexpr (s == 8 && i == 4) { s1 = asf(v[0])[0]; s2 = asf(v[0])[2]; }
          if constexpr (s == 8 && i >= 5 && i < 14 && i != 11 && i != 12) { constexpr int e = i < 11 ? i - 4 : 7; s1 += asf(v[e])[0]; OPQ(s1); s2 += asf(v[e])[2]; OPQ(s2); }
          if constexpr (s == 9 && i == 5) { s1 *= 1.f / 512; OPQ(s1); s3 = s1 * s1; OPQ(s3); }
          if constexpr (s == 9 && i == 6) { s2 = fmx(s2 * (1.f / 512) - s3, 0.f); OPQ(s2); }
          if constexpr (s == 9 && i == 7) { s3 = rsq(s2 + EPS); OPQ(s3); }
          if constexpr (s == 9 && i == 8) { s4 = 1.f + s3; OPQ(s4); s2 *= s4; OPQ(s2); }
          if constexpr (s == 9 && i == 9) { s2 = rsq(s2 * s4 + EPS); OPQ(s2); }
          if constexpr (s == 9 && i == 10) { s2 *= s4; OPQ(s2); s3 *= -s1; OPQ(s3); }
          if constexpr (s == 10 && i == 5) { lds_w<float>(rc_, s2); lds_w<float>(rc_ + 320, s2 * s1); }
          if constexpr (s == 10 && i == 6) { lds_w<float>(rc_ + 640, s4); lds_w<float>(rc_ + 960, s3); }
          if constexpr ((s == CL_STEP || s == CL_STEP + 1) && (i == 5 || i == 7 || i == 9))
            st16c(ws, sent, i < 9 ? ooff : ooff4, (s == CL_STEP ? WS_OB : WS_XB) + BUFB + tile3 * TILEB + hg3 * 2048 + (i - 5) / 2 * 32768);
          SB();
        });
        if constexpr (s < 15)
          if (__builtin_expect(bad, 0)) refetch(z[n][0], z[n][1], z[n][2], z[n][3], z[n][4], zg0, zg1, xb - 16384 + (s + 1) * 1024, 4);
        SB();
      });
      STAMP(8);
#pragma unroll
      for (int i = 0; i < 4; ++i) so_[i] += BUFB;
      WAITV(0);
      BAR();
    }
  }
  STAMP_FLUSH();
}

DEV void role_f(const int j, unsigned g_tent) {
  const Params P = args_wait(args_go());
  const int wv = __builtin_amdgcn_readfirstlane(threadIdx.x >> 6), l = threadIdx.x & 63;
  const int lr = l & 15, g = l >> 4, tid = wv * 64 + l;
  const rsrc_t ws = mk(P.ws, WS_USED);
  const unsigned epoch = tag_issue(P.ws);
  STAMP_INIT();
  STAMP(0);
  const int noct = j < 11 ? 12 : 8, f0 = 32 * (j < 11 ? 3 * j : 33 + 2 * (j - 11));
  u4 G[16][3], Dn[3][8];
  const unsigned mg1 = lmask(g == 1), mg2 = lmask(g >= 2);
  auto gather = [&](const f4& a0, const f4& a1, const f4& a2, float& gv, float& uv) {
    KEEP(a0); KEEP(a1); KEEP(a2);
    const float d0 = bself(mg2, a2[0], bself(mg1, a1[0], a0[0])), d1 = bself(mg2, a2[1], bself(mg1, a1[1], a0[1]));
    gv = __uint_as_float(__builtin_amdgcn_update_dpp(__float_as_uint(d0), __float_as_uint(d1), 0x128, 0xF, 0xC, false));
    uv = __uint_as_float(__builtin_amdgcn_update_dpp(__float_as_uint(d1), __float_as_uint(d0), 0x128, 0xF, 0x3, false));
  };
  float csg, csu, wsum;
  unsigned tag;
  {
    const rsrc_t rgu = mk(wv < 2 ? P.wg : P.wu, 512 * 2752), rd = mk(P.wd, 1376 * 1024);
    unsigned go[6];
#pragma unroll
    for (int i = 0; i < 6; ++i) { const int n = 64 * ((wv & 1) * 6 + i) + l, row = n / 12; go[i] = row * 2752 + f0 * 2 + (n - 12 * row) * 16; }
    const unsigned dof = (f0 + g) * 1024 + wv * 256 + lr * 16;
    auto issue = [&](int rd_) {
      if (rd_ < 8) {
#pragma unroll
        for (int i = 0; i < 6; ++i) dma<0>(rgu, go[i], rd_ * 64 * 2752, wv * 6144, (rd_ & 3) * 32768 + i * 1024);
      } else {
#pragma unroll
        for (int i = 0; i < 8; ++i) dma<0>(rd, dof, (32 * (rd_ - 8) + 4 * i) * 1024, wv * 8192, (rd_ & 3) * 32768 + i * 1024);
      }
    };
    unsigned tg = ((l >> 1) & 1) * 12288 + (8 * g + (lr >> 2)) * 192 + wv * 48 + (l & 1) * 8, td = wv * 8192 + (8 * g + (lr >> 2)) * 256 + (l & 3) * 8;
    u4 w8;
    const u4 sent = {SENT, SENT, SENT, SENT};
    asm volatile("global_load_dwordx4 %0, %1, %2" : "=v"(w8) : "v"(l * 16), "s"(P.wr) : "memory");
    st16(ws, sent, j * 4096 + tid * 16, WS_YP); st16(ws, sent, j * 4096 + tid * 16, WS_YP + 65536);
    STAMP(17);
    issue(0); issue(1); issue(2);
    tag = tag_finish(epoch);
    u4 one = {ONE2, ONE2, ONE2, ONE2};
    OPQ(one);
    f4 a[3];
#pragma unroll
    for (int rd_ = 0; rd_ < 11; ++rd_) {
      if (rd_ == 1 || rd_ == 2) WAITC(13, 0); else if (rd_ < 6) WAITC(12, 0); else if (rd_ == 6) WAITC(14, 0); else if (rd_ < 9) WAITC(16, 0); else if (rd_ == 9) WAITC(8, 0); else WAITC(0, 0);
      BAR();
      if (rd_ == 0) STAMP(19); if (rd_ == 4) STAMP(21); if (rd_ == 8) STAMP(23); if (rd_ == 10) STAMP(24);
      if (rd_ == 0) {
        st4(ws, tag, tid == 0 ? 0 : OOB, WS_FF + j * 128);
        OPQ(w8);
        const f4 t = up4((u2){w8[0], w8[1]}) + up4((u2){w8[2], w8[3]});
        wsum = wave_sum((t[0] + t[1]) + (t[2] + t[3]));
      }
      if (rd_ + 3 < 11) issue(rd_ + 3);
      unsigned bg = tg + (rd_ & 3) * 32768, bd = td + (rd_ & 3) * 32768;
      OPQ(bg); OPQ(bd);
      if (rd_ < 8) {
#pragma unroll
        for (int ks = 0; ks < 2; ++ks)
#pragma unroll
          for (int c = 0; c < 3; ++c) G[2 * rd_ + ks][c] = trfrag2(bg + ks * 32 * 192 + c * 16, 4 * 192);
#pragma unroll
        for (int ks = 0; ks < 2; ++ks)
#pragma unroll
          for (int c = 0; c < 3; ++c) { if (rd_ + ks == 0) mAW<true>(a[c], one, G[0][c]); else mAW(a[c], one, G[2 * rd_ + ks][c]); }
      } else {
#pragma unroll
        for (int c = 0; c < 8; ++c) Dn[rd_ - 8][c] = trfrag2(bd + c * 32, 4 * 256);
        SB();
        if (rd_ == 8) gather(a[0], a[1], a[2], csg, csu);
      }
    }
    WAITL();
    BAR();
  }
  constexpr unsigned F_ACT = 0, F_KEY = 1024;
  const unsigned mo1 = lmask(l & 1), mo8 = lmask(l & 8), t2o = tid < 3 ? l * 8 : OOB;
  const bool live = g < 3 && 3 * wv + g < noct;
  const unsigned actw = F_ACT + (g < 3 ? (lr >> 3) * 192 + (24 * wv + 8 * g + (l & 7)) * 2 : 512 + l * 2);
  const int tok = 32 * (4 * j + wv) + (l & 31);
  unsigned sto, huo, loc;
  { const int p = tok >> 1, ti = max(p - 7, 0) / 68, u = p - 68 * ti, tl = (tok & 1) * 15 + ti, rho = u + u / 15; loc = tl * 128 + rho; sto = ((tl * 8 + 4 * (l >> 5)) * 80 + rho) * 16; }
  huo = l < 60 ? (wv * 60 + l) * 128 : OOB;
  unsigned cw = l < 32 ? (l >> 1) * 1024 + (4 * j + wv) * 16 + (l & 1) * 8 : OOB, co = j * 1024 + l * 16, keyw = F_KEY + (l == 0 ? wv * 4 : 64 + l * 4);
  unsigned m2 = lmask(l & 2), yo = lr < 4 ? j * 4096 + (l & 1) * 2048 + wv * 512 + (l & 2) * 32 + g * 16 : OOB;
  OPQ(sto); OPQ(huo); OPQ(loc); OPQ(cw); OPQ(co); OPQ(keyw); OPQ(yo);
  STAMP(1);

#pragma unroll 1
  for (int L = 0; L < 2; ++L) {
    {
      const unsigned t0 = now();
      unsigned t = t0;
      bool need = l < 60;
#pragma unroll 1
      for (int sp = 0; sp < SPIN_CAP && __any(need); ++sp) {
        const u2 z = peek8(ws, need ? huo : OOB, WS_HU + L * 32768);
        t = need ? z[0] : t;
        need = need && z[1] != tag;
      }
      lds_w<unsigned>(keyw, max_x32(max_x16(max_row16(t - t0 + 0x40000000u))) + t0);
      WAITL();
      BAR();
      const u4 q = lds_r<u4>(F_KEY) - t0;
      const unsigned tt = rfl(max(max(q[0], q[1]), max(q[2], q[3]))) + t0 - 0x40000000u + LAG_ST;
#pragma unroll 1
      for (int sp = 0; sp < 128 && (int)(tt - now()) > 0; ++sp) {}
    }
    STAMP(7 + 8 * L);
    u4 v[4];
#pragma unroll 1
    for (int sp = 0; sp < SPIN_CAP; ++sp) {
      CFENCE();
      CNT(12 + L);
#pragma unroll
      for (int i = 0; i < 4; ++i) v[i] = ld16(ws, sto, WS_ST + L * STB + i * 1280);
      if (__all(v[0][3] == tag && v[1][3] == tag && v[2][3] == tag && v[3][3] == tag)) break;
      __builtin_amdgcn_s_sleep(1);
    }
    STAMP(2 + 8 * L);
    {
      const f4 s = (asf(v[0]) + asf(v[1])) + (asf(v[2]) + asf(v[3]));
      const float m = xadd32(s[0]) * (1.f / 512), dt = xadd32(s[1]), qq = xadd32(s[2]) * (1.f / 512);
      const float sc = (dt - m * wsum) * rsq(fmx(qq - m * m, 0.f) + EPS);
      unsigned u = __float_as_uint(sc); u ^= (unsigned)((int)u >> 31) | 0x80000000u;
      const unsigned key = (u & 0xFFFFF000u) | loc;
      const unsigned k1 = max_x16(max_row16(key)), k2 = max_x16(max_row16(key == k1 ? 0u : key));
      st8(ws, (u2){bsel(mo1, k2, k1), tag}, cw, WS_CAND + L * 16384);
    }
    STAMP(3 + 8 * L);
    pause(LAG_C);
    u4 c;
#pragma unroll 1
    for (int sp = 0; sp < SPIN_CAP; ++sp) {
      CNT(14 + L);
      ld16_go(c, ws, co, WS_CAND + L * 16384);
      WAITV(0);
      OPQ(c);
      if (__all(c[1] == tag && c[3] == tag)) break;
    }
    STAMP(4 + 8 * L);
    const unsigned k1 = max_x32(max_x16(max_row16(c[0]))), k2 = max_x32(max_x16(max_row16(c[0] == k1 ? c[2] : c[0])));
    const unsigned ka = bsel(mo1, k2, k1), kb_ = bsel(mo8, k2, k1);
    const unsigned vo_ = ((ka >> 7) & 31) * TILEB + ((ka >> 4) & 7) * 16384 + (ka & 15) * 16 + g * 256;
    const unsigned so_ = (((kb_ >> 7) & 31) * 640 + (kb_ & 127)) * 16 + (l & 7) * 1280;
    {
      f4 a[3];
      float sm, sq;
#pragma unroll 1
      for (int sp = 0; sp < SPIN_CAP; ++sp) {
        CNT(16 + L);
        u4 h[16], sr;
        sfor<0, 16>([&](auto K_) __attribute__((always_inline)) {
          constexpr int ks = decltype(K_)::v;
          ld16_go<(ks & 3) * 1024>(h[ks], ws, vo_, WS_XB + L * BUFB + (ks >> 2) * 4096);
        });
        ld16_go(sr, ws, so_, WS_ST + L * STB);
        st8(ws, (u2){l == 2 ? now() + T_Y : ka, tag}, t2o, WS_SEL + L * 2048 + j * 128);
        unsigned m = 0;
        SB();
        sfor<0, 16>([&](auto K_) __attribute__((always_inline)) {
          constexpr int ks = decltype(K_)::v;
          WAITV(17 - ks);
          OPQ(h[ks]);
          mAW<ks == 0>(a[0], h[ks], G[ks][0]);
          if constexpr (ks & 1) { m = max(max(m, h[ks - 1][0]), h[ks][0]); OPQ(m); }
          SB();
          mAW<ks == 0>(a[1], h[ks], G[ks][1]); mAW<ks == 0>(a[2], h[ks], G[ks][2]);
          SB();
        });
        WAITV(1);
        OPQ(sr);
        sm = asf(sr)[0]; sq = asf(sr)[2]; sm += dppf<0xB1>(sm); sq += dppf<0xB1>(sq); sm += dppf<0x4E>(sm); sq += dppf<0x4E>(sq); sm += dppf<0x141>(sm); sq += dppf<0x141>(sq);
        if (__builtin_expect(!__any(m == SENT), 1)) break;
      }
      const float m = sm * (1.f / 512), rs = rsq(fmx(sq * (1.f / 512) - m * m, 0.f) + EPS);
      float gg, uu;
      SB();
      gather(a[0], a[1], a[2], gg, uu);
      gg = (gg - m * csg) * rs; uu = (uu - m * csu) * rs;
      const float act = live ? gg * sigm(gg) * uu : 0.f;
      lds_w<u16>(actw, (u16)pk(act, act));
    }
    STAMP(5 + 8 * L);
    WAITL();
    BAR();
    {
      u4 b[3];
#pragma unroll
      for (int ks = 0; ks < 3; ++ks) b[ks] = lds_r<u4>(F_ACT + (l & 1) * 192 + ks * 64 + g * 16);
      f4 y[8];
      auto put = [&](int c) { u4 w = asu(bsel4(m2, y[2 * c + 1], y[2 * c])); w[0] = umin(w[0], SENT - 1); st16(ws, w, yo, WS_YP + L * 65536 + c * 128); };
      SB();
#pragma unroll
      for (int c = 0; c < 4; ++c) {
        mWA<true>(y[2 * c], Dn[0][2 * c], b[0]); mWA<true>(y[2 * c + 1], Dn[0][2 * c + 1], b[0]);
        mWA(y[2 * c], Dn[1][2 * c], b[1]); mWA(y[2 * c + 1], Dn[1][2 * c + 1], b[1]);
        mVV(y[2 * c], Dn[2][2 * c], b[2]); mVV(y[2 * c + 1], Dn[2][2 * c + 1], b[2]);
        SB();
        if (c >= 2) { put(c - 2); SB(); }
      }
      put(2);
      SB();
      NOPS(1);
      put(3);
    }
    STAMP(6 + 8 * L);
  }
  if (j == 0) st4(ws, tag, tid == 0 ? 0 : OOB, WS_EPOCH);
  STAMP_FLUSH();
}

__global__ void __launch_bounds__(256, 1) fused_kernel(Params) {
  unsigned g_tent = 0;
  u64 tent; asm volatile("s_memrealtime %0\n\ts_waitcnt lgkmcnt(0)" : "=s"(tent) :: "memory");
  g_tent = (unsigned)tent;
  const int b = blockIdx.x;
  if (__builtin_expect(b < 240, 1)) role_a(b, g_tent);
  else role_f(b - 240, g_tent);
}

extern "C" __device__ __attribute__((used, noinline)) void code_end_marker() {}

extern "C" void kernel_launch(void* const* d_in, const int* in_sizes, int n_in, void* d_out, int out_size, void* d_ws, size_t ws_size, hipStream_t stream) {
  Params P;
  P.x = (const u16*)d_in[0]; P.wqkv = (const u16*)d_in[1]; P.wo = (const u16*)d_in[2];
  P.wr = (const u16*)d_in[4]; P.wg = (const u16*)d_in[5]; P.wu = (const u16*)d_in[6]; P.wd = (const u16*)d_in[7];
  P.out = (u16*)d_out; P.ws = (char*)d_ws;
  (void)hipFuncSetAttribute((const void*)fused_kernel, hipFuncAttributeMaxDynamicSharedMemorySize, LDS_BYTES);
  fused_kernel<<<256, 256, LDS_BYTES, stream>>>(P);
}
```

```cpp
#include <hip/hip_runtime.h>
#include <hip/hip_bf16.h>
#include <stdint.h>

typedef unsigned short u16;
typedef unsigned long long u64;
typedef __bf16 bf2 __attribute__((ext_vector_type(2)));
typedef short s4 __attribute__((ext_vector_type(4)));
typedef float f4 __attribute__((ext_vector_type(4)));
typedef float f2 __attribute__((ext_vector_type(2)));
typedef unsigned u4 __attribute__((ext_vector_type(4)));
typedef unsigned u2 __attribute__((ext_vector_type(2)));
typedef __amdgpu_buffer_rsrc_t rsrc_t;
#define LAS __attribute__((address_space(3)))
#define DEV __device__ __forceinline__

constexpr unsigned TILEB = 81920;
constexpr unsigned BUFB = 30 * TILEB;
constexpr unsigned WS_EPOCH = 0;
constexpr unsigned WS_FF = 4096;
constexpr unsigned WS_SEL = 8192;
constexpr unsigned WS_CAND = 98304;
constexpr unsigned WS_FA = 32768;
constexpr unsigned WS_HU = 917504;
constexpr unsigned WS_XG = 983040;
constexpr unsigned WS_YP = 131072;
constexpr unsigned WS_ST = 262144;
constexpr unsigned STB = 30 * 8 * 80 * 16;
constexpr unsigned WS_OB = 1048576;
constexpr unsigned WS_XB = WS_OB + 2 * BUFB;
constexpr unsigned WS_USED = WS_XB + 2 * BUFB;
constexpr unsigned L_RING = 81920;
constexpr unsigned L_RED2 = L_RING + 16384;
constexpr unsigned L_MISC = 155648;
constexpr unsigned O_RED = 0;
constexpr unsigned O_RC = 4096, O_RM = O_RC + 320, O_RA = O_RM + 320, O_RB = O_RA + 320;
constexpr unsigned O_SEL = O_RB + 320;
constexpr unsigned LDS_BYTES = 163840;
constexpr unsigned SENT = 0xFFFFFFFFu, OOB = 0x80000000u, ONE2 = 0x3F803F80u;
#define T_O0 90
#define T_O1 75
#define T_ST 78
#define T_Y 120
#define PFR 64
#define LAG_O 15
#define LAG_ST 15
#define XST0 0
#define XST1 0
#define LAG_C 22
#define LAG_X 25
#define LAG_Y 15
#define HU_STEP 9
#define ROT 2
#define ROTQ(t, h) (ROT == 2 ? ((t) + (h)) & 3 : ROT == 1 ? (t) & 3 : 0)
#define NT_F 0
#define NT_X 0
#define NT_W 0
#define NT_O 0
#define PFW 3
#define C1_BLOCK 12
#define CL_STEP 13
#define TCL 0
constexpr int SPIN_CAP = 1 << 15;
constexpr float EPS = 1e-5f;

#define CFENCE() asm volatile("" ::: "memory")
#define WAITC(vm, lg) do { CFENCE(); __builtin_amdgcn_s_waitcnt(((vm) & 15) | 0x70 | ((lg) << 8) | (((vm) >> 4) << 14)); CFENCE(); } while (0)
#define WAITV(n) asm volatile("s_waitcnt vmcnt(%0)" ::"i"(n) : "memory")
#define WAITL() WAITC(63, 0)
#define BAR() do { CFENCE(); __builtin_amdgcn_s_barrier(); CFENCE(); } while (0)
#define SB() __builtin_amdgcn_sched_barrier(0)
#define OPQ(x) asm volatile("" : "+v"(x))
#define KEEP(x) asm volatile("" ::"v"(x))

template <int V> struct IC { static constexpr int v = V; };
template <int I, int N, class F> DEV void sfor(F&& f) { if constexpr (I < N) { f(IC<I>{}); sfor<I + 1, N>(f); } }

typedef unsigned u16v __attribute__((ext_vector_type(16)));
struct Params {
  const u16* x; const u16* wqkv; const u16* wo; const u16* wr; u16* out; char* ws; const u16* wg; const u16* wu; const u16* wd;
};
struct KArg { u16v a; u64 b; };
DEV KArg args_go() {
  KArg K;
  asm volatile("s_load_dwordx16 %0, %2, 0x0\n\ts_load_dwordx2 %1, %2, 0x40" : "=&s"(K.a), "=&s"(K.b) : "s"(__builtin_amdgcn_kernarg_segment_ptr()));
  return K;
}
DEV Params args_wait(KArg K) {
  asm volatile("s_waitcnt lgkmcnt(0)" : "+s"(K.a), "+s"(K.b) : "s"(__builtin_amdgcn_kernarg_segment_ptr()) : "memory");
  auto p = [](unsigned lo, unsigned hi) { return (char*)(((u64)hi << 32) | lo); };
  const u16v a = K.a;
  Params P;
  P.x = (const u16*)p(a[0], a[1]); P.wqkv = (const u16*)p(a[2], a[3]); P.wo = (const u16*)p(a[4], a[5]); P.wr = (const u16*)p(a[6], a[7]);
  P.out = (u16*)p(a[8], a[9]); P.ws = p(a[10], a[11]); P.wg = (const u16*)p(a[12], a[13]); P.wu = (const u16*)p(a[14], a[15]);
  P.wd = (const u16*)K.b;
  return P;
}
DEV void opq_v(int& a) { asm volatile("" : "+v"(a)); }
DEV void opq_s(int& a, int& b, int& c) { asm volatile("" : "+s"(a), "+s"(b), "+s"(c)); }
DEV void opq_s4(unsigned& a, unsigned& b, unsigned& c, unsigned& d) { asm volatile("" : "+s"(a), "+s"(b), "+s"(c), "+s"(d)); }
DEV rsrc_t mk(const void* p, unsigned bytes) { return __builtin_amdgcn_make_buffer_rsrc((void*)p, 0, bytes, 0x00020000); }
DEV void setm0(unsigned base, int imm) { asm volatile("s_add_u32 m0, %0, %1" ::"s"(base), "i"(imm) : "scc", "memory"); }
template <int SC> DEV void dma_go(rsrc_t rs, unsigned voff, unsigned soff) {
  if (SC == 1) asm volatile("buffer_load_dwordx4 %0, %1, %2 offen sc1 lds" ::"v"(voff), "s"(rs), "s"(soff) : "memory");
  else if (SC == 2) asm volatile("buffer_load_dwordx4 %0, %1, %2 offen nt lds" ::"v"(voff), "s"(rs), "s"(soff) : "memory");
  else asm volatile("buffer_load_dwordx4 %0, %1, %2 offen lds" ::"v"(voff), "s"(rs), "s"(soff) : "memory");
}
DEV void dma_go_m(rsrc_t rs, unsigned voff, unsigned soff, u64 mask) {
  if (NT_X) asm volatile("s_mov_b64 exec, %3\n\tbuffer_load_dwordx4 %0, %1, %2 offen nt lds\n\ts_mov_b64 exec, -1" ::"v"(voff), "s"(rs), "s"(soff), "s"(mask) : "memory");
  else asm volatile("s_mov_b64 exec, %3\n\tbuffer_load_dwordx4 %0, %1, %2 offen lds\n\ts_mov_b64 exec, -1" ::"v"(voff), "s"(rs), "s"(soff), "s"(mask) : "memory");
}
template <int SC> DEV void dma(rsrc_t rs, unsigned voff, unsigned soff, unsigned base, int imm) {
  if (SC == 1) asm volatile("s_add_u32 m0, %3, %4\n\ts_nop 0\n\tbuffer_load_dwordx4 %0, %1, %2 offen sc1 lds" ::"v"(voff), "s"(rs), "s"(soff), "s"(base), "i"(imm) : "scc", "memory");
  else if (SC == 2) asm volatile("s_add_u32 m0, %3, %4\n\ts_nop 0\n\tbuffer_load_dwordx4 %0, %1, %2 offen nt lds" ::"v"(voff), "s"(rs), "s"(soff), "s"(base), "i"(imm) : "scc", "memory");
  else asm volatile("s_add_u32 m0, %3, %4\n\ts_nop 0\n\tbuffer_load_dwordx4 %0, %1, %2 offen lds" ::"v"(voff), "s"(rs), "s"(soff), "s"(base), "i"(imm) : "scc", "memory");
}
DEV void st16(rsrc_t rs, u4 v, unsigned voff, unsigned soff) {
  asm volatile("buffer_store_dwordx4 %0, %1, %2, %3 offen sc1\n\ts_nop 1" ::"v"(v), "v"(voff), "s"(rs), "s"(soff) : "memory");
}
DEV void touch_go(unsigned& d, rsrc_t rs, unsigned voff, unsigned soff) { asm volatile("buffer_load_dword %0, %1, %2, %3 offen" : "=v"(d) : "v"(voff), "s"(rs), "s"(soff) : "memory"); }
DEV void wrap_add(unsigned& o) { asm volatile("s_add_u32 %0, %0, 0x18000\n\ts_and_b32 %0, %0, 0x7ffff" : "+s"(o) :: "scc"); }
DEV void st16c(rsrc_t rs, u4 v, unsigned voff, unsigned soff) {
  asm volatile("buffer_store_dwordx4 %0, %1, %2, %3 offen sc1" ::"v"(v), "v"(voff), "s"(rs), "s"(soff) : "memory");
}
DEV void st4(rsrc_t rs, unsigned v, unsigned voff, unsigned soff) {
  asm volatile("buffer_store_dword %0, %1, %2, %3 offen sc1" ::"v"(v), "v"(voff), "s"(rs), "s"(soff) : "memory");
}
DEV u4 ld16(rsrc_t rs, unsigned voff, unsigned soff) { return __builtin_amdgcn_raw_buffer_load_b128(rs, voff, soff, 16); }
DEV unsigned ld4(rsrc_t rs, unsigned voff, unsigned soff) { return __builtin_amdgcn_raw_buffer_load_b32(rs, voff, soff, 16); }

template <bool Z = false> DEV void mWA(f4& c, u4 w, u4 a) {
  if (Z) asm volatile("v_mfma_f32_16x16x32_bf16 %0, %1, %2, 0" : "=v"(c) : "a"(w), "v"(a));
  else asm volatile("v_mfma_f32_16x16x32_bf16 %0, %1, %2, %0" : "+v"(c) : "a"(w), "v"(a));
}
template <bool Z = false> DEV void mAW(f4& c, u4 a, u4 w) {
  if (Z) asm volatile("v_mfma_f32_16x16x32_bf16 %0, %1, %2, 0" : "=v"(c) : "v"(a), "a"(w));
  else asm volatile("v_mfma_f32_16x16x32_bf16 %0, %1, %2, %0" : "+v"(c) : "v"(a), "a"(w));
}
template <bool Z = false> DEV void mVV(f4& c, u4 a, u4 b) {
  if (Z) asm volatile("v_mfma_f32_16x16x32_bf16 %0, %1, %2, 0" : "=v"(c) : "v"(a), "v"(b));
  else asm volatile("v_mfma_f32_16x16x32_bf16 %0, %1, %2, %0" : "+v"(c) : "v"(a), "v"(b));
}
template <bool Z = false> DEV void m16(f4& c, u2 a, u2 b) {
  if (Z) asm volatile("v_mfma_f32_16x16x16_bf16 %0, %1, %2, 0" : "=v"(c) : "v"(a), "v"(b));
  else asm volatile("v_mfma_f32_16x16x16_bf16 %0, %1, %2, %0" : "+v"(c) : "v"(a), "v"(b));
}
DEV f4 m16c(u2 a, u2 b, f4 c) { f4 d; asm volatile("v_mfma_f32_16x16x16_bf16 %0, %1, %2, %3" : "=&v"(d) : "v"(a), "v"(b), "v"(c)); return d; }
template <bool Z = false> DEV void mf32(f4& c, float a, float b) {
  if (Z) asm volatile("v_mfma_f32_16x16x4_f32 %0, %1, %2, 0" : "=&v"(c) : "v"(a), "v"(b));
  else asm volatile("v_mfma_f32_16x16x4_f32 %0, %1, %2, %0" : "+v"(c) : "v"(a), "v"(b));
}
#define NOPS(n) asm volatile("s_nop %0" ::"i"((n) - 1))
DEV unsigned pk(float a, float b) { f2 v = {a, b}; return __builtin_bit_cast(unsigned, __builtin_convertvector(v, bf2)); }
DEV u2 pk4(f4 v) { return (u2){pk(v[0], v[1]), pk(v[2], v[3])}; }
DEV float lo(unsigned u) { return __uint_as_float(u << 16); }
DEV float hi(unsigned u) { return __uint_as_float(u & 0xFFFF0000u); }
DEV f4 up4(u2 u) { return (f4){lo(u[0]), hi(u[0]), lo(u[1]), hi(u[1])}; }
DEV f4 asf(u4 v) { return __builtin_bit_cast(f4, v); }
DEV u4 asu(f4 v) { return __builtin_bit_cast(u4, v); }
DEV unsigned bsel(unsigned m, unsigned a, unsigned b) { return __builtin_amdgcn_bitop3_b32(m, a, b, 0xCA); }
DEV float bself(unsigned m, float a, float b) { return __uint_as_float(bsel(m, __float_as_uint(a), __float_as_uint(b))); }
DEV f4 bsel4(unsigned m, f4 a, f4 b) { return (f4){bself(m, a[0], b[0]), bself(m, a[1], b[1]), bself(m, a[2], b[2]), bself(m, a[3], b[3])}; }
DEV unsigned lmask(bool c) { unsigned m = c ? ~0u : 0u; OPQ(m); return m; }
DEV float fmx(float a, float b) { return __builtin_elementwise_maximum(a, b); }
template <class T> DEV T lds_r(unsigned a) { return *(const LAS T*)(size_t)a; }
template <class T> DEV void lds_w(unsigned a, T v) { *(LAS T*)(size_t)a = v; }
DEV u4 trfrag2(unsigned a, unsigned step) {
  u2 x = __builtin_bit_cast(u2, __builtin_amdgcn_ds_read_tr16_b64_v4i16((LAS s4*)(size_t)a));
  u2 y = __builtin_bit_cast(u2, __builtin_amdgcn_ds_read_tr16_b64_v4i16((LAS s4*)(size_t)(a + step)));
  return (u4){x[0], x[1], y[0], y[1]};
}
DEV f2 swap16(float a, float b) { auto r = __builtin_amdgcn_permlane16_swap(__float_as_uint(a), __float_as_uint(b), false, false); return (f2){__uint_as_float(r[0]), __uint_as_float(r[1])}; }
DEV f2 swap32(float a, float b) { auto r = __builtin_amdgcn_permlane32_swap(__float_as_uint(a), __float_as_uint(b), false, false); return (f2){__uint_as_float(r[0]), __uint_as_float(r[1])}; }
DEV float xadd16(float v) { f2 r = swap16(v, v); return r[0] + r[1]; }
DEV float xadd32(float v) { f2 r = swap32(v, v); return r[0] + r[1]; }
template <int C> DEV unsigned dpp(unsigned v) { return __builtin_amdgcn_update_dpp(0u, v, C, 0xF, 0xF, true); }
template <int C> DEV float dppf(float v) { return __uint_as_float(dpp<C>(__float_as_uint(v))); }
DEV float wave_sum(float v) {
  v += dppf<0xB1>(v); v += dppf<0x4E>(v); v += dppf<0x141>(v); v += dppf<0x140>(v);
  return xadd32(xadd16(v));
}
DEV unsigned max_row16(unsigned v) { v = max(v, dpp<0xB1>(v)); v = max(v, dpp<0x4E>(v)); v = max(v, dpp<0x141>(v)); return max(v, dpp<0x140>(v)); }
DEV unsigned max_x16(unsigned v) { auto r = __builtin_amdgcn_permlane16_swap(v, v, false, false); return max(r[0], r[1]); }
DEV unsigned max_x32(unsigned v) { auto r = __builtin_amdgcn_permlane32_swap(v, v, false, false); return max(r[0], r[1]); }
DEV unsigned umin(unsigned a, unsigned b) { return a < b ? a : b; }
DEV float dot2(unsigned a, unsigned b, float c) { return __builtin_amdgcn_fdot2_f32_bf16(__builtin_bit_cast(bf2, a), __builtin_bit_cast(bf2, b), c, false); }
DEV unsigned rfl(unsigned v) { return __builtin_amdgcn_readfirstlane(v); }
DEV unsigned rdl(unsigned v, int l) { return __builtin_amdgcn_readlane(v, l); }
DEV float rsq(float v) { return __builtin_amdgcn_rsqf(v); }
DEV float sigm(float v) { return __builtin_amdgcn_rcpf(1.f + __builtin_amdgcn_exp2f(-1.44269504f * v)); }
DEV float key_score(unsigned k) { unsigned u = k & 0xFFFFF000u; return __uint_as_float((u & 0x80000000u) ? (u ^ 0x80000000u) : ~u); }

#define DBGARG
#define DBG(i)
#define TC(set, i)
#define TCFLUSH(set, n)
#define TCFLUSHB(set, n, base)
#define CNT(i)
#define STAMP(i)
#define STAMP_INIT()
#define STAMP_FLUSH()
DEV unsigned tag_issue(const char* ws) { unsigned e; asm volatile("s_load_dword %0, %1, 0x0" : "=s"(e) : "s"(ws) : "memory"); return e; }
DEV unsigned tag_finish(unsigned e, const char* ws) { unsigned t; asm volatile("s_waitcnt lgkmcnt(0)\n\ts_add_u32 %0, %1, 1\n\ts_addc_u32 %0, %0, 0" : "=&s"(t) : "s"(e), "s"(ws) : "memory", "scc"); return t; }

DEV void peek8_go(u2& z, rsrc_t ws, unsigned voff, unsigned soff) {
  asm volatile("buffer_atomic_or_x2 %0, %1, %2, %3 offen sc0 sc1" : "+v"(z) : "v"(voff), "s"(ws), "s"(soff) : "memory");
}
template <int OFF = 0> DEV void ld16_go(u4& v, rsrc_t ws, unsigned voff, unsigned soff) {
  asm volatile("buffer_load_dwordx4 %0, %1, %2, %3 offen offset:%4 sc1" : "=v"(v) : "v"(voff), "s"(ws), "s"(soff), "i"(OFF) : "memory");
}
DEV void ld4_go(unsigned& v, rsrc_t ws, unsigned voff, unsigned soff) { asm volatile("buffer_load_dword %0, %1, %2, %3 offen sc1" : "=v"(v) : "v"(voff), "s"(ws), "s"(soff) : "memory"); }
DEV u64 time_go() { u64 t; asm volatile("s_memrealtime %0" : "=s"(t) :: "memory"); return t; }
DEV unsigned time_get(u64 t) { asm volatile("s_waitcnt lgkmcnt(0)" : "+s"(t) :: "memory"); return (unsigned)t; }
DEV unsigned now() { return (unsigned)__builtin_amdgcn_s_memrealtime(); }
template <int SLEEP> DEV void poll_words(rsrc_t ws, u2& z, unsigned voff, unsigned soff, unsigned tag, u64 act) {
  unsigned c = SPIN_CAP, z0 = z[0], z1 = z[1];
  asm volatile("v_cmp_ne_u32 vcc, %6, %1\n\ts_and_b64 vcc, vcc, %7\n\ts_cbranch_vccz 2f\n"
               "1:\n\ts_mov_b64 exec, vcc\n\tv_mov_b64 v[254:255], 0\n\ts_sleep %8\n\tbuffer_atomic_or_x2 v[254:255], %3, %4, %5 offen sc0 sc1\n\ts_waitcnt vmcnt(0)\n\t"
               "v_mov_b32 %0, v254\n\tv_mov_b32 %1, v255\n\tv_cmp_ne_u32 vcc, %6, v255\n\ts_mov_b64 exec, -1\n\ts_sub_u32 %2, %2, 1\n\ts_cbranch_scc1 2f\n\ts_cbranch_vccnz 1b\n2:"
               : "+&v"(z0), "+&v"(z1), "+&s"(c) : "v"(voff), "s"(ws), "s"(soff), "s"(tag), "s"(act), "i"(SLEEP) : "vcc", "scc", "v254", "v255", "memory");
  z = (u2){z0, z1};
}
DEV void wait_time(unsigned t, int lag, u64 act) {
  unsigned c = 64, d;
  asm volatile("1:\n\ts_memrealtime vcc\n\ts_waitcnt lgkmcnt(0)\n\ts_sub_u32 vcc_lo, vcc_lo, %3\n\tv_subrev_u32 %0, vcc_lo, %2\n\tv_cmp_lt_i32 vcc, 0, %0\n\t"
               "s_sub_u32 %1, %1, 1\n\ts_cbranch_scc1 2f\n\ts_and_b64 vcc, vcc, %4\n\ts_cbranch_vccnz 1b\n2:"
               : "=&v"(d), "+&s"(c) : "v"(t), "i"(lag), "s"(act) : "vcc", "scc", "memory");
}
DEV void wait_time_s(unsigned t) {
  unsigned c = 192;
  asm volatile("1:\n\ts_memrealtime vcc\n\ts_waitcnt lgkmcnt(0)\n\ts_sub_u32 %0, %0, 1\n\ts_cbranch_scc1 2f\n\ts_sub_u32 vcc_lo, vcc_lo, %1\n\ts_cmp_lt_i32 vcc_lo, 0\n\ts_cbranch_scc1 1b\n2:"
               : "+&s"(c) : "s"(t) : "vcc", "scc", "memory");
}
DEV u4 wait_rec(rsrc_t ws, unsigned soff, unsigned tag) {
  u4 c;
#pragma unroll 1
  for (int sp = 0; sp < SPIN_CAP; ++sp) {
    CFENCE();
    c = ld16(ws, 0, soff);
    if (rfl(c[3]) == tag) break;
    __builtin_amdgcn_s_sleep(2);
  }
  return c;
}
DEV void wait_flags_ld(rsrc_t ws, unsigned voff, unsigned soff, unsigned tag) {
#pragma unroll 1
  for (int sp = 0; sp < SPIN_CAP; ++sp) {
    CFENCE();
    if (__all(ld4(ws, voff, soff) == tag)) break;
    __builtin_amdgcn_s_sleep(1);
  }
}
DEV void st8(rsrc_t rs, u2 v, unsigned voff, unsigned soff) {
  asm volatile("buffer_store_dwordx2 %0, %1, %2, %3 offen sc1" ::"v"(v), "v"(voff), "s"(rs), "s"(soff) : "memory");
}

struct P1 {
  static constexpr int op(int s, int k) {
    constexpr int T[16][5] = {{72, 73, 3, -1, -1}, {74, 75, 4, -1, -1}, {76, 5, -1, -1, -1}, {80, 6, -1, -1, -1}, {81, 82, 7, -1, -1}, {83, 84, 8, -1, -1},
                              {9, -1, -1, -1, -1}, {88, 10, -1, -1, -1}, {89, 90, 11, -1, -1}, {91, 92, 12, -1, -1}, {13, -1, -1, -1, -1},
                              {14, -1, -1, -1, -1}, {15, -1, -1, -1, -1}, {-1, -1, -1, -1, -1}, {128, -1, -1, -1, -1}, {129, 130, -1, -1, -1}};
    return T[s][k];
  }
  static constexpr int size(int o) { return o < 0 ? 0 : (o >> 6) == 1 ? 1 : 3; }
  static constexpr int ndma(int s) { int n = 0; for (int k = 0; k < 5; ++k) n += size(op(s, k)); return n; }
  static constexpr int nops(int s) { return ndma(s) + (s == C1_BLOCK ? 3 : 0); }
  static constexpr int sub(int s, int d) { for (int k = 0; k < 5; ++k) { if (d < size(op(s, k))) return op(s, k) * 4 + d; d -= size(op(s, k)); } return -1; }
  static constexpr int pos(int o) {
    if (o < 3) return 8 + 3 * o;
    int n = 14 + (PFW & 1) + (PFW >> 1 & 1) + (PFW >> 2);
    for (int s = 0; s < 16; ++s) { int m = n; for (int k = 0; k < 5; ++k) { m += size(op(s, k)); if (op(s, k) == o) return m; } n += nops(s); }
    return 0;
  }
  static constexpr int nv(int s) {
    int n = 14 + (PFW & 1) + (PFW >> 1 & 1) + (PFW >> 2); for (int t = 0; t < s; ++t) n += nops(t);
    int need = pos(s);
    if (s == 4 || s == 8 || s == 12) { const int x = pos(64 + (s >> 2) * 8 + 4); need = x > need ? x : need; }
    return n - need;
  }
};
struct RowC { float c[5], cm[5]; f4 c4[5], cm4[5]; };
DEV void rowc_read(RowC& R, unsigned r1, unsigned r4, int i) {
  const int t = i / 3;
  if (i % 3 == 0) { R.c[t] = lds_r<float>(r1 + t * 64); R.cm[t] = lds_r<float>(r1 + 320 + t * 64); }
  else if (i % 3 == 1) R.c4[t] = lds_r<f4>(r4 + t * 64);
  else R.cm4[t] = lds_r<f4>(r4 + 320 + t * 64);
}

DEV void role_a(const int b, unsigned g_tent) {
  const KArg K = args_go();
  const int wv = __builtin_amdgcn_readfirstlane(threadIdx.x >> 6), l = threadIdx.x & 63;
  unsigned pfc;
  {
    u64 pc; unsigned len;
    asm volatile("s_getpc_b64 %0\n.Lpf_here:\n\ts_mov_b32 %1, code_end_marker-.Lpf_here" : "=s"(pc), "=s"(len));
    const rsrc_t rc = mk((const void*)pc, len);
    asm volatile("buffer_load_dword %0, %1, %2, %3 offen" : "=v"(pfc) : "v"(min(l, 3) * 128), "s"(rc), "s"(((b >> 3) * 4 + wv) * 512) : "memory");
  }
  const int xcd = b & 7, r = b >> 3;
  const int q = (xcd >> 1) * 30 + r, tile = q >> 2, hg = (xcd & 1) * 4 + (q & 3);
  const int lr = l & 15, g = l >> 4;
  const int par = tile >= 15, p0 = 68 * (tile - 15 * par), rr = 4 * wv + g, k8 = l >> 3;
  unsigned xoff = (2 * (p0 + rr) + par) * 1024 + ((lr ^ rr) << 4);
  const unsigned swA = (((l & 7) ^ (((k8 >> 1) & 1) << 1)) << 4) ^ ((wv & 1) << 6);
  const int rq = ROTQ(tile, hg);
  unsigned woffA = rq * 4 * 98304 + k8 * 3072 + hg * 128 + swA, woffB = woffA ^ 64, wro = (16 * wv + 4 * g + 64 * hg) * 2;
  unsigned lx = wv * 1024, lw = L_RING + wv * 3072, wso[3];
  u64 xmask = wv == 3 ? 0x0000FFFFFFFFFFFFull : ~0ull;
#pragma unroll
  for (int j = 0; j < 3; ++j) { const int i = wv * 3 + j; wso[j] = (i & 3) * 8 * 3072 + (i >> 2) * 1024; }
  asm volatile("" : "+v"(xoff), "+v"(woffA), "+v"(woffB), "+v"(wro));
  asm volatile("" : "+s"(lx), "+s"(lw), "+s"(wso[0]), "+s"(wso[1]), "+s"(wso[2]), "+s"(xmask));
  const Params P = args_wait(K);
  const rsrc_t ws = mk(P.ws, WS_USED);
  const unsigned epoch = tag_issue(P.ws);
  STAMP_INIT();
  STAMP(0);
  u4 W[16][3];
  f4 aq[5], ak[5], av[5];
  u2 xp[5];
  RowC R;
  unsigned tag, pm;
  f4 mask;
  u2 wr2, spa[4];
  const rsrc_t rwo = mk(P.wo, 512 * 1024);
  unsigned wooffA, wooffB, oso[3], wo5;
  unsigned trb, ooff, ooff4, lo2, l16, fao, lw0, row3, sv_, hg0, so_[4], t4[3];
  {
    const rsrc_t rx = mk(P.x, 2048 * 1024), rw = mk(P.wqkv, 512 * 3072);
    auto ldx = [&](int pr) {
      asm volatile("s_mov_b64 exec, %0" ::"s"(xmask) : "memory");
#pragma unroll
      for (int j = 0; j < 5; ++j) dma<NT_X>(rx, xoff, ((pr + rq) & 3) * 256 + j * 30720, lx, pr * 20480 + j * 4096);
      asm volatile("s_mov_b64 exec, -1" ::: "memory");
    };
    auto ldw = [&](int s) {
#pragma unroll
      for (int j = 0; j < 3; ++j) dma<NT_W>(rw, (j & 1) ? woffB : woffA, wso[j] + s * 98304, lw, (s % 6) * 12288 + j * 1024);
    };
    asm volatile("buffer_load_dwordx2 %0, %1, %2, 0 offen" : "=v"(wr2) : "v"(wro), "s"(mk(P.wr, 1024)) : "memory");
    TC(2, 0);
    SB();
    ldx(0);
    SB();
    TC(2, 1);
    ldw(0);
    TC(2, 2);
    auto ldw1 = [&](int s_, int j) { SB(); dma<NT_W>(rw, (j & 1) ? woffB : woffA, wso[j] + s_ * 98304, lw, s_ * 12288 + j * 1024); SB(); };
    SB();
    {
      const u4 one = {ONE2, ONE2, ONE2, ONE2};
      lds_w<u4>(rr * 4096 + 15 * 256 + lr * 16, one);
      lds_w<u4>(g * 4096 + 16 * 4096 + 15 * 256 + lr * 16, one);
    }
    trb = L_RING + (8 * g + (lr >> 2)) * 128 + (((2 * wv + ((l & 3) >> 1)) ^ ((((lr >> 3) & 1) | ((g & 1) << 1)) << 1)) << 4) + 8 * (l & 1);
    OPQ(trb);
    ldw1(1, 0);
    unsigned xa[4], xh[4], wd[3], xq[3];
#pragma unroll
    for (int i = 0; i < 4; ++i) { xa[i] = lr * 256 + (((4 * i + g) ^ lr) << 4); xh[i] = xa[i] + 61440; OPQ(xa[i]); OPQ(xh[i]); }
    unsigned xg = lr * 256 + ((rr ^ lr) << 4), xgh = xg + 61440;
    unsigned xr = (((hg >> 1) - rq) & 3) * 20480 + lr * 256 + ((((hg & 1) * 8 + 2 * wv + (g >> 1)) ^ lr) << 4) + 8 * (g & 1);
    OPQ(xg); OPQ(xgh); OPQ(xr);
    ldw1(1, 1);
#pragma unroll
    for (int i = 0; i < 3; ++i) { wd[i] = i + 1 + rq == 4 ? 98304 - 16 * 98304 : 98304; xq[i] = ((i + 1 + rq) & 3) * 256; }
#pragma unroll
    for (int j = 0; j < 3; ++j) { const int i = wv * 3 + j; oso[j] = (i * 8192 + hg * 65536) & 0x7FFFF; }
    unsigned obase = WS_OB + tile * TILEB;
    wo5 = (((15 + 2 * hg) & 15) * 4 + wv) * 8192;
    asm volatile("" : "+s"(wd[0]), "+s"(wd[1]), "+s"(wd[2]), "+s"(xq[0]), "+s"(xq[1]), "+s"(xq[2]), "+s"(oso[0]), "+s"(oso[1]), "+s"(oso[2]), "+s"(obase), "+s"(wo5));
    ldw1(1, 2);
#pragma unroll
    for (int i = 0; i < 3; ++i) { so_[i] = obase + ((2 * hg + 2 + 4 * i + wv) & 15) * 1024; t4[i] = min((wv >> 1) * 3 + i, 4) * 16384; }
    so_[3] = obase + ((2 * hg + 14 + (wv & 1)) & 15) * 1024;
    obase += hg * 2048;
    asm volatile("" : "+s"(so_[0]), "+s"(so_[1]), "+s"(so_[2]), "+s"(so_[3]), "+s"(t4[0]), "+s"(t4[1]), "+s"(t4[2]), "+s"(obase));
    ldw1(2, 0);
    ooff = (((g & 1) * 32 + wv) * 2 + (g >> 1)) * 256 + lr * 16;
    ooff4 = ooff | ((g & 1) << 31);
    lo2 = ooff + 65536 - (g & 1) * (65536 - 2048);
    l16 = l * 16;
    int n32 = wv ? 0 : 32, n2 = wv ? 0 : 2, rcnt = wv < 2 ? 24 : 16, rbase = wv < 2 ? 24 * wv : 16 + 16 * wv;
    asm volatile("" : "+s"(n32), "+s"(n2), "+s"(rcnt), "+s"(rbase));
    fao = l < n32 ? tile * 1024 + l * 32 : OOB;
    hg0 = l < n2 ? l * 65536 : OOB;
    lw0 = l == 0 ? 0 : OOB;
    row3 = rbase + min(l, rcnt - 1);
    sv_ = l < rcnt ? row3 * 16 : OOB;
    unsigned rb_ = L_MISC + O_RED + row3 * 8, rc_ = L_MISC + O_RC + row3 * 4;
    OPQ(ooff); OPQ(ooff4); OPQ(lo2); OPQ(l16); OPQ(fao); OPQ(hg0); OPQ(lw0); OPQ(row3); OPQ(sv_); OPQ(rb_); OPQ(rc_);
    ldw1(2, 1);
    u4 sent = {SENT, SENT, SENT, SENT};
    wooffA = k8 * 1024 + hg * 128 + swA; wooffB = wooffA ^ 64;
    const unsigned dum = L_MISC + O_RED + 2560 + l * 4, red = L_MISC + O_RED + (wv * 80 + lr) * 8;
    unsigned mq = g == (lr >> 2) ? red + 4 : dum, ms = l >= 48 ? red : dum;
    unsigned r1 = L_MISC + O_RC + lr * 4, r4 = L_MISC + O_RC + g * 16;
    const unsigned m1 = lmask(l & 1), m2 = lmask(l & 2);
    unsigned mt[4], sA = ONE2 & ~(m1 | m2), sB = m1 & ~m2;
    OPQ(sent); OPQ(wooffA); OPQ(wooffB); OPQ(mq); OPQ(ms); OPQ(r1); OPQ(r4); OPQ(sA); OPQ(sB);
    ldw1(2, 2);
#pragma unroll
    for (int t = 0; t < 4; ++t) mt[t] = lmask((lr >> 2) == t);
    pm = lmask(lr < 8 && l >= 32);
    {
      unsigned d = lr + (pm & 15) - 4 * g;
      OPQ(d);
#pragma unroll
      for (int i = 0; i < 3; ++i) mask[i] = d - i <= 7 ? 0.f : -1e30f;
      mask[3] = (d - 3) + (l >= 48 ? 64 : 0) <= 7 ? 0.f : -1e30f;
    }
    woffA += 3 * 98304; woffB += 3 * 98304;
    OPQ(mask); OPQ(woffA); OPQ(woffB);
    SB();
    unsigned touch = 0, pw = 0, pf = 0;
    if (PFW & 4) {
      const int rh = (3 - (int)(((size_t)P.wqkv) >> 7)) & 3;
      touch_go(touch, rw, min(l, 12) * 1024, (r * 52 + wv * 13) * 1024 + ((xcd & 1) * 4 + rh) * 128);
    }
    if (PFW & 1) touch_go(pw, rwo, l < 5 ? ((b * 4 + wv) * 5 + l) * 128 : OOB, 0);
    if (PFW & 2) {
      const rsrc_t rf = mk(wv == 0 ? P.wg : wv == 1 ? P.wu : P.wd, 1376 * 1024);
      const int per = wv < 2 ? (4 * PFR + 29) / 30 : 26, n = r * per + l, hi_ = n >= 2 * PFR, jf = xcd + 8 * (wv < 2 ? hi_ : wv - 2), f0 = 32 * (jf < 11 ? 3 * jf : 33 + 2 * (jf - 11));
      const unsigned o = wv < 2 ? (512 - PFR + ((n - 2 * PFR * hi_) >> 1)) * 2752 + f0 * 2 + (n & 1) * 128 : (f0 + (n >> 3)) * 1024 + (n & 7) * 128;
      touch_go(pf, rf, l < per && n < (wv < 2 ? 4 * PFR : 768) ? o : OOB, 0);
    }
    TC(2, 3);
    SB();
    TC(2, 4);
    tag = tag_finish(epoch, P.ws);
    TC(2, 5);
    f4 gr[5];
    u4 a[2][5], z[2][5];
    f2 rv[4];
    float dg, sa, sb;
    sfor<0, 17>([&](auto S_) __attribute__((always_inline)) {
      constexpr int s = decltype(S_)::v;
      if constexpr (s < 16) WAITC(P1::nv(s), 0); else WAITL();
      if (s == 0) TC(2, 6);
      BAR();
      if (s == 0) TC(2, 7);
      if (s == 1) { TC(2, 8); TCFLUSH(2, 9); }
      TC(1, s);
      if (s == 0) STAMP(1);
      if (s == 4) STAMP(19);
      if (s == 8) STAMP(20);
      if (s == 12) STAMP(21);
      if (s == 16) STAMP(22);
      SB();
      constexpr auto gtile = [](int s_, int i) constexpr {
        if (s_ < 0) return -1;
        if (s_ < 5) return i == 0 ? s_ : -1;
        if (s_ < 7) return i < 2 ? 8 + 2 * (s_ - 5) + i : -1;
        if (s_ == 7) return i == 0 ? 12 : -1;
        if (s_ == 8) return i < 2 ? 16 + i : -1;
        if (s_ < 12) return i == 0 ? 16 + s_ - 7 : -1;
        if (s_ == 12) return 24 + i;
        return -1;
      };
      constexpr int nd = s < 16 ? P1::ndma(s) : 0, ng0 = s == 13 ? 5 : 0;
      sfor<0, 20>([&](auto I_) __attribute__((always_inline)) {
        constexpr int i = decltype(I_)::v;
        if constexpr (s > 0) {
          constexpr int pb = (s - 1) & 1, im = i - ng0, t = im / 3, ig = ng0 ? i : i - 15;
          if constexpr (im >= 0 && im < 15) {
            if constexpr (im % 3 == 0) mWA<s == 1>(aq[t], W[s - 1][0], a[pb][t]); else if constexpr (im % 3 == 1) mWA<s == 1>(ak[t], W[s - 1][1], a[pb][t]); else mAW<s == 1>(av[t], a[pb][t], W[s - 1][2]);
          } else if constexpr (gtile(s - 1, ig) >= 0) mVV<(gtile(s - 1, ig) < 8)>(gr[gtile(s - 1, ig) & 7], z[pb][ig], z[pb][ig]);
        }
        if constexpr (s < 16) {
          constexpr bool dense = nd > 5;
          constexpr int d0 = dense ? i - 3 : ((i - 3) & 1) == 0 ? (i - 3) / 2 : -1, d1 = dense ? i - 3 : ((i - 3) & 1) == 1 ? (i - 3) / 2 : -1;
          if constexpr (i >= 3 && d0 >= 0 && d0 < nd) {
            constexpr int o = P1::sub(s, d0) >> 2, j = P1::sub(s, d0) & 3;
            if constexpr ((o >> 6) == 0) setm0(lw, (o % 6) * 12288 + j * 1024);
            else if constexpr ((o >> 6) == 1) setm0(lx, ((o >> 3) & 7) * 20480 + (o & 7) * 4096);
            else setm0(lw, ((4 + (o & 7)) % 6) * 12288 + j * 1024);
          }
          if constexpr (i < 3) W[s][i] = trfrag2(trb + (s % 6) * 12288 + i * 4096, 512);
          else if constexpr (i < 8) a[s & 1][i - 3] = lds_r<u4>((s < 12 ? xa[s & 3] + (s >> 2) * 20480 : xh[s & 3]) + (i - 3) * 4096);
          else if constexpr (i < 13 && gtile(s, i - 8) >= 0) z[s & 1][i - 8] = lds_r<u4>((s < 12 ? xg + (gtile(s, i - 8) >> 3) * 20480 : xgh) + (gtile(s, i - 8) & 7) * 4096);
          if constexpr (i >= 3 && d1 >= 0 && d1 < nd) {
            constexpr int o = P1::sub(s, d1) >> 2, j = P1::sub(s, d1) & 3;
            if constexpr (dense || s == 0) { SB(); if constexpr ((o >> 6) != 1) NOPS(1); }
            if constexpr ((o >> 6) == 0) {
              dma_go<NT_W>(rw, (j & 1) ? woffB : woffA, wso[j]);
              if constexpr (o < 15 && j == 1) { woffB += (o & 3) == 3 ? wd[(o >> 2) % 3] : 98304; OPQ(woffB); }
              if constexpr (o < 15 && j == 2) { woffA += (o & 3) == 3 ? wd[(o >> 2) % 3] : 98304; OPQ(woffA); }
            } else if constexpr ((o >> 6) == 1) dma_go_m(rx, xoff, xq[((o >> 3) & 7) - 1] + (o & 7) * 30720, xmask);
            else { dma_go<NT_O>(rwo, (j & 1) ? wooffB : wooffA, oso[j]); wrap_add(oso[j]); }
          }
          if constexpr (s == C1_BLOCK && (i == 9 || i == 11 || i == 13)) st16c(ws, sent, i < 13 ? ooff : ooff4, obase + (i - 9) / 2 * 32768);
          if constexpr (s == C1_BLOCK + 1 && i == 0) KEEP(sent);
          if constexpr (s == 2 && i == 13) { OPQ(wr2); wr2 = (wr2 & sB) | sA; OPQ(wr2); }
          if constexpr ((s == 5 || s == 7) && (i == 13 || i == 14)) { constexpr int t = (s - 5) + i - 13; spa[t] = wr2 & mt[t]; OPQ(spa[t]); }
          constexpr int rt = s == 13 && i >= 13 && i < 19 ? i - 13 : s == 14 && i >= 11 && i < 15 ? i - 5 : -1;
          if constexpr (rt >= 0 && (rt & 1) == 0) { const f4 v = gr[rt / 2]; dg = bself(m2, bself(m1, v[3], v[2]), bself(m1, v[1], v[0])); OPQ(dg); }
          if constexpr (rt >= 0 && (rt & 1) == 1) { lds_w<float>(mq + (rt / 2) * 128, dg); lds_w<float>(ms + (rt / 2) * 128, gr[rt / 2][3]); }
          if constexpr (s == 14 && i >= 8 && i < 11) xp[i - 8] = lds_r<u2>(xr + (i - 8) * 4096);
          if constexpr (s == 15 && (i == 13 || i == 14)) xp[i - 10] = lds_r<u2>(xr + (i - 10) * 4096);
          if constexpr (s == 15 && i == 0) { rv[0] = lds_r<f2>(rb_); rv[1] = lds_r<f2>(rb_ + 640); rv[2] = lds_r<f2>(rb_ + 1280); rv[3] = lds_r<f2>(rb_ + 1920); }
          if constexpr (s == 15 && i == 8) { sa = rv[0][0] + rv[1][0]; OPQ(sa); sb = rv[0][1] + rv[1][1]; OPQ(sb); }
          if constexpr (s == 15 && i == 9) { sa += rv[2][0]; OPQ(sa); }
          if constexpr (s == 15 && i == 10) { sb += rv[2][1]; OPQ(sb); }
          if constexpr (s == 15 && i == 11) { sa += rv[3][0]; OPQ(sa); }
          if constexpr (s == 15 && i == 12) { sb += rv[3][1]; OPQ(sb); sa *= 1.f / 512; OPQ(sa); }
          if constexpr (s == 15 && i == 13) { sb = sb * (1.f / 512) - sa * sa; OPQ(sb); }
          if constexpr (s == 15 && i == 14) { sb = rsq(fmx(sb, 0.f) + EPS); lds_w<float>(rc_, sb); lds_w<float>(rc_ + 320, sb * sa); lds_w<float>(rc_ + 640, 1.f); lds_w<float>(rc_ + 960, 0.f); }
        } else {
          if (i < 15) rowc_read(R, r1, r4, i);
          if constexpr (i < 12 && (i & 1) == 0) setm0(lw, (1 + i / 6) * 12288 + (i % 6) / 2 * 1024);
          if constexpr (i < 12 && (i & 1) == 1) { constexpr int j = (i % 6) / 2; dma_go<NT_O>(rwo, (j & 1) ? wooffB : wooffA, oso[j]); if constexpr (i < 6) wrap_add(oso[j]); }
          if constexpr (i == 12) setm0(lx, L_RING + 3 * 12288);
          if constexpr (i == 13) dma_go<NT_O>(rwo, wooffA, wo5);
        }
        SB();
      });
    });
    asm volatile("" ::"v"(touch), "v"(pfc), "v"(pw), "v"(pf));
  }
  NOPS(2);
  SB();
  f4 csq, csk;
#pragma unroll
  for (int i = 0; i < 4; ++i) { csq[i] = dppf<0x15F>(aq[0][i]); csk[i] = dppf<0x15F>(ak[0][i]); }
  float csv;
  { f2 x = swap32(av[0][3], av[0][3]); f2 y = swap16(x[1], x[1]); csv = y[1]; }
  TC(1, 17);
  TCFLUSH(1, 18);
  STAMP(2);
  u4 WO[16];

  unsigned ff;
  sfor<0, 2>([&](auto L_) __attribute__((always_inline)) {
    constexpr int L = decltype(L_)::v;
#define FRESH(n) int l##n = l, wv##n = wv, hg##n = hg, tile##n = tile; opq_v(l##n); opq_s(wv##n, hg##n, tile##n); \
    const int lr##n = l##n & 15, g##n = l##n >> 4; (void)lr##n; (void)g##n;
    FRESH(1)
    const unsigned ob = WS_OB + L * BUFB + tile1 * TILEB;
    TC(3, 0);
    u2 kb[5], vb[5], qb[5], o[5];
    u2 one2 = {ONE2, ONE2};
    float kap = 0.36067376f;
    OPQ(one2); OPQ(kap);
    const unsigned gate = WS_FA + L * 32768 + (tile1 * 8 + hg1) * 128 + wv1 * 32;
    u2 fa = {0, 0};
    u4 sel;
    if constexpr (L == 1) ld16_go(sel, ws, 0, WS_SEL + (b & 15) * 128);
    u64 tg0;
    if constexpr (L == 0) tg0 = time_go();
    const f4 neg = {-1e30f, -1e30f, -1e30f, -1e30f};
    auto ex = [](float x) { return __builtin_amdgcn_exp2f(x); };
    {
      f4 ss[5], sp[5], sc[5], sum[5], oa[5];
      u2 pb[5], ps[5], pp[5];
      float mx[5];
      sfor<0, 5>([&](auto T_) __attribute__((always_inline)) {
        constexpr int t = decltype(T_)::v;
        f4 fq = aq[t] * R.c[t] - csq * R.cm[t], fk = ak[t] * R.c[t] - csk * R.cm[t], fv = av[t] * R.c4[t] - R.cm4[t] * csv;
        OPQ(fq); OPQ(fk); OPQ(fv);
        SB();
        if constexpr (t > 0) ss[t - 1] = m16c(kb[t - 1], qb[t - 1], mask);
        qb[t] = pk4(fq); kb[t][0] = pk(fk[0], fk[1]);
        SB();
        if constexpr (t > 1) sp[t - 1] = m16c(kb[t - 2], qb[t - 1], mask);
        kb[t][1] = pk(fk[2], fk[3]);
        OPQ(kb[t]);
        SB();
        vb[t] = pk4(fv);
        OPQ(vb[t]);
        SB();
        if constexpr (t == 0 && L == 0) {
          WAITV(16);
          st8(ws, (u2){time_get(tg0) + T_O0, tag}, lw0, gate);
          u4 sent = {SENT, SENT, SENT, SENT};
          OPQ(sent);
#pragma unroll
          for (int i = 0; i < 3; ++i) st16c(ws, sent, i < 2 ? ooff : ooff4, WS_XB + tile1 * TILEB + hg1 * 2048 + i * 32768);
          NOPS(2);
          KEEP(sent);
          SB();
        }
      });
      auto pick = [&](int t) {
        sc[t] = bsel4(pm, t ? sp[t] : neg, ss[t]);
        mx[t] = fmx(fmx(fmx(sc[t][0], sc[t][1]), sc[t][2]), sc[t][3]);
      };
      TC(3, 1);
      ss[4] = m16c(kb[4], qb[4], mask); SB();
      pick(0); SB();
      sp[4] = m16c(kb[3], qb[4], mask); SB();
      pick(1); pick(2); SB();
      pick(3); pick(4);
      {
        f2 x = swap16(mx[0], mx[1]), y = swap16(mx[2], mx[3]), w = swap16(mx[4], mx[4]);
        f2 u = swap32(fmx(x[0], x[1]), fmx(y[0], y[1]));
        const float w1 = fmx(w[0], w[1]), c = fmx(u[0], u[1]) * kap;
        f2 w2 = swap32(w1, w1), v = swap32(c, c);
        f2 v0 = swap16(v[0], v[0]), v1 = swap16(v[1], v[1]);
        mx[0] = v0[0]; mx[1] = v0[1]; mx[2] = v1[0]; mx[3] = v1[1]; mx[4] = fmx(w2[0], w2[1]) * kap;
      }
      TC(3, 2);
      SB();
      TC(3, 3);
      auto norm = [&](int t) { KEEP(sum[t]); o[t] = pk4(oa[t] * __builtin_amdgcn_rcpf(sum[t][0])); OPQ(o[t]); };
      sfor<0, 5>([&](auto T_) __attribute__((always_inline)) {
        constexpr int t = decltype(T_)::v;
        f4 d = sc[t] * kap - mx[t];
        OPQ(d);
        SB();
        if constexpr (t > 0) m16<true>(oa[t - 1], vb[t - 1], ps[t - 1]);
        float e0 = ex(d[0]), e1 = ex(d[1]);
        OPQ(e0); OPQ(e1);
        SB();
        if constexpr (t > 1) m16(oa[t - 1], vb[t - 2], pp[t - 1]);
        float e2 = ex(d[2]), e3 = ex(d[3]);
        OPQ(e2); OPQ(e3);
        SB();
        if constexpr (t > 0) m16<true>(sum[t - 1], one2, pb[t - 1]);
        pb[t] = (u2){pk(e0, e1), pk(e2, e3)};
        if constexpr (t > 0) { pp[t] = pb[t] & pm; ps[t] = pb[t] ^ pp[t]; } else ps[t] = pb[t];
        SB();
        if constexpr (t > 1) { norm(t - 2); SB(); }
      });
      m16<true>(oa[4], vb[4], ps[4]); m16(oa[4], vb[3], pp[4]); m16<true>(sum[4], one2, pb[4]);
      SB();
      if constexpr (L == 0) peek8_go(fa, ws, fao, WS_FA);
      NOPS(2);
      norm(3);
      SB();
      NOPS(2);
      norm(4);
    }
    TC(3, 4);
    if constexpr (L == 1) {
      STAMP(10);
      WAITV(0);
      OPQ(sel);
      BAR();
      if (__builtin_expect(rfl(sel[3]) != tag, 0)) sel = wait_rec(ws, WS_SEL + (b & 15) * 128, tag);
      const int halo1 = (tile1 != 0 && tile1 != 15);
      auto local = [&](unsigned key) { const int tl = (key >> 7) & 31, rho = key & 127; return tl == tile1 ? rho : (halo1 && tl == tile1 - 1 && rho >= 72) ? rho - 72 : -1; };
      const int row0 = local(rfl(sel[0])), row1 = local(rfl(sel[1]));
      STAMP(9);
      if (row0 < 0 && row1 < 0) st8(ws, (u2){now() + 5, tag}, lw0, gate);
#pragma unroll 1
      for (int i = 0; i < 2; ++i) {
        const int rho = i ? row1 : row0;
        if (rho < 0) continue;
        const unsigned key = rfl(i ? sel[1] : sel[0]);
        FRESH(5)
        const int ts = rho >> 4, rs_ = rho & 15;
        const unsigned ra = L_MISC + O_RA + rho * 4;
        const int col = 128 * wv5 + 4 * (l5 & 31), rz = ROTQ(tile5, hg5) * 256;
        unsigned yo = (l5 >> 5) * 32768 + col * 4, xo = (col >> 5) * 1024 + ((col >> 3) & 3) * 256 + (col & 4) * 2;
        OPQ(yo); OPQ(xo);
        wait_time_s(rfl(sel[2]) + LAG_Y);
        u4 y[8];
        u2 xr;
        asm volatile("buffer_load_dwordx2 %0, %1, %2, %3 offen sc1" : "=v"(xr) : "v"(xo), "s"(ws), "s"(WS_XB + ((key >> 7) & 31) * TILEB + ((key >> 4) & 7) * 16384 + (key & 15) * 16) : "memory");
#pragma unroll
        for (int j = 0; j < 8; ++j) ld16_go(y[j], ws, yo, WS_YP + j * 4096 + i * 2048);
        SB();
        float sg = sigm(key_score(key));
        const float a1 = lds_r<float>(ra), b1 = lds_r<float>(ra + 320);
        unsigned rb = L_RING + i * 1024, zb = rb + g5 * 16, zr = rb + l16, zw = rb + ((col * 2 - rz) & 1023), zxa = rb + (((64 * hg5 + 16 * wv5 + 4 * g5) * 2 - rz) & 1023);
        const unsigned half = (rs_ & 1) ? 0xFFFF0000u : 0xFFFFu;
        unsigned me = lmask(lr5 == rs_), mv0 = lmask(2 * g5 == (rs_ >> 1)) & half, mv1 = lmask(2 * g5 + 1 == (rs_ >> 1)) & half;
        OPQ(zb); OPQ(zr); OPQ(zw); OPQ(zxa); OPQ(sg); OPQ(mv0); OPQ(mv1);
        u2 Ka, Kb, Kc, Qb, Qc, Va, Vb, Vc, Xb;
        f4 mk0 = mask;
        sfor<0, 5>([&](auto T_) __attribute__((always_inline)) {
          constexpr int t = decltype(T_)::v, tp = t > 0 ? t - 1 : 0, tn = t < 4 ? t + 1 : 4;
          if (ts == t) { CFENCE(); Ka = kb[tp]; Kb = kb[t]; Kc = kb[tn]; Qb = qb[t]; Qc = qb[tn]; Va = vb[tp]; Vb = vb[t]; Vc = vb[tn]; Xb = xp[t]; if (t == 0) mk0 = neg; }
        });
        OPQ(Ka); OPQ(Kb); OPQ(Kc); OPQ(Qb); OPQ(Qc); OPQ(Va); OPQ(Vb); OPQ(Vc); OPQ(Xb); OPQ(mk0);
        WAITV(8);
        OPQ(xr);
        f4 x0 = up4(xr) * a1 + b1;
        OPQ(x0);
        WAITV(0);
        asm volatile("" : "+v"(y[0]), "+v"(y[1]), "+v"(y[2]), "+v"(y[3]), "+v"(y[4]), "+v"(y[5]), "+v"(y[6]), "+v"(y[7]));
        auto ybad = [&]() { CNT(10); return __any(max(max(max(max(y[0][0], y[1][0]), y[2][0]), max(max(y[3][0], y[4][0]), y[5][0])), max(y[6][0], y[7][0])) == SENT); };
        if (__builtin_expect(ybad(), 0)) {
#pragma unroll 1
          for (int sp = 0; sp < SPIN_CAP; ++sp) {
            __builtin_amdgcn_s_sleep(1);
            CFENCE();
#pragma unroll
            for (int j = 0; j < 8; ++j) y[j] = ld16(ws, yo, WS_YP + j * 4096 + i * 2048);
            if (!ybad()) break;
          }
        }
        STAMP(27);
        if (i == 1 || row1 < 0) st8(ws, (u2){now() + T_O1, tag}, lw0, gate);
        {
          const f4 t = ((asf(y[0]) + asf(y[1])) + (asf(y[2]) + asf(y[3]))) + ((asf(y[4]) + asf(y[5])) + (asf(y[6]) + asf(y[7])));
          const f2 p = swap32(t[0], t[1]), q = swap32(t[2], t[3]);
          const float e = p[0] + p[1], f = q[0] + q[1];
          const f2 ee = swap32(e, e), ff_ = swap32(f, f);
          lds_w<u2>(zw, pk4((f4){ee[0], ee[1], ff_[0], ff_[1]} * sg + x0));
        }
        WAITL();
        BAR();
        const u4 zz = lds_r<u4>(zr);
        const u2 z0 = {zz[0], zz[1]}, z1 = {zz[2], zz[3]};
        f4 tq, tk, tv;
        u4 z[16];
#pragma unroll
        for (int s = 0; s < 4; ++s) z[s] = lds_r<u4>(zb + s * 64);
        const u2 zx = lds_r<u2>(zxa);
        float sm, sq;
        SB();
        sfor<0, 16>([&](auto S_) __attribute__((always_inline)) {
          constexpr int s = decltype(S_)::v;
          mWA<s == 0>(tq, W[s][0], z[s]);
          if constexpr (s + 4 < 16) z[s + 4] = lds_r<u4>(zb + (s + 4) * 64);
          SB();
          mWA<s == 0>(tk, W[s][1], z[s]);
          if (s == 0) { sq = dot2(z0[1], z0[1], dot2(z0[0], z0[0], 0.f)); OPQ(sq); }
          if (s == 1) { sm = dot2(z0[1], one2[0], dot2(z0[0], one2[0], 0.f)); OPQ(sm); }
          if (s == 2) sq = dot2(z1[1], z1[1], dot2(z1[0], z1[0], sq));
          if (s == 3) sm = dot2(z1[1], one2[0], dot2(z1[0], one2[0], sm));
          if (s == 4) { sm += dppf<0xB1>(sm); sq += dppf<0xB1>(sq); } if (s == 5) { sm += dppf<0x4E>(sm); sq += dppf<0x4E>(sq); }
          if (s == 6) { sm += dppf<0x141>(sm); sq += dppf<0x141>(sq); } if (s == 7) { sm += dppf<0x140>(sm); sq += dppf<0x140>(sq); }
          if (s == 8) sm = xadd16(sm); if (s == 9) sq = xadd16(sq); if (s == 10) sm = xadd32(sm) * (1.f / 512); if (s == 11) sq = xadd32(sq) * (1.f / 512);
          if (s == 12) sq = fmx(sq - sm * sm, 0.f) + EPS; if (s == 13) sq = rsq(sq); if (s == 14) sm *= sq;
          if (s == 15) { lds_w<float>(ra, 1.f); lds_w<float>(ra + 320, 0.f); }
          if (s >= 2 && s < 15) { OPQ(sm); OPQ(sq); }
          SB();
          mAW<s == 0>(tv, z[s], W[s][2]);
          SB();
        });
        Xb = (u2){bsel(me, zx[0], Xb[0]), bsel(me, zx[1], Xb[1])};
        f4 cq = csq * -sm, ck = csk * -sm;
        float cv = csv * -sm;
        OPQ(Xb); OPQ(cq); OPQ(ck); OPQ(cv);
        SB();
        if (i == 1 || row1 < 0) peek8_go(fa, ws, fao, WS_FA + 32768);
        const u2 nq = pk4(tq * sq + cq), nk = pk4(tk * sq + ck);
        KEEP(tv);
        const float nv = tv[0] * sq + cv;
        const unsigned nvp = pk(nv, nv);
        Qb = (u2){bsel(me, nq[0], Qb[0]), bsel(me, nq[1], Qb[1])}; Kb = (u2){bsel(me, nk[0], Kb[0]), bsel(me, nk[1], Kb[1])};
        OPQ(Qb); OPQ(Kb);
        SB();
        Vb = (u2){bsel(mv0, nvp, Vb[0]), bsel(mv1, nvp, Vb[1])};
        OPQ(Vb);
        SB();
        f4 sb = m16c(Kb, Qb, mask), pb_ = m16c(Ka, Qb, mk0), sc_ = m16c(Kc, Qc, mask), pc_ = m16c(Kb, Qc, mask);
        SB();
        NOPS(6);
        SB();
        sb = bsel4(pm, pb_, sb);
        float mb = fmx(fmx(fmx(sb[0], sb[1]), sb[2]), sb[3]);
        OPQ(mb);
        SB();
        sc_ = bsel4(pm, pc_, sc_);
        float mc = fmx(fmx(fmx(sc_[0], sc_[1]), sc_[2]), sc_[3]);
        { f2 x = swap16(mb, mc); const float y_ = fmx(x[0], x[1]); f2 w = swap32(y_, y_); const float c = fmx(w[0], w[1]) * kap; f2 v = swap16(c, c); mb = v[0]; mc = v[1]; }
        const f4 db = sb * kap - mb, dc = sc_ * kap - mc;
        u2 Pb = {pk(ex(db[0]), ex(db[1])), pk(ex(db[2]), ex(db[3]))}, Pc = {pk(ex(dc[0]), ex(dc[1])), pk(ex(dc[2]), ex(dc[3]))};
        u2 Pbp = Pb & pm, Pbs = Pb ^ Pbp, Pcp = Pc & pm, Pcs = Pc ^ Pcp;
        OPQ(Pbp); OPQ(Pbs); OPQ(Pcp); OPQ(Pcs);
        f4 ob_, oc_, lb, lc;
        SB();
        m16<true>(lb, one2, Pb); m16<true>(lc, one2, Pc); m16<true>(ob_, Vb, Pbs); m16(ob_, Va, Pbp); m16<true>(oc_, Vc, Pcs); m16(oc_, Vb, Pcp);
        SB();
        NOPS(4);
        SB();
        KEEP(lb); KEEP(lc);
        float ib = __builtin_amdgcn_rcpf(lb[0]), ic = __builtin_amdgcn_rcpf(lc[0]);
        OPQ(ib); OPQ(ic);
        SB();
        NOPS(1);
        u2 Ob = pk4(ob_ * ib);
        OPQ(Ob);
        SB();
        const u2 Oc = pk4(oc_ * ic);
        sfor<0, 5>([&](auto T_) __attribute__((always_inline)) {
          constexpr int t = decltype(T_)::v;
          if (ts == t) { CFENCE(); kb[t] = Kb; qb[t] = Qb; vb[t] = Vb; xp[t] = Xb; o[t] = Ob; if constexpr (t < 4) o[t + 1] = Oc; }
        });
      }
      STAMP(25);
    }
    {
      u4 e[3];
#pragma unroll
      for (int i = 0; i < 3; ++i) {
        u2 A = o[2 * i], B = o[i < 2 ? 2 * i + 1 : 4];
        auto s0 = __builtin_amdgcn_permlane16_swap(A[0], B[0], false, false); auto s1 = __builtin_amdgcn_permlane16_swap(A[1], B[1], false, false);
        e[i] = (u4){umin(s0[0], SENT - 1), s1[0], s0[1], s1[1]};
        st16c(ws, e[i], i < 2 ? ooff : ooff4, ob + hg1 * 2048 + i * 32768);
      }
      SB();
#pragma unroll
      for (int i = 0; i < 3; ++i) lds_w<u4>(i < 2 ? ooff + i * 32768 : lo2, e[i]);
    }
    TC(3, 5);
    STAMP(3 + 8 * L);
    FRESH(2)
    unsigned l16h = l16 + 65536, r1 = L_MISC + O_RA + lr2 * 4;
    OPQ(l16h); OPQ(r1);
    f4 acc[5];
    {
      float ra[5], rb[5];
#pragma unroll
      for (int t = 0; t < 5; ++t) { ra[t] = lds_r<float>(r1 + t * 64); rb[t] = lds_r<float>(r1 + 320 + t * 64); }
      SB();
#pragma unroll
      for (int t = 0; t < 5; ++t) acc[t] = up4(xp[t]);
      SB();
#pragma unroll
      for (int t = 0; t < 5; ++t) { acc[t] = acc[t] * ra[t] + rb[t]; OPQ(acc[t]); }
    }
    unsigned rw_ = L_RED2 + wv2 * 2048 + l16, rr_ = L_RED2 + row3 * 16;
    unsigned lw2 = wv2 * 1024, lw4 = (wv2 & 1) * 1024, hub = WS_HU + L * 32768 + (tile2 * 8 + hg2) * 128, stb = WS_ST + L * STB + (tile2 * 8 + hg2) * 1280;
    u2 hu = {0, 0};
    unsigned fo = l2 < (wv2 ? 0 : (L == 0 && tile2 != 0 && tile2 != 15) ? 16 : 8) ? ((l2 & 7) - hg2 - (l2 & 8)) * 128 + 2048 : OOB;
    OPQ(rw_); OPQ(rr_); OPQ(hu); OPQ(fo);
    opq_s4(lw2, lw4, hub, stb);
    TC(3, 6);
    WAITC(3, 0);
    TC(3, 7);
    OPQ(fa);
    {
      const u64 act = wv2 ? 0 : 0xFFFFFFFFull;
      poll_words<0>(ws, fa, fao, WS_FA + L * 32768, tag, act);
      wait_time(fa[0], LAG_O, act);
    }
    TC(3, 8);
    BAR();
    TC(3, 9);
    STAMP(23);
    u4 a[3][5];
    auto rda = [&](int j, int t) { a[j % 3][t] = lds_r<u4>((t < 4 ? l16 + t * 16384 : l16h) + j * 1024); };
    auto rdwo = [&](int j) { WO[j] = trfrag2(trb + ((4 + j / 3) % 6) * 12288 + (j % 3) * 4096, 512); };
    if (L == 0) { rdwo(0); asm volatile("" : "+a"(WO[0])); }
#pragma unroll
    for (int t = 0; t < 5; ++t) rda(0, t);
#pragma unroll
    for (int t = 0; t < 5; ++t) rda(1, t);
    STAMP(24);
    TC(3, 10);
    SB();
#pragma unroll
    for (int t = 0; t < 5; ++t) { setm0(lw2, 2048 + t * 16384); SB(); mWA(acc[t], WO[0], a[0][t]); SB(); dma_go<1>(ws, l16, so_[0] + t * 16384); }
#pragma unroll
    for (int t = 0; t < 5; ++t) dma<1>(ws, l16, so_[1] + t * 16384, lw2, 6 * 1024 + t * 16384);
    TC(3, 11);
    if (L == 0) {
#pragma unroll
      for (int j = 1; j < 16; ++j) rdwo(j);
      asm volatile("" : "+a"(WO[1]), "+a"(WO[2]), "+a"(WO[3]), "+a"(WO[4]), "+a"(WO[5]), "+a"(WO[6]), "+a"(WO[7]), "+a"(WO[8]), "+a"(WO[9]), "+a"(WO[10]), "+a"(WO[11]), "+a"(WO[12]), "+a"(WO[13]), "+a"(WO[14]), "+a"(WO[15]));
    }
    STAMP(4 + 8 * L);
    auto refetch = [&](u4& c0, u4& c1, u4& c2, u4& c3, u4& c4, unsigned v0, unsigned v1, unsigned so, int ci) {
      CNT(ci);
#pragma unroll 1
      for (int sp = 0; sp < SPIN_CAP; ++sp) {
        CNT(ci + 1);
        CFENCE();
        c0 = ld16(ws, v0, so); c1 = ld16(ws, v1, so + 16384); c2 = ld16(ws, v1, so + 32768); c3 = ld16(ws, v1, so + 49152); c4 = ld16(ws, v1, so + 65536);
        if (!__any(max(max(max(c0[0], c1[0]), c2[0]), max(c3[0], c4[0])) == SENT)) break;
        __builtin_amdgcn_s_sleep(6);
      }
      NOPS(2);
    };
    TC(3, 12);
    WAITV(5);
    BAR();
    TC(3, 13);
#pragma unroll
    for (int t = 0; t < 5; ++t) rda(2, t);
    SB();
    sfor<1, 16>([&](auto J_) __attribute__((always_inline)) {
      constexpr int j = decltype(J_)::v, n = (j + 1) % 3;
      u64 th;
      if (j == 8) th = time_go();
      if (j == 4) WAITV(5); else if (j == 8) WAITV(3); else if (j == 12) WAITV(1);
      if (j == 4 || j == 8 || j == 12) BAR();
      if (j == 4) TC(3, 14); if (j == 8) TC(3, 15); if (j == 12) TC(3, 16);
      if (j == 8) st8(ws, (u2){time_get(th) + T_ST, tag}, hg0, hub);
      SB();
      unsigned m;
      sfor<0, 5>([&](auto T_) __attribute__((always_inline)) {
        constexpr int t = decltype(T_)::v, k = j < 4 ? 2 * (j - 1) + (t >> 1) : 5 + (j - 4);
        constexpr bool dm = j < 4 ? (t < 4 && k < 5) : (j < 7 && t < 2);
        mWA(acc[t], WO[j], a[j % 3][t]);
        if constexpr (j + 2 < 16) rda(j + 2, t);
        if constexpr (dm && (t & 1) == 0) { if constexpr (k < 5) setm0(lw2, 10 * 1024 + k * 16384); else setm0(lw4 + t4[k - 5], 14 * 1024); }
        if constexpr (dm && (t & 1) == 1) { if constexpr (k < 5) dma_go<1>(ws, l16, so_[2] + k * 16384); else dma_go<1>(ws, l16, so_[3] + t4[k - 5]); }
        if constexpr (j + 1 < 16) {
          if constexpr (t == 1) WAITC(63, j + 2 < 16 ? 2 : 0);
          if constexpr (t == 2) { m = max(max(a[n][0][0], a[n][1][0]), a[n][2][0]); OPQ(m); }
          if constexpr (t == 3) { m = max(max(m, a[n][3][0]), a[n][4][0]); OPQ(m); }
        }
        SB();
      });
      if constexpr (j + 1 < 16)
        if (__builtin_expect(__any(m == SENT), 0)) refetch(a[n][0], a[n][1], a[n][2], a[n][3], a[n][4], l16, l16, ob + ((2 * hg2 + j + 1) & 15) * 1024, 2 * L);
      SB();
    });
    TC(3, 17);
    STAMP(5 + 8 * L);
    {
      float q2[5];
      f4 da, ea;
      peek8_go(hu, ws, fo, hub + (WS_XG - WS_HU - 2048));
      NOPS(3);
      SB();
      sfor<0, 5>([&](auto T_) __attribute__((always_inline)) {
        constexpr int t = decltype(T_)::v;
        xp[t] = pk4(acc[t]);
        if constexpr (t > 0) { q2[t - 1] = dot2(xp[t - 1][1], xp[t - 1][1], dot2(xp[t - 1][0], xp[t - 1][0], 0.f)); OPQ(q2[t - 1]); }
        OPQ(xp[t]);
        SB();
        if constexpr (t > 0) m16<t == 1>(da, spa[t - 1], xp[t - 1]);
        SB();
      });
      q2[4] = dot2(xp[4][1], xp[4][1], dot2(xp[4][0], xp[4][0], 0.f));
      OPQ(q2[4]);
      SB();
      m16<true>(ea, spa[0], xp[4]);
      SB();
      const f2 x = swap16(q2[0], q2[1]), y = swap16(q2[2], q2[3]), u = swap32(x[0] + x[1], y[0] + y[1]);
      float qa = u[0] + u[1], qe = xadd32(xadd16(q2[4]));
      OPQ(qa); OPQ(qe);
      SB();
      KEEP(da); KEEP(ea);
      lds_w<f4>(rw_, (f4){da[0], da[1], qa, da[3]});
      lds_w<f4>(rw_ + 1024, (f4){ea[0], ea[1], qe, ea[3]});
    }
    WAITL();
    BAR();
    {
      u4 w = asu((lds_r<f4>(rr_) + lds_r<f4>(rr_ + 2048)) + (lds_r<f4>(rr_ + 4096) + lds_r<f4>(rr_ + 6144)));
      w[3] = tag;
      st16(ws, w, sv_, stb);
    }
    TC(3, 18);
    TCFLUSHB(3, 19, L == TCL ? 0 : 32);
    STAMP(6 + 8 * L);
    FRESH(3)
    const unsigned xb = WS_XB + L * BUFB + tile3 * TILEB;
    const int par3 = tile3 >= 15, ti3 = tile3 - 15 * par3, p03 = 68 * ti3;
    const bool halo = L == 0 && ti3 > 0;
    if constexpr ((L ? XST1 : XST0) > 0) wait_time_s(now() + (L ? XST1 : XST0));
#pragma unroll
    for (int i = 0; i < 3; ++i) {
      u2 A = xp[2 * i], B = xp[i < 2 ? 2 * i + 1 : 4];
      auto s0 = __builtin_amdgcn_permlane16_swap(A[0], B[0], false, false); auto s1 = __builtin_amdgcn_permlane16_swap(A[1], B[1], false, false);
      st16(ws, (u4){umin(s0[0], SENT - 1), s1[0], s0[1], s1[1]}, i < 2 ? ooff : ooff4, xb + hg3 * 2048 + i * 32768);
    }
    unsigned so = (halo && row3 < 7) ? ((tile3 - 1) * 640 + 72 + row3) * 16 : (tile3 * 640 + row3) * 16;
    unsigned zg1 = l16 + 16384, zg0 = zg1 - ((halo && lr3 < 7) ? 16256 : 0);
    unsigned rc_ = L_MISC + O_RC + row3 * 4, q1 = L_MISC + O_RC + lr3 * 4, q4 = L_MISC + O_RC + g3 * 16;
    OPQ(so); OPQ(zg0); OPQ(zg1); OPQ(rc_); OPQ(q1); OPQ(q4);
    u4 v[8];
    auto stats_ok = [&]() { return __all(((v[0][3] & v[1][3] & v[2][3] & v[3][3] & v[4][3] & v[5][3] & v[6][3] & v[7][3]) == tag) && ((v[0][3] | v[1][3] | v[2][3] | v[3][3] | v[4][3] | v[5][3] | v[6][3] | v[7][3]) == tag)); };
    auto stats_wait = [&]() {
#pragma unroll 1
      for (int sp = 0; sp < SPIN_CAP; ++sp) {
        CFENCE();
#pragma unroll
        for (int i = 0; i < 8; ++i) v[i] = ld16(ws, so, WS_ST + L * STB + i * 1280);
        if (stats_ok()) break;
        __builtin_amdgcn_s_sleep(1);
      }
    };
    auto rowstats = [&]() {
      const f4 st = ((asf(v[0]) + asf(v[1])) + (asf(v[2]) + asf(v[3]))) + ((asf(v[4]) + asf(v[5])) + (asf(v[6]) + asf(v[7])));
      const float m = st[0] * (1.f / 512), var = fmx(st[2] * (1.f / 512) - m * m, 0.f), rs = rsq(var + EPS), a1 = 1.f + rs;
      const float c = a1 * rsq(var * a1 * a1 + EPS);
      lds_w<float>(rc_, c); lds_w<float>(rc_ + 320, c * m); lds_w<float>(rc_ + 640, a1); lds_w<float>(rc_ + 960, -m * rs);
    };
    WAITV(4);
    OPQ(hu);
    {
      const u64 act = wv3 ? 0 : halo ? 0xFFFF : 0xFF;
      poll_words<0>(ws, hu, fo, hub + (WS_XG - WS_HU - 2048), tag, act);
      wait_time(hu[0], L ? LAG_ST + 10 : LAG_X + XST0, act);
    }
    BAR();
    if constexpr (L == 1) {
      const rsrc_t ro = mk(P.out, 2048 * 1024);
      stats_wait();
      STAMP(15);
      rowstats();
      FRESH(4)
      WAITL();
      BAR();
#pragma unroll
      for (int t = 0; t < 5; ++t) {
        const float ra = lds_r<float>(q1 + 640 + t * 64), rb = lds_r<float>(q1 + 960 + t * 64);
        lds_w<u2>((16 * t + lr4) * 144 + wv4 * 32 + g4 * 8, pk4(up4(xp[t]) * ra + rb));
      }
      WAITL();
      BAR();
#pragma unroll
      for (int i = 0; i < 3; ++i) {
        const int row = min((i * 4 + wv4) * 8 + (l4 >> 3), 79), u = row - (row >> 4), p = p03 + u;
        const bool ok = (i * 4 + wv4) < 10 && (row & 15) != 15 && (ti3 == 0 || u >= 7);
        st16(ro, lds_r<u4>(row * 144 + (l4 & 7) * 16), ok ? (2 * p + par3) * 1024 + hg4 * 128 + (l4 & 7) * 16 : OOB, 0);
      }
      STAMP(16);
      WAITV(0);
      BAR();
      if (wv4 < 2) {
        const u4 zz = wait_rec(ws, WS_SEL + 2048 + (b & 15) * 128, tag);
        const unsigned key = rfl(wv4 ? zz[1] : zz[0]), ty = rfl(zz[2]);
        STAMP(17);
        if (((key >> 7) & 31) == tile4) {
          const int rho = key & 127;
          f4 base = up4(lds_r<u2>(rho * 144 + lr4 * 8));
          float sg = sigm(key_score(key));
          unsigned oo = l4 < 16 ? (2 * (p03 + rho - (rho >> 4)) + par3) * 1024 + hg4 * 128 + lr4 * 8 : OOB;
          OPQ(base); OPQ(sg); OPQ(oo);
          wait_time_s(ty + LAG_Y);
          u4 y[4];
          auto ldy = [&]() {
            CNT(11);
            unsigned m = 0;
#pragma unroll
            for (int j = 0; j < 4; ++j) { y[j] = ld16(ws, g4 * 4096 + lr4 * 16, WS_YP + 65536 + j * 16384 + wv4 * 2048 + hg4 * 256); m = max(m, y[j][0]); }
            return __any(m == SENT);
          };
          if (__builtin_expect(ldy(), 0)) {
#pragma unroll 1
            for (int sp = 0; sp < SPIN_CAP; ++sp) { __builtin_amdgcn_s_sleep(1); CFENCE(); if (!ldy()) break; }
          }
          const f4 s = (asf(y[0]) + asf(y[1])) + (asf(y[2]) + asf(y[3]));
          const f2 x = swap16(s[0], s[1]), w = swap16(s[2], s[3]), vv = swap32(x[0] + x[1], w[0] + w[1]);
          const float e = vv[0] + vv[1];
          const f2 v2 = swap32(e, e), e01 = swap16(v2[0], v2[0]), e23 = swap16(v2[1], v2[1]);
          st8(ro, pk4((f4){e01[0], e01[1], e23[0], e23[1]} * sg + base), oo, 0);
        }
        STAMP(18);
      }
    }
    if constexpr (L == 0) {
      const int rq3 = ROTQ(tile3, hg3);
      const unsigned xs = xb - 16384 + wv3 * 1024, lw3 = wv3 * 1024;
      unsigned xs4[4];
#pragma unroll
      for (int i = 0; i < 4; ++i) xs4[i] = xs + ((i + rq3) & 3) * 4096;
      u4 sent = {SENT, SENT, SENT, SENT};
      OPQ(sent);
      STAMP(7);
      TC(4, 0);
#pragma unroll
      for (int i = 0; i < 2; ++i)
#pragma unroll
        for (int t = 0; t < 5; ++t) dma<1>(ws, t ? zg1 : zg0, xs4[i] + t * 16384, lw3, t * 16384 + i * 4096);
      WAITV(5);
      BAR();
      STAMP(26);
      TC(4, 1);
      u4 z[2][5];
      unsigned tg;
      float s1, s2, s3, s4;
#pragma unroll
      for (int t = 0; t < 5; ++t) z[0][t] = lds_r<u4>(t < 4 ? l16 + t * 16384 : l16h);
      if (__builtin_expect(__any(max(max(max(z[0][0][0], z[0][1][0]), z[0][2][0]), max(z[0][3][0], z[0][4][0])) == SENT), 0)) refetch(z[0][0], z[0][1], z[0][2], z[0][3], z[0][4], zg0, zg1, xb - 16384 + rq3 * 4096, 4);
      SB();
      sfor<0, 16>([&](auto S_) __attribute__((always_inline)) {
        constexpr int s = decltype(S_)::v, c = s & 1, n = c ^ 1;
        TC(4, s + 2);
        if (s == 3) { WAITV(12); BAR(); } else if (s == 7) { WAITV(3); BAR(); } else if (s == 11) { WAITC(0, 0); BAR(); }
        if constexpr (s == 12) { OPQ(ff); if (__builtin_expect(!__all(ff == tag), 0)) wait_flags_ld(ws, lr3 * 128, WS_FF, tag); }
        if (s == 8) {
          asm volatile("" : "+v"(v[0]), "+v"(v[1]), "+v"(v[2]), "+v"(v[3]), "+v"(v[4]), "+v"(v[5]), "+v"(v[6]), "+v"(v[7]));
          if (__builtin_expect(__any(tg != 0), 0)) { CNT(9); stats_wait(); }
        }
        SB();
        unsigned m;
        bool bad;
        sfor<0, 15>([&](auto I_) __attribute__((always_inline)) {
          constexpr int i = decltype(I_)::v, t = i / 3, k = 2 * s + (i > 6);
          if constexpr (i % 3 == 0) mWA<s == 0>(aq[t], W[s][0], z[c][t]); else if constexpr (i % 3 == 1) mWA<s == 0>(ak[t], W[s][1], z[c][t]); else mAW<s == 0>(av[t], z[c][t], W[s][2]);
          if constexpr (s == 15) rowc_read(R, q1, q4, i);
          else if constexpr (i < 5) z[n][i] = lds_r<u4>((i < 4 ? l16 + i * 16384 : l16h) + (s + 1) * 1024);
          if constexpr (s < 5 && (i == 5 || i == 9)) setm0(lw3, (k % 5) * 16384 + (2 + k / 5) * 4096);
          if constexpr (s < 5 && (i == 6 || i == 10)) dma_go<1>(ws, (k % 5) ? zg1 : zg0, xs4[2 + k / 5] + (k % 5) * 16384);
          if constexpr (s < 4 && (i == 7 || i == 13)) ld16_go(v[2 * s + (i > 7)], ws, so, WS_ST + (2 * s + (i > 7)) * 1280);
          if constexpr (s == 4 && i == 7) ld4_go(ff, ws, lr3 * 128, WS_FF);
          if constexpr (s < 15 && i == 11) { WAITC(63, 0); m = max(max(z[n][0][0], z[n][1][0]), z[n][2][0]); OPQ(m); }
          if constexpr (s < 15 && i == 12) { m = max(max(m, z[n][3][0]), z[n][4][0]); OPQ(m); }
          if constexpr (s < 15 && i == 13) bad = __any(m == SENT);
          if constexpr (s == 7 && i >= 5 && i < 9) { constexpr int e = 2 * (i - 5); OPQ(v[e]); OPQ(v[e + 1]); const unsigned w = (v[e][3] ^ tag) | (v[e + 1][3] ^ tag); tg = e ? tg | w : w; OPQ(tg); }
          if constexpr (s == 8 && i == 4) { s1 = asf(v[0])[0]; s2 = asf(v[0])[2]; }
          if constexpr (s == 8 && i >= 5 && i < 14 && i != 11 && i != 12) { constexpr int e = i < 11 ? i - 4 : 7; s1 += asf(v[e])[0]; OPQ(s1); s2 += asf(v[e])[2]; OPQ(s2); }
          if constexpr (s == 9 && i == 5) { s1 *= 1.f / 512; OPQ(s1); s3 = s1 * s1; OPQ(s3); }
          if constexpr (s == 9 && i == 6) { s2 = fmx(s2 * (1.f / 512) - s3, 0.f); OPQ(s2); }
          if constexpr (s == 9 && i == 7) { s3 = rsq(s2 + EPS); OPQ(s3); }
          if constexpr (s == 9 && i == 8) { s4 = 1.f + s3; OPQ(s4); s2 *= s4; OPQ(s2); }
          if constexpr (s == 9 && i == 9) { s2 = rsq(s2 * s4 + EPS); OPQ(s2); }
          if constexpr (s == 9 && i == 10) { s2 *= s4; OPQ(s2); s3 *= -s1; OPQ(s3); }
          if constexpr (s == 10 && i == 5) { lds_w<float>(rc_, s2); lds_w<float>(rc_ + 320, s2 * s1); }
          if constexpr (s == 10 && i == 6) { lds_w<float>(rc_ + 640, s4); lds_w<float>(rc_ + 960, s3); }
          if constexpr ((s == CL_STEP || s == CL_STEP + 1) && (i == 5 || i == 7 || i == 9))
            st16c(ws, sent, i < 9 ? ooff : ooff4, (s == CL_STEP ? WS_OB : WS_XB) + BUFB + tile3 * TILEB + hg3 * 2048 + (i - 5) / 2 * 32768);
          SB();
        });
        if constexpr (s < 15)
          if (__builtin_expect(bad, 0)) refetch(z[n][0], z[n][1], z[n][2], z[n][3], z[n][4], zg0, zg1, xb - 16384 + ((s + 1 + 4 * rq3) & 15) * 1024, 4);
        SB();
      });
      TC(4, 18);
      TCFLUSH(4, 19);
      STAMP(8);
#pragma unroll
      for (int i = 0; i < 4; ++i) so_[i] += BUFB;
      NOPS(2);
    }
  });
  STAMP_FLUSH();
}

DEV void role_f(const int j, unsigned g_tent) {
  const Params P = args_wait(args_go());
  const int wv = __builtin_amdgcn_readfirstlane(threadIdx.x >> 6), l = threadIdx.x & 63;
  const int lr = l & 15, g = l >> 4, tid = wv * 64 + l;
  const rsrc_t ws = mk(P.ws, WS_USED);
  const unsigned epoch = tag_issue(P.ws);
  STAMP_INIT();
  STAMP(0);
  const int noct = j < 11 ? 12 : 8, f0 = 32 * (j < 11 ? 3 * j : 33 + 2 * (j - 11));
  u4 G[16][3], Dn[3][8];
  const unsigned mg1 = lmask(g == 1), mg2 = lmask(g >= 2);
  auto gather = [&](const f4& a0, const f4& a1, const f4& a2, float& gv, float& uv) {
    KEEP(a0); KEEP(a1); KEEP(a2);
    const float d0 = bself(mg2, a2[0], bself(mg1, a1[0], a0[0])), d1 = bself(mg2, a2[1], bself(mg1, a1[1], a0[1]));
    gv = __uint_as_float(__builtin_amdgcn_update_dpp(__float_as_uint(d0), __float_as_uint(d1), 0x128, 0xF, 0xC, false));
    uv = __uint_as_float(__builtin_amdgcn_update_dpp(__float_as_uint(d1), __float_as_uint(d0), 0x128, 0xF, 0x3, false));
  };
  float csg, csu, wsum;
  unsigned mo1, mo8, t2o, live, actw, sto, huo, loc, cw, co, keyw, m2, yo, pco, hro;
  constexpr unsigned F_ACT = 0, F_KEY = 1024, F_ROW = 2048;
  unsigned tag;
  {
    const rsrc_t rgu = mk(wv < 2 ? P.wg : P.wu, 512 * 2752), rd = mk(P.wd, 1376 * 1024);
    unsigned go[6];
#pragma unroll
    for (int i = 0; i < 6; ++i) { const int n = 64 * ((wv & 1) * 6 + i) + l, row = n / 12; go[i] = row * 2752 + f0 * 2 + (n - 12 * row) * 16; }
    const unsigned dof = (f0 + g) * 1024 + wv * 256 + ((lr ^ (g << 1)) << 4);
    auto issue = [&](int rd_) {
      if (rd_ < 8) {
#pragma unroll
        for (int i = 0; i < 6; ++i) dma<NT_F>(rgu, go[i], rd_ * 64 * 2752, wv * 6144, (rd_ & 3) * 32768 + i * 1024);
      } else {
#pragma unroll
        for (int i = 0; i < 8; ++i) dma<NT_F>(rd, (i & 2) ? dof ^ 128 : dof, (32 * (rd_ - 8) + 4 * i) * 1024, wv * 8192, (rd_ & 3) * 32768 + i * 1024);
      }
    };
    unsigned tg = ((l >> 1) & 1) * 12288 + (8 * g + (lr >> 2)) * 192 + wv * 48 + (l & 1) * 8, td = wv * 8192 + (8 * g + (lr >> 2)) * 256 + (l & 3) * 8 + (((lr >> 2) | ((g & 1) << 2)) << 5);
    u4 w8;
    const u4 sent = {SENT, SENT, SENT, SENT};
    asm volatile("buffer_load_dwordx4 %0, %1, %2, 0 offen" : "=v"(w8) : "v"(l * 16), "s"(mk(P.wr, 1024)) : "memory");
    st16(ws, sent, j * 4096 + tid * 16, WS_YP); st16(ws, sent, j * 4096 + tid * 16, WS_YP + 65536);
    STAMP(17);
    issue(0); issue(1); issue(2);
    tag = tag_finish(epoch, P.ws);
    SB();
    mo1 = lmask(l & 1); mo8 = lmask(l & 8); m2 = lmask(l & 2);
    t2o = tid == 0 ? 0 : OOB;
    live = lmask(g < 3 && 3 * wv + g < noct);
    actw = F_ACT + (g < 3 ? (lr >> 3) * 192 + (24 * wv + 8 * g + (l & 7)) * 2 : 512 + l * 2);
    const int tok = 32 * (4 * j + wv) + 2 * lr + (g & 1);
    { const int p = tok >> 1, ti = max(p - 7, 0) / 68, u = p - 68 * ti, tl = (tok & 1) * 15 + ti, rho = u + u / 15; loc = tl * 128 + rho; sto = ((tl * 8 + 4 * (l >> 5)) * 80 + rho) * 16; }
    huo = l < 60 ? (wv * 60 + l) * 128 : OOB;
    cw = l < 32 ? (l >> 1) * 1024 + (4 * j + wv) * 16 + (l & 1) * 8 : OOB; co = j * 1024 + l * 16; keyw = F_KEY + (l == 0 ? wv * 4 : 64 + l * 4);
    yo = lr < 4 ? j * 4096 + (l & 1) * 2048 + wv * 512 + (l & 2) * 32 + g * 16 : OOB;
    pco = wv < 2 ? (l >> 2) * 1024 + (l & 3) * 256 : OOB; hro = F_ROW + (l & 1) * 1024 + g * 16;
    OPQ(t2o); OPQ(actw); OPQ(sto); OPQ(huo); OPQ(loc); OPQ(cw); OPQ(co); OPQ(keyw); OPQ(yo); OPQ(pco); OPQ(hro);
    SB();
    u4 one = {ONE2, ONE2, ONE2, ONE2};
    OPQ(one);
    f4 a[3];
#pragma unroll
    for (int rd_ = 0; rd_ < 11; ++rd_) {
      if (rd_ == 1 || rd_ == 2) WAITC(13, 0); else if (rd_ < 6) WAITC(12, 0); else if (rd_ == 6) WAITC(14, 0); else if (rd_ < 9) WAITC(16, 0); else if (rd_ == 9) WAITC(8, 0); else WAITC(0, 0);
      BAR();
      if (rd_ == 0) STAMP(19); if (rd_ == 4) STAMP(21); if (rd_ == 8) STAMP(23); if (rd_ == 10) STAMP(24);
      if (rd_ == 0) {
        st4(ws, tag, tid == 0 ? 0 : OOB, WS_FF + j * 128);
        OPQ(w8);
        const f4 t = up4((u2){w8[0], w8[1]}) + up4((u2){w8[2], w8[3]});
        wsum = wave_sum((t[0] + t[1]) + (t[2] + t[3]));
      }
      if (rd_ + 3 < 11) issue(rd_ + 3);
      unsigned bg = tg + (rd_ & 3) * 32768, bd = td + (rd_ & 3) * 32768;
      OPQ(bg); OPQ(bd);
      if (rd_ < 8) {
#pragma unroll
        for (int ks = 0; ks < 2; ++ks)
#pragma unroll
          for (int c = 0; c < 3; ++c) G[2 * rd_ + ks][c] = trfrag2(bg + ks * 32 * 192 + c * 16, 4 * 192);
#pragma unroll
        for (int ks = 0; ks < 2; ++ks)
#pragma unroll
          for (int c = 0; c < 3; ++c) { if (rd_ + ks == 0) mAW<true>(a[c], one, G[0][c]); else mAW(a[c], one, G[2 * rd_ + ks][c]); }
      } else {
#pragma unroll
        for (int c = 0; c < 8; ++c) Dn[rd_ - 8][c] = trfrag2(bd ^ (c * 32), 4 * 256);
        SB();
        if (rd_ == 8) gather(a[0], a[1], a[2], csg, csu);
      }
    }
    WAITL();
    BAR();
  }
  STAMP(1);

#pragma unroll 1
  for (int L = 0; L < 2; ++L) {
    {
      const unsigned t0 = now();
      u2 z = {t0, ~tag};
      poll_words<0>(ws, z, huo, WS_HU + L * 32768, tag, 0x0FFFFFFFFFFFFFFFull);
      const unsigned t = z[0];
      lds_w<unsigned>(keyw, max_x32(max_x16(max_row16(t - t0 + 0x40000000u))) + t0);
      WAITL();
      BAR();
      const u4 q = lds_r<u4>(F_KEY) - t0;
      const unsigned tt = rfl(max(max(q[0], q[1]), max(q[2], q[3])) + (t0 - 0x40000000u + LAG_ST));
      wait_time_s(tt);
    }
    STAMP(7 + 8 * L);
    TC(5, 0);
    u4 v[4];
    auto ldv = [&]() {
      CNT(12 + L);
#pragma unroll
      for (int i = 0; i < 4; ++i) v[i] = ld16(ws, sto, WS_ST + L * STB + i * 1280);
      return __any((((v[0][3] ^ tag) | (v[1][3] ^ tag)) | ((v[2][3] ^ tag) | (v[3][3] ^ tag))) != 0);
    };
    if (__builtin_expect(ldv(), 0)) {
#pragma unroll 1
      for (int sp = 0; sp < SPIN_CAP; ++sp) { __builtin_amdgcn_s_sleep(1); CFENCE(); if (!ldv()) break; }
    }
    TC(5, 1);
    STAMP(2 + 8 * L);
    {
      const f4 s = (asf(v[0]) + asf(v[1])) + (asf(v[2]) + asf(v[3]));
      const float m = xadd32(s[0]) * (1.f / 512), dt = xadd32(s[1]), qq = xadd32(s[2]) * (1.f / 512);
      const float sc = (dt - m * wsum) * rsq(fmx(qq - m * m, 0.f) + EPS);
      unsigned u = __float_as_uint(sc); u ^= (unsigned)((int)u >> 31) | 0x80000000u;
      const unsigned key = (u & 0xFFFFF000u) | loc;
      const unsigned k1 = max_x16(max_row16(key)), k2 = max_x16(max_row16(key == k1 ? 0u : key));
      st8(ws, (u2){bsel(mo1, k2, k1), tag}, cw, WS_CAND + L * 16384);
    }
    TC(5, 2);
    STAMP(3 + 8 * L);
    wait_time_s(now() + LAG_C);
    TC(5, 3);
    u4 c;
    auto ldc = [&]() { CNT(14 + L); ld16_go(c, ws, co, WS_CAND + L * 16384); WAITV(0); OPQ(c); return __any(((c[1] ^ tag) | (c[3] ^ tag)) != 0); };
    if (__builtin_expect(ldc(), 0)) {
#pragma unroll 1
      for (int sp = 0; sp < SPIN_CAP; ++sp) if (!ldc()) break;
    }
    TC(5, 4);
    STAMP(4 + 8 * L);
    const unsigned k1 = max_x32(max_x16(max_row16(c[0]))), k2 = max_x32(max_x16(max_row16(c[0] == k1 ? c[2] : c[0])));
    const unsigned kb_ = bsel(mo8, k2, k1), kw = (wv & 1) ? k2 : k1;
    const unsigned vo_ = ((kw >> 7) & 31) * TILEB + ((kw >> 4) & 7) * 16384 + (kw & 15) * 16 + pco;
    const unsigned so_ = (((kb_ >> 7) & 31) * 640 + (kb_ & 127)) * 16 + (l & 7) * 1280;
    {
      f4 a[3];
      float sm, sq;
      u4 sr;
      dma<1>(ws, vo_, WS_XB + L * BUFB, wv * 1024, F_ROW);
      ld16_go(sr, ws, so_, WS_ST + L * STB);
      st16(ws, (u4){k1, k2, now() + T_Y, tag}, t2o, WS_SEL + L * 2048 + j * 128);
      TC(5, 5);
      WAITV(2);
      BAR();
      TC(5, 6);
#pragma unroll 1
      for (int sp = 0; sp < SPIN_CAP; ++sp) {
        CNT(16 + L);
        u4 h[16];
#pragma unroll
        for (int ks = 0; ks < 3; ++ks) h[ks] = lds_r<u4>(hro + ks * 64);
        unsigned m = 0;
        SB();
        sfor<0, 16>([&](auto K_) __attribute__((always_inline)) {
          constexpr int ks = decltype(K_)::v;
          mAW<ks == 0>(a[0], h[ks], G[ks][0]);
          if constexpr (ks + 3 < 16) h[ks + 3] = lds_r<u4>(hro + (ks + 3) * 64);
          SB();
          mAW<ks == 0>(a[1], h[ks], G[ks][1]);
          if constexpr (ks & 1) { m = max(max(m, h[ks - 1][0]), h[ks][0]); OPQ(m); }
          if constexpr (ks == 8) { WAITV(1); OPQ(sr); sm = asf(sr)[0]; sq = asf(sr)[2]; }
          if constexpr (ks == 10) { sm += dppf<0xB1>(sm); sq += dppf<0xB1>(sq); OPQ(sm); OPQ(sq); }
          if constexpr (ks == 12) { sm += dppf<0x4E>(sm); sq += dppf<0x4E>(sq); OPQ(sm); OPQ(sq); }
          if constexpr (ks == 14) { sm += dppf<0x141>(sm); sq += dppf<0x141>(sq); OPQ(sm); OPQ(sq); }
          SB();
          mAW<ks == 0>(a[2], h[ks], G[ks][2]);
          SB();
        });
        if (__builtin_expect(!__any(m == SENT), 1)) break;
        __builtin_amdgcn_s_sleep(4);
        dma<1>(ws, ((k1 >> 7) & 31) * TILEB + ((k1 >> 4) & 7) * 16384 + (k1 & 15) * 16 + (l >> 2) * 1024 + (l & 3) * 256, WS_XB + L * BUFB, 0, F_ROW);
        dma<1>(ws, ((k2 >> 7) & 31) * TILEB + ((k2 >> 4) & 7) * 16384 + (k2 & 15) * 16 + (l >> 2) * 1024 + (l & 3) * 256, WS_XB + L * BUFB, 0, F_ROW + 1024);
        WAITV(0);
      }
      TC(5, 7);
      NOPS(2);
      const float m = sm * (1.f / 512), rs = rsq(fmx(sq * (1.f / 512) - m * m, 0.f) + EPS);
      float gg, uu;
      SB();
      gather(a[0], a[1], a[2], gg, uu);
      gg = (gg - m * csg) * rs; uu = (uu - m * csu) * rs;
      const float act = gg * sigm(gg) * uu;
      lds_w<u16>(actw, (u16)(pk(act, act) & live));
    }
    TC(5, 8);
    STAMP(5 + 8 * L);
    WAITL();
    BAR();
    TC(5, 9);
    {
      u4 b[3];
#pragma unroll
      for (int ks = 0; ks < 3; ++ks) b[ks] = lds_r<u4>(F_ACT + (l & 1) * 192 + ks * 64 + g * 16);
      f4 y[8];
      auto put = [&](int c) { u4 w = asu(bsel4(m2, y[2 * c + 1], y[2 * c])); w[0] = umin(w[0], SENT - 1); st16(ws, w, yo, WS_YP + L * 65536 + c * 128); };
      SB();
#pragma unroll
      for (int c = 0; c < 4; ++c) {
        mWA<true>(y[2 * c], Dn[0][2 * c], b[0]); mWA<true>(y[2 * c + 1], Dn[0][2 * c + 1], b[0]);
        mWA(y[2 * c], Dn[1][2 * c], b[1]); mWA(y[2 * c + 1], Dn[1][2 * c + 1], b[1]);
        mVV(y[2 * c], Dn[2][2 * c], b[2]); mVV(y[2 * c + 1], Dn[2][2 * c + 1], b[2]);
        SB();
        if (c >= 2) { put(c - 2); SB(); }
      }
      put(2);
      SB();
      NOPS(1);
      put(3);
    }
    TC(5, 10);
    TCFLUSHB(5, 11, L == TCL ? 0 : 32);
    STAMP(6 + 8 * L);
  }
  if (j == 0) st4(ws, tag, tid == 0 ? 0 : OOB, WS_EPOCH);
  STAMP_FLUSH();
}

__global__ void __launch_bounds__(256, 1) fused_kernel(Params) {
  unsigned g_tent = 0;
  const int b = blockIdx.x;
  if (__builtin_expect(b < 240, 1)) role_a(b, g_tent);
  else role_f(b - 240, g_tent);
}

extern "C" __device__ __attribute__((used, noinline)) void code_end_marker() {}

extern "C" void kernel_launch(void* const* d_in, const int* in_sizes, int n_in, void* d_out, int out_size, void* d_ws, size_t ws_size, hipStream_t stream) {
  Params P;
  P.x = (const u16*)d_in[0]; P.wqkv = (const u16*)d_in[1]; P.wo = (const u16*)d_in[2];
  P.wr = (const u16*)d_in[4]; P.wg = (const u16*)d_in[5]; P.wu = (const u16*)d_in[6]; P.wd = (const u16*)d_in[7];
  P.out = (u16*)d_out; P.ws = (char*)d_ws;
  (void)hipFuncSetAttribute((const void*)fused_kernel, hipFuncAttributeMaxDynamicSharedMemorySize, LDS_BYTES);
  fused_kernel<<<256, 256, LDS_BYTES, stream>>>(P);
}
```
